```python
import torch

def _softmax(x, axis=-1):
    return torch.softmax(x, dim=axis)
def _gelu(x):
    return torch.nn.functional.gelu(x, approximate="tanh")
def _silu(x):
    return torch.nn.functional.silu(x)
def _sigmoid(x):
    return torch.sigmoid(x)
def _layernorm(x, axis=-1, eps=1e-5):
    m = x.mean(dim=axis, keepdim=True)
    v = x.var(dim=axis, keepdim=True, unbiased=False)
    return (x - m) / torch.sqrt(v + eps)
def _scatter_add(tgt, idx, src, axis=0):
    return tgt.clone().index_add_(axis, idx, src)
def _segment_sum(src, seg, n):
    out = torch.zeros((n,) + src.shape[1:], dtype=src.dtype)
    return out.index_add_(0, seg, src)
def _pad(x, pw):
    flat = tuple(v for pair in reversed(pw) for v in pair)
    return torch.nn.functional.pad(x, flat)

kernel_name = 'sequential_preLN__spectral_gfnet__dense_activation_squared_relu__prompt'

def setup_inputs():

    _w = (lambda a: (lambda t: t.to(torch.bfloat16) if t.is_floating_point() else t)(torch.as_tensor(a)))
    def _A_setup():
        B, S, D = 1, 2048, 640
        Df = D // 2 + 1
        return {
            "x": _w((lambda *s: torch.randn(*s, dtype=torch.bfloat16) * 0.05)(B, S, D)),
            "w_r": _w((lambda *s: torch.randn(*s, dtype=torch.bfloat16) * 0.4)(S, Df) * 0.02),
            "w_i": _w((lambda *s: torch.randn(*s, dtype=torch.bfloat16) * 0.4)(S, Df) * 0.02),
        }
    _A_in = _A_setup()

    def _B_setup():
        B, S, D, F = 1, 2048, 640, 1536
        return {
            "x": _w((lambda *s: torch.randn(*s, dtype=torch.bfloat16) * 0.05)(B, S, D)),
            "w1": _w((lambda *s: torch.randn(*s, dtype=torch.bfloat16) * 0.05)(D, F)),
            "b1": _w((lambda *s: torch.randn(*s, dtype=torch.bfloat16) * 0.2)(F)),
            "w2": _w((lambda *s: torch.randn(*s, dtype=torch.bfloat16) * 0.05)(F, D)),
            "b2": _w((lambda *s: torch.randn(*s, dtype=torch.bfloat16) * 1.6)(D)),
        }
    _B_in = _B_setup()

    return {
        "A_x": _A_in["x"],
        "A_w_r": _A_in["w_r"],
        "A_w_i": _A_in["w_i"],
        "B_x": _B_in["x"],
        "B_w1": _B_in["w1"],
        "B_b1": _B_in["b1"],
        "B_w2": _B_in["w2"],
        "B_b2": _B_in["b2"],
    }

def reference(A_x, A_w_r, A_w_i, B_x, B_w1, B_b1, B_w2, B_b2):
    def _A_ref(x, w_r, w_i):
        B, S, D = x.shape
        X = (lambda x, axes=(-2,-1): torch.fft.rfft2(x.float(), dim=axes))(x, axes=(-2, -1))
        W = w_r + 1j * w_i
        X = X * W
        y = (lambda x, s, axes=(-2,-1): torch.fft.irfft2(x, s=s, dim=axes).to(torch.bfloat16))(X, (S, D), axes=(-2, -1))
        return y

    def _B_ref(x, w1, b1, w2, b2):
        h = torch.einsum("bsd,df->bsf", x, w1) + b1
        zero = torch.zeros_like(h)
        r = torch.where(h > zero, h, zero)
        h = r * r
        out = torch.einsum("bsf,fd->bsd", h, w2) + b2
        return out

    x = A_x
    h = x + _A_ref(_layernorm(x, axis=-1), A_w_r, A_w_i)
    h2 = h + _B_ref(_layernorm(h, axis=-1), B_w1, B_b1, B_w2, B_b2)
    return h2
```

```cpp
#include <hip/hip_runtime.h>
#include <hip/hip_bf16.h>
#include <stdint.h>

typedef float f32x2 __attribute__((ext_vector_type(2)));
typedef float f32x4 __attribute__((ext_vector_type(4)));
typedef short bf16x8 __attribute__((ext_vector_type(8)));
typedef unsigned int u32x4 __attribute__((ext_vector_type(4)));
typedef unsigned int u32x2 __attribute__((ext_vector_type(2)));

#define DEV __device__ __forceinline__

static constexpr int S_ = 2048, D_ = 640, F_ = 1536, DF_ = 321;
static constexpr int TP = 328;

DEV float bf2f(uint32_t b16) { return __builtin_bit_cast(float, b16 << 16); }
DEV uint32_t f2bf(float f) {
  __hip_bfloat16 h = __float2bfloat16(f);
  return (uint32_t)__builtin_bit_cast(uint16_t, h);
}
DEV float bfr(float f) { return bf2f(f2bf(f)); }

DEV f32x2 cmul(f32x2 a, f32x2 b) {
  f32x2 r;
  r.x = a.x * b.x - a.y * b.y;
  r.y = a.x * b.y + a.y * b.x;
  return r;
}
DEV f32x2 cmulc(f32x2 a, f32x2 b) {
  f32x2 r;
  r.x = a.x * b.x + a.y * b.y;
  r.y = a.y * b.x - a.x * b.y;
  return r;
}
DEV f32x2 twid(float f) {
  f32x2 r;
  r.x = __builtin_amdgcn_cosf(f);
  r.y = -__builtin_amdgcn_sinf(f);
  return r;
}
template <bool INV> DEV f32x2 rot(f32x2 a) {
  f32x2 r;
  if (INV) { r.x = -a.y; r.y = a.x; } else { r.x = a.y; r.y = -a.x; }
  return r;
}
template <bool INV> DEV void fft4(f32x2& a0, f32x2& a1, f32x2& a2, f32x2& a3) {
  f32x2 b0 = a0 + a2, b1 = a0 - a2, b2 = a1 + a3, b3 = rot<INV>(a1 - a3);
  a0 = b0 + b2; a1 = b1 + b3; a2 = b0 - b2; a3 = b1 - b3;
}
template <bool INV> DEV void fft8(f32x2 (&v)[8]) {
  const float r = 0.70710678118654752440f;
  f32x2 e0 = v[0] + v[4], e1 = v[1] + v[5], e2 = v[2] + v[6], e3 = v[3] + v[7];
  f32x2 o0 = v[0] - v[4], o1 = v[1] - v[5], o2 = v[2] - v[6], o3 = v[3] - v[7];
  f32x2 t;
  if (INV) {
    t.x = (o1.x - o1.y) * r; t.y = (o1.x + o1.y) * r; o1 = t;
    t.x = (-o3.x - o3.y) * r; t.y = (o3.x - o3.y) * r; o3 = t;
  } else {
    t.x = (o1.x + o1.y) * r; t.y = (o1.y - o1.x) * r; o1 = t;
    t.x = (o3.y - o3.x) * r; t.y = (-o3.x - o3.y) * r; o3 = t;
  }
  o2 = rot<INV>(o2);
  fft4<INV>(e0, e1, e2, e3);
  fft4<INV>(o0, o1, o2, o3);
  v[0] = e0; v[2] = e1; v[4] = e2; v[6] = e3;
  v[1] = o0; v[3] = o1; v[5] = o2; v[7] = o3;
}
template <bool INV> DEV void fft5(f32x2 (&v)[5]) {
  const float c1 = 0.30901699437494742410f, c2 = -0.80901699437494742410f;
  const float s1 = 0.95105651629515357212f, s2 = 0.58778525229247312917f;
  f32x2 t1 = v[1] + v[4], t2 = v[2] + v[3], t3 = v[1] - v[4], t4 = v[2] - v[3];
  f32x2 a1 = v[0] + c1 * t1 + c2 * t2;
  f32x2 a2 = v[0] + c2 * t1 + c1 * t2;
  f32x2 b1 = rot<INV>(s1 * t3 + s2 * t4);
  f32x2 b2 = rot<INV>(s2 * t3 - s1 * t4);
  v[0] = v[0] + t1 + t2;
  v[1] = a1 + b1; v[4] = a1 - b1;
  v[2] = a2 + b2; v[3] = a2 - b2;
}

template <int CTRL> DEV float dppmov(float v) {
  return __builtin_bit_cast(float, __builtin_amdgcn_update_dpp(0, __builtin_bit_cast(int, v), CTRL, 0xf, 0xf, false));
}
DEV float wave_sum(float v) {
  v += dppmov<0xB1>(v);
  v += dppmov<0x4E>(v);
  v += dppmov<0x141>(v);
  v += dppmov<0x140>(v);
  int iv = __builtin_bit_cast(int, v);
  float a = __builtin_bit_cast(float, __builtin_amdgcn_readlane(iv, 0));
  float b = __builtin_bit_cast(float, __builtin_amdgcn_readlane(iv, 16));
  float c = __builtin_bit_cast(float, __builtin_amdgcn_readlane(iv, 32));
  float d = __builtin_bit_cast(float, __builtin_amdgcn_readlane(iv, 48));
  return (a + b) + (c + d);
}
DEV void wave_sync() {
  __builtin_amdgcn_fence(__ATOMIC_RELEASE, "wavefront");
  __builtin_amdgcn_wave_barrier();
  __builtin_amdgcn_fence(__ATOMIC_ACQUIRE, "wavefront");
}

DEV void row_layernorm(const float (&xv)[10], float (&zv)[10]) {
  float s = 0.f;
#pragma unroll
  for (int i = 0; i < 10; ++i) s += xv[i];
  const float mean = wave_sum(s) * (1.0f / 640.0f);
  float q = 0.f;
#pragma unroll
  for (int i = 0; i < 10; ++i) { float d = xv[i] - mean; q += d * d; }
  const float var = wave_sum(q) * (1.0f / 640.0f);
  const float mb = bfr(mean);
  const float vb = bfr(var);
  const float den = bfr(sqrtf(bfr(vb + 1e-5f)));
#pragma unroll
  for (int i = 0; i < 10; ++i) zv[i] = bfr(bfr(xv[i] - mb) / den);
}

template <int K, int N, int NT>
DEV void tile_weight_block(const uint16_t* __restrict__ w, u32x4* __restrict__ wt, int blk, int lane) {
  constexpr int KS = K / 32;
  const int nt = blk % NT, ks = (blk / NT) % KS, cb = blk / (NT * KS);
  const uint16_t* src = w + (size_t)(ks * 32 + 8 * (lane >> 4)) * N + (cb * NT + nt) * 16 + (lane & 15);
  uint32_t e[8];
#pragma unroll
  for (int j = 0; j < 8; ++j) e[j] = src[(size_t)j * N];
  u32x4 o;
  o.x = e[0] | (e[1] << 16); o.y = e[2] | (e[3] << 16); o.z = e[4] | (e[5] << 16); o.w = e[6] | (e[7] << 16);
  wt[(size_t)blk * 64 + lane] = o;
}

__global__ __launch_bounds__(512) void k_fwd_rows(const uint32_t* __restrict__ x, f32x2* __restrict__ T,
                                                  const uint16_t* __restrict__ w1, u32x4* __restrict__ w1t) {
  __shared__ f32x2 ex[8][360];
  __shared__ f32x2 zz[8][320];
  const int tid = threadIdx.x, lane = tid & 63, j = tid >> 6;
  if (blockIdx.x >= 256) {
    tile_weight_block<D_, F_, 6>(w1, w1t, (blockIdx.x - 256) * 8 + j, lane);
    return;
  }
  const int s0 = blockIdx.x;
  const int row = s0 + 256 * j;
  uint32_t xd[5];
#pragma unroll
  for (int i = 0; i < 5; ++i) xd[i] = x[row * 320 + lane + 64 * i];

  f32x2 tw1[4], tw2[7], twq[7];
#pragma unroll
  for (int r = 1; r < 5; ++r) tw1[r - 1] = twid((float)(lane * r) * (1.0f / 320.0f));
  const int l0 = lane & 7, rr = lane >> 3;
#pragma unroll
  for (int q = 1; q < 8; ++q) tw2[q - 1] = twid((float)(l0 * q) * (1.0f / 64.0f));
#pragma unroll
  for (int q = 1; q < 8; ++q) twq[q - 1] = twid((float)(s0 * q) * (1.0f / 2048.0f));
  const f32x2 twk = twid((float)tid * (1.0f / 640.0f));

  float xv[10], zv[10];
#pragma unroll
  for (int i = 0; i < 5; ++i) { xv[2 * i] = bf2f(xd[i] & 0xffffu); xv[2 * i + 1] = bf2f(xd[i] >> 16); }
  row_layernorm(xv, zv);

  f32x2* E = ex[j];
  {
    f32x2 u[5];
#pragma unroll
    for (int i = 0; i < 5; ++i) { u[i].x = zv[2 * i]; u[i].y = zv[2 * i + 1]; }
    fft5<false>(u);
    E[lane] = u[0];
#pragma unroll
    for (int r = 1; r < 5; ++r) E[r * 72 + lane] = cmul(u[r], tw1[r - 1]);
  }
  wave_sync();
  f32x2 v[8];
  if (lane < 40) {
#pragma unroll
    for (int l1 = 0; l1 < 8; ++l1) v[l1] = E[rr * 72 + l0 + 8 * l1];
    fft8<false>(v);
#pragma unroll
    for (int q = 1; q < 8; ++q) v[q] = cmul(v[q], tw2[q - 1]);
  }
  wave_sync();
  if (lane < 40) {
#pragma unroll
    for (int q = 0; q < 8; ++q) E[rr * 72 + q * 9 + l0] = v[q];
  }
  wave_sync();
  if (lane < 40) {
#pragma unroll
    for (int a = 0; a < 8; ++a) v[a] = E[rr * 72 + l0 * 9 + a];
    fft8<false>(v);
#pragma unroll
    for (int p = 0; p < 8; ++p) zz[j][rr + 5 * l0 + 40 * p] = v[p];
  }
  __syncthreads();
  if (tid < DF_) {
    const int k = tid;
    const int ka = (k == 320) ? 0 : k, kb = (k == 0) ? 0 : 320 - k;
#pragma unroll
    for (int jj = 0; jj < 8; ++jj) {
      f32x2 a = zz[jj][ka], b = zz[jj][kb];
      b.y = -b.y;
      f32x2 e = a + b, o = rot<false>(a - b);
      v[jj] = e + cmul(o, twk);
    }
    fft8<false>(v);
    f32x2* dst = T + (size_t)s0 * TP + k;
    dst[0] = v[0];
#pragma unroll
    for (int q = 1; q < 8; ++q) dst[(size_t)q * 256 * TP] = cmul(v[q], twq[q - 1]);
  }
}

__global__ __launch_bounds__(256) void k_cols(f32x2* __restrict__ T, const uint16_t* __restrict__ wr,
                                              const uint16_t* __restrict__ wi, const uint16_t* __restrict__ w2,
                                              u32x4* __restrict__ w2t) {
  __shared__ f32x2 e1[2048];
  __shared__ f32x2 e2[4][576];
  const int tid = threadIdx.x, lane = tid & 63, wv = tid >> 6;
  if (blockIdx.x >= 328) {
    const int b0 = ((blockIdx.x - 328) * 4 + wv) * 2;
    tile_weight_block<F_, D_, 5>(w2, w2t, b0, lane);
    tile_weight_block<F_, D_, 5>(w2, w2t, b0 + 1, lane);
    return;
  }
  const int q = blockIdx.x & 7, kg = blockIdx.x >> 3;
  const int c = tid & 7, a = tid >> 3;
  const int k = kg * 8 + c;
  const bool valid = k < DF_;
  f32x2* col = T + (size_t)(q * 256 + a) * TP + k;
  f32x2 v[8];
#pragma unroll
  for (int j1 = 0; j1 < 8; ++j1) v[j1] = valid ? col[(size_t)j1 * 32 * TP] : (f32x2){0.f, 0.f};

  const int b = (lane >> 3) & 3, p1lo = lane >> 5, p1 = 2 * wv + p1lo;
  uint32_t wre[8], wim[8];
#pragma unroll
  for (int e = 0; e < 2; ++e)
#pragma unroll
    for (int mm = 0; mm < 4; ++mm) {
      const int m = p1 + 8 * (2 * b + e) + 64 * mm;
      const int idx = (q + 8 * m) * DF_ + k;
      wre[e * 4 + mm] = valid ? wr[idx] : 0;
      wim[e * 4 + mm] = valid ? wi[idx] : 0;
    }
  f32x2 twa[7], twb[7];
#pragma unroll
  for (int p = 1; p < 8; ++p) twa[p - 1] = twid((float)(a * p) * (1.0f / 256.0f));
#pragma unroll
  for (int p = 1; p < 8; ++p) twb[p - 1] = twid((float)(b * p) * (1.0f / 32.0f));

  fft8<false>(v);
  e1[tid] = v[0];
#pragma unroll
  for (int p = 1; p < 8; ++p) e1[p * 256 + tid] = cmul(v[p], twa[p - 1]);
  __syncthreads();
  f32x2* E1 = e1 + p1 * 256 + (lane & 31);
#pragma unroll
  for (int j2 = 0; j2 < 8; ++j2) v[j2] = E1[32 * j2];
  fft8<false>(v);
  f32x2* E2w = e2[wv] + p1lo * 288 + (lane & 31);
  E2w[0] = v[0];
#pragma unroll
  for (int p = 1; p < 8; ++p) E2w[p * 36] = cmul(v[p], twb[p - 1]);
  wave_sync();
  f32x2* E2r = e2[wv] + p1lo * 288 + (2 * b) * 36 + c;
#pragma unroll
  for (int e = 0; e < 2; ++e)
#pragma unroll
    for (int bb = 0; bb < 4; ++bb) v[e * 4 + bb] = E2r[e * 36 + bb * 8];
  fft4<false>(v[0], v[1], v[2], v[3]);
  fft4<false>(v[4], v[5], v[6], v[7]);
#pragma unroll
  for (int i = 0; i < 8; ++i) {
    f32x2 w;
    w.x = bf2f(wre[i]); w.y = bf2f(wim[i]);
    v[i] = cmul(v[i], w);
  }
  fft4<true>(v[0], v[1], v[2], v[3]);
  fft4<true>(v[4], v[5], v[6], v[7]);
  wave_sync();
#pragma unroll
  for (int e = 0; e < 2; ++e)
#pragma unroll
    for (int bb = 0; bb < 4; ++bb) E2r[e * 36 + bb * 8] = v[e * 4 + bb];
  wave_sync();
  v[0] = E2w[0];
#pragma unroll
  for (int p = 1; p < 8; ++p) v[p] = cmulc(E2w[p * 36], twb[p - 1]);
  fft8<true>(v);
#pragma unroll
  for (int j2 = 0; j2 < 8; ++j2) E1[32 * j2] = v[j2];
  __syncthreads();
  v[0] = e1[tid];
#pragma unroll
  for (int p = 1; p < 8; ++p) v[p] = cmulc(e1[p * 256 + tid], twa[p - 1]);
  fft8<true>(v);
  if (valid) {
#pragma unroll
    for (int j1 = 0; j1 < 8; ++j1) col[(size_t)j1 * 32 * TP] = v[j1];
  }
}

__global__ __launch_bounds__(512) void k_inv_rows(const f32x2* __restrict__ T, const uint32_t* __restrict__ x,
                                                  uint32_t* __restrict__ h, uint32_t* __restrict__ hn) {
  __shared__ f32x2 ex[8][360];
  __shared__ f32x2 xx[8][324];
  const int tid = threadIdx.x, lane = tid & 63, j = tid >> 6;
  const int s0 = blockIdx.x;
  const int row = s0 + 256 * j;
  f32x2 v[8];
  if (tid < DF_) {
    const f32x2* src = T + (size_t)s0 * TP + tid;
#pragma unroll
    for (int q = 0; q < 8; ++q) v[q] = src[(size_t)q * 256 * TP];
  }
  uint32_t xd[5];
#pragma unroll
  for (int i = 0; i < 5; ++i) xd[i] = x[row * 320 + lane + 64 * i];

  f32x2 tw1[4], tw2[7], twq[7], twk[8];
#pragma unroll
  for (int r = 1; r < 5; ++r) tw1[r - 1] = twid((float)(lane * r) * (1.0f / 320.0f));
  const int l0 = lane & 7, rr = lane >> 3;
#pragma unroll
  for (int q = 1; q < 8; ++q) tw2[q - 1] = twid((float)(l0 * q) * (1.0f / 64.0f));
#pragma unroll
  for (int q = 1; q < 8; ++q) twq[q - 1] = twid((float)(s0 * q) * (1.0f / 2048.0f));
#pragma unroll
  for (int p = 0; p < 8; ++p) twk[p] = twid((float)(rr + 5 * l0 + 40 * p) * (1.0f / 640.0f));

  if (tid < DF_) {
#pragma unroll
    for (int q = 1; q < 8; ++q) v[q] = cmulc(v[q], twq[q - 1]);
    fft8<true>(v);
    const bool realbin = (tid == 0) || (tid == 320);
#pragma unroll
    for (int jj = 0; jj < 8; ++jj) {
      if (realbin) v[jj].y = 0.f;
      xx[jj][tid] = v[jj];
    }
  }
  __syncthreads();
  f32x2* E = ex[j];
  if (lane < 40) {
#pragma unroll
    for (int p = 0; p < 8; ++p) {
      const int k = rr + 5 * l0 + 40 * p;
      f32x2 a = xx[j][k], b = xx[j][320 - k];
      b.y = -b.y;
      f32x2 e = a + b, o = cmulc(a - b, twk[p]);
      v[p] = e + rot<true>(o);
    }
    fft8<true>(v);
    E[rr * 72 + l0 * 9] = v[0];
#pragma unroll
    for (int a = 1; a < 8; ++a) E[rr * 72 + l0 * 9 + a] = cmulc(v[a], tw2[a - 1]);
  }
  wave_sync();
  if (lane < 40) {
#pragma unroll
    for (int q = 0; q < 8; ++q) v[q] = E[rr * 72 + q * 9 + l0];
    fft8<true>(v);
  }
  wave_sync();
  if (lane < 40) {
#pragma unroll
    for (int l1 = 0; l1 < 8; ++l1) E[rr * 72 + l0 + 8 * l1] = v[l1];
  }
  wave_sync();
  f32x2 u[5];
  u[0] = E[lane];
#pragma unroll
  for (int r = 1; r < 5; ++r) u[r] = cmulc(E[r * 72 + lane], tw1[r - 1]);
  fft5<true>(u);

  const float sc = 1.0f / 2621440.0f;
  float hv[10], zv[10];
#pragma unroll
  for (int i = 0; i < 5; ++i) {
    hv[2 * i] = bfr(bf2f(xd[i] & 0xffffu) + bfr(u[i].x * sc));
    hv[2 * i + 1] = bfr(bf2f(xd[i] >> 16) + bfr(u[i].y * sc));
  }
  row_layernorm(hv, zv);
  const int rb = row >> 7, mt = (row >> 4) & 7, r16 = row & 15;
#pragma unroll
  for (int i = 0; i < 5; ++i) {
    h[row * 320 + lane + 64 * i] = f2bf(hv[2 * i]) | (f2bf(hv[2 * i + 1]) << 16);
    const int ks = 4 * i + (lane >> 4), g = (lane >> 2) & 3;
    hn[(((rb * 20 + ks) * 8 + mt) * 64 + r16 + 16 * g) * 4 + (lane & 3)] = f2bf(zv[2 * i]) | (f2bf(zv[2 * i + 1]) << 16);
  }
}

#define WAIT_VM(n) asm volatile("s_waitcnt vmcnt(" #n ")" ::: "memory")
#define RAW_BAR() do { asm volatile("s_waitcnt lgkmcnt(0)" ::: "memory"); __builtin_amdgcn_s_barrier(); } while (0)

template <int N> DEV void wait_vm() {
  if constexpr (N == 0) WAIT_VM(0);
  else if constexpr (N == 7) WAIT_VM(7);
  else if constexpr (N == 9) WAIT_VM(9);
  else if constexpr (N == 14) WAIT_VM(14);
  else if constexpr (N == 18) WAIT_VM(18);
  else if constexpr (N == 21) WAIT_VM(21);
  else if constexpr (N == 27) WAIT_VM(27);
  else if constexpr (N == 28) WAIT_VM(28);
}

DEV void glds16(const u32x4* src, char* lds) {
  __builtin_amdgcn_global_load_lds((const unsigned*)src, (unsigned*)lds, 16, 0, 0);
}

__global__ __launch_bounds__(256) void k_gemm1(const u32x4* __restrict__ hn, const u32x4* __restrict__ w1t,
                                               const uint16_t* __restrict__ b1, u32x2* __restrict__ h1) {
  constexpr int NST = 10, NSLOT = 5, SB = 28 * 1024;
  __shared__ __attribute__((aligned(16))) char lds[NSLOT * SB + 256];
  const int tid = threadIdx.x, lane = tid & 63, wv = tid >> 6;
  const int xcd = blockIdx.x & 7, idx = blockIdx.x >> 3;
  const int rb = 4 * (xcd >> 1) + (idx >> 3), cb = 8 * (xcd & 1) + (idx & 7);
  const int wm = wv >> 1, wn = wv & 1;
  const u32x4* asrc = hn + (size_t)rb * 20 * 8 * 64 + lane;
  const u32x4* bsrc = w1t + (size_t)cb * 20 * 6 * 64 + lane;

  auto stage = [&](int st) {
    char* dst = lds + (st % NSLOT) * SB + lane * 16;
#pragma unroll
    for (int p = 0; p < 7; ++p) {
      const int piece = wv + 4 * p;
      if (piece < 16) glds16(asrc + (size_t)(st * 16 + piece) * 64, dst + piece * 1024);
      else glds16(bsrc + (size_t)(st * 12 + piece - 16) * 64, dst + piece * 1024);
    }
  };
  if (wv == 0 && lane < 12) glds16((const u32x4*)(b1 + cb * 96) + lane, lds + NSLOT * SB + lane * 16);
#pragma unroll
  for (int st = 0; st < NSLOT - 1; ++st) stage(st);

  f32x4 acc[4][3];
#pragma unroll
  for (int mi = 0; mi < 4; ++mi)
#pragma unroll
    for (int ni = 0; ni < 3; ++ni) acc[mi][ni] = (f32x4){0.f, 0.f, 0.f, 0.f};

#pragma unroll
  for (int st = 0; st < NST; ++st) {
    constexpr int dummy = 0; (void)dummy;
    const int inflight = ((st + NSLOT - 2 < NST - 1) ? (st + NSLOT - 2) : (NST - 1)) - st;
    if (inflight == 3) wait_vm<21>();
    else if (inflight == 2) wait_vm<14>();
    else if (inflight == 1) wait_vm<7>();
    else wait_vm<0>();
    RAW_BAR();
    if (st + NSLOT - 1 < NST) stage(st + NSLOT - 1);
    const char* base = lds + (st % NSLOT) * SB + lane * 16;
#pragma unroll
    for (int ks = 0; ks < 2; ++ks) {
      bf16x8 af[4], bf[3];
#pragma unroll
      for (int mi = 0; mi < 4; ++mi) af[mi] = *(const bf16x8*)(base + (ks * 8 + 4 * wm + mi) * 1024);
#pragma unroll
      for (int ni = 0; ni < 3; ++ni) bf[ni] = *(const bf16x8*)(base + (16 + ks * 6 + 3 * wn + ni) * 1024);
#pragma unroll
      for (int mi = 0; mi < 4; ++mi)
#pragma unroll
        for (int ni = 0; ni < 3; ++ni)
          acc[mi][ni] = __builtin_amdgcn_mfma_f32_16x16x32_bf16(bf[ni], af[mi], acc[mi][ni], 0, 0, 0);
    }
  }
  float bias[3][4];
#pragma unroll
  for (int ni = 0; ni < 3; ++ni) {
    const u32x2 bb = *(const u32x2*)(lds + NSLOT * SB + ((3 * wn + ni) * 16 + 4 * (lane >> 4)) * 2);
    bias[ni][0] = bf2f(bb.x & 0xffffu); bias[ni][1] = bf2f(bb.x >> 16);
    bias[ni][2] = bf2f(bb.y & 0xffffu); bias[ni][3] = bf2f(bb.y >> 16);
  }
#pragma unroll
  for (int mi = 0; mi < 4; ++mi)
#pragma unroll
    for (int ni = 0; ni < 3; ++ni) {
      uint32_t o[4];
#pragma unroll
      for (int i = 0; i < 4; ++i) {
        float t = bfr(bfr(acc[mi][ni][i]) + bias[ni][i]);
        t = fmaxf(t, 0.f);
        o[i] = f2bf(t * t);
      }
      const int mrow = rb * 128 + (4 * wm + mi) * 16;
      const int rb2 = mrow >> 6, mt2 = (mrow >> 4) & 3;
      const int NT = cb * 6 + 3 * wn + ni;
      const int ks2 = NT >> 1, g = 2 * (NT & 1) + (lane >> 5), half = (lane >> 4) & 1;
      u32x2 ov;
      ov.x = o[0] | (o[1] << 16); ov.y = o[2] | (o[3] << 16);
      h1[((((rb2 * 48 + ks2) * 4 + mt2) * 64 + (lane & 15) + 16 * g) * 2) + half] = ov;
    }
}

__global__ __launch_bounds__(256) void k_gemm2(const u32x4* __restrict__ h1, const u32x4* __restrict__ w2t,
                                               const uint16_t* __restrict__ b2, const uint16_t* __restrict__ h,
                                               uint16_t* __restrict__ out) {
  constexpr int NST = 12, NSLOT = 4, SB = 36 * 1024;
  constexpr int RES_OFF = NSLOT * SB, BIAS_OFF = RES_OFF + 10240;
  __shared__ __attribute__((aligned(16))) char lds[BIAS_OFF + 256];
  const int tid = threadIdx.x, lane = tid & 63, wv = tid >> 6;
  const int xcd = blockIdx.x & 7, idx = blockIdx.x >> 3;
  const int rb2 = 8 * (xcd >> 1) + (idx >> 2), cb2 = 4 * (xcd & 1) + (idx & 3);
  const u32x4* asrc = h1 + (size_t)rb2 * 48 * 4 * 64 + lane;
  const u32x4* bsrc = w2t + (size_t)cb2 * 48 * 5 * 64 + lane;

  auto stage = [&](int st) {
    char* dst = lds + (st % NSLOT) * SB + lane * 16;
#pragma unroll
    for (int p = 0; p < 9; ++p) {
      const int piece = wv + 4 * p;
      if (piece < 16) glds16(asrc + (size_t)(st * 16 + piece) * 64, dst + piece * 1024);
      else glds16(bsrc + (size_t)(st * 20 + piece - 16) * 64, dst + piece * 1024);
    }
  };
  if (wv == 0 && lane < 10) glds16((const u32x4*)(b2 + cb2 * 80) + lane, lds + BIAS_OFF + lane * 16);
#pragma unroll
  for (int p = 0; p < 3; ++p) {
    const int piece = wv + 4 * p;
    if (piece < 10) {
      const int id = piece * 64 + lane, r = id / 10, ch = id % 10;
      glds16((const u32x4*)(h + (size_t)(rb2 * 64 + r) * D_ + cb2 * 80) + ch, lds + RES_OFF + id * 16);
    }
  }
#pragma unroll
  for (int st = 0; st < NSLOT - 1; ++st) stage(st);

  const int mrow = rb2 * 64 + wv * 16 + (lane & 15);
  const int ncol = cb2 * 80 + 4 * (lane >> 4);

  f32x4 acc[5];
#pragma unroll
  for (int ni = 0; ni < 5; ++ni) acc[ni] = (f32x4){0.f, 0.f, 0.f, 0.f};

#pragma unroll
  for (int st = 0; st < NST; ++st) {
    const int inflight = ((st + NSLOT - 2 < NST - 1) ? (st + NSLOT - 2) : (NST - 1)) - st;
    if (inflight == 2) wait_vm<18>();
    else if (inflight == 1) wait_vm<9>();
    else wait_vm<0>();
    RAW_BAR();
    if (st + NSLOT - 1 < NST) stage(st + NSLOT - 1);
    const char* base = lds + (st % NSLOT) * SB + lane * 16;
#pragma unroll
    for (int ks = 0; ks < 4; ++ks) {
      bf16x8 af, bf[5];
      af = *(const bf16x8*)(base + (ks * 4 + wv) * 1024);
#pragma unroll
      for (int ni = 0; ni < 5; ++ni) bf[ni] = *(const bf16x8*)(base + (16 + ks * 5 + ni) * 1024);
#pragma unroll
      for (int ni = 0; ni < 5; ++ni) acc[ni] = __builtin_amdgcn_mfma_f32_16x16x32_bf16(bf[ni], af, acc[ni], 0, 0, 0);
    }
  }
#pragma unroll
  for (int ni = 0; ni < 5; ++ni) {
    const u32x2 bb = *(const u32x2*)(lds + BIAS_OFF + (16 * ni + 4 * (lane >> 4)) * 2);
    const u32x2 rs = *(const u32x2*)(lds + RES_OFF + (wv * 16 + (lane & 15)) * 160 + (16 * ni + 4 * (lane >> 4)) * 2);
    float bv[4] = {bf2f(bb.x & 0xffffu), bf2f(bb.x >> 16), bf2f(bb.y & 0xffffu), bf2f(bb.y >> 16)};
    float rv[4] = {bf2f(rs.x & 0xffffu), bf2f(rs.x >> 16), bf2f(rs.y & 0xffffu), bf2f(rs.y >> 16)};
    uint32_t o[4];
#pragma unroll
    for (int i = 0; i < 4; ++i) o[i] = f2bf(rv[i] + bfr(bfr(acc[ni][i]) + bv[i]));
    u32x2 ov;
    ov.x = o[0] | (o[1] << 16); ov.y = o[2] | (o[3] << 16);
    *(u32x2*)(out + (size_t)mrow * D_ + ncol + 16 * ni) = ov;
  }
}

extern "C" void kernel_launch(void* const* d_in, const int* in_sizes, int n_in, void* d_out, int out_size,
                              void* d_ws, size_t ws_size, hipStream_t stream) {
  const uint32_t* x = (const uint32_t*)d_in[0];
  const uint16_t* wr = (const uint16_t*)d_in[1];
  const uint16_t* wi = (const uint16_t*)d_in[2];
  const uint16_t* w1 = (const uint16_t*)d_in[4];
  const uint16_t* b1 = (const uint16_t*)d_in[5];
  const uint16_t* w2 = (const uint16_t*)d_in[6];
  const uint16_t* b2 = (const uint16_t*)d_in[7];
  char* ws = (char*)d_ws;
  size_t off = 0;
  f32x2* T = (f32x2*)(ws + off); off += (size_t)8 * 256 * TP * 8;
  uint32_t* h = (uint32_t*)(ws + off); off += (size_t)S_ * D_ * 2;
  uint32_t* hn = (uint32_t*)(ws + off); off += (size_t)S_ * D_ * 2;
  u32x4* h1 = (u32x4*)(ws + off); off += (size_t)S_ * F_ * 2;
  u32x4* w1t = (u32x4*)(ws + off); off += (size_t)D_ * F_ * 2;
  u32x4* w2t = (u32x4*)(ws + off); off += (size_t)D_ * F_ * 2;

  k_fwd_rows<<<256 + 240, 512, 0, stream>>>(x, T, w1, w1t);
  k_cols<<<328 + 240, 256, 0, stream>>>(T, wr, wi, w2, w2t);
  k_inv_rows<<<256, 512, 0, stream>>>(T, x, h, hn);
  k_gemm1<<<256, 256, 0, stream>>>((const u32x4*)hn, w1t, b1, (u32x2*)h1);
  k_gemm2<<<256, 256, 0, stream>>>(h1, w2t, b2, (const uint16_t*)h, (uint16_t*)d_out);
}
```

```cpp
#define W2_MODE 1
#define WT_C
#define WT_D
#include <hip/hip_runtime.h>
#include <hip/hip_bf16.h>
#include <stdint.h>

typedef float f32x2 __attribute__((ext_vector_type(2)));
typedef float f32x4 __attribute__((ext_vector_type(4)));
typedef short bf16x8 __attribute__((ext_vector_type(8)));
typedef unsigned int u32x4 __attribute__((ext_vector_type(4)));
typedef unsigned int u32x2 __attribute__((ext_vector_type(2)));

#define DEV __device__ __forceinline__

#define STAMP(kern, i)
#define STAMP_END(kern, i)
#define STAMP_TID 0

#define PF_W 1
static constexpr int S_ = 2048, D_ = 640, F_ = 1536, DF_ = 321;
static constexpr int TP = 328;

DEV float bf_lo(uint32_t p) { return __builtin_bit_cast(float, p << 16); }
DEV float bf_hi(uint32_t p) { return __builtin_bit_cast(float, p & 0xffff0000u); }
DEV f32x2 bf_unpack(uint32_t p) { f32x2 r; r.x = bf_lo(p); r.y = bf_hi(p); return r; }
typedef __bf16 bf16x2_t __attribute__((ext_vector_type(2)));
DEV uint32_t bf_pack(f32x2 v) {
  return __builtin_bit_cast(uint32_t, __builtin_convertvector(v, bf16x2_t));
}
DEV f32x2 bf_round(f32x2 v) { return bf_unpack(bf_pack(v)); }
DEV float bfr(float f) { f32x2 v; v.x = f; v.y = f; return bf_lo(bf_pack(v)); }

DEV f32x2 cadd_mi(f32x2 b, f32x2 d) {
  f32x2 r;
  asm("v_pk_add_f32 %0, %1, %2 op_sel:[0,1] op_sel_hi:[1,0] neg_hi:[0,1]" : "=v"(r) : "v"(b), "v"(d));
  return r;
}
DEV f32x2 cadd_pi(f32x2 b, f32x2 d) {
  f32x2 r;
  asm("v_pk_add_f32 %0, %1, %2 op_sel:[0,1] op_sel_hi:[1,0] neg_lo:[0,1]" : "=v"(r) : "v"(b), "v"(d));
  return r;
}
DEV f32x2 cnadd_mi(f32x2 b, f32x2 d) {
  f32x2 r;
  asm("v_pk_add_f32 %0, %1, %2 op_sel:[0,1] op_sel_hi:[1,0] neg_lo:[1,0] neg_hi:[1,1]" : "=v"(r) : "v"(b), "v"(d));
  return r;
}
DEV f32x2 cnadd_pi(f32x2 b, f32x2 d) {
  f32x2 r;
  asm("v_pk_add_f32 %0, %1, %2 op_sel:[0,1] op_sel_hi:[1,0] neg_lo:[1,1] neg_hi:[1,0]" : "=v"(r) : "v"(b), "v"(d));
  return r;
}
DEV f32x2 cfma_mi(f32x2 s, f32x2 d, f32x2 b) {
  f32x2 r;
  asm("v_pk_fma_f32 %0, %1, %2, %3 op_sel:[0,1,0] op_sel_hi:[1,0,1] neg_hi:[0,1,0]" : "=v"(r) : "v"(s), "v"(d), "v"(b));
  return r;
}
DEV f32x2 cfma_pi(f32x2 s, f32x2 d, f32x2 b) {
  f32x2 r;
  asm("v_pk_fma_f32 %0, %1, %2, %3 op_sel:[0,1,0] op_sel_hi:[1,0,1] neg_lo:[0,1,0]" : "=v"(r) : "v"(s), "v"(d), "v"(b));
  return r;
}
DEV f32x2 pfma(f32x2 a, f32x2 b, f32x2 c) { return __builtin_elementwise_fma(a, b, c); }
DEV f32x2 cmul(f32x2 a, f32x2 w) {
  f32x2 t, r;
  asm("v_pk_mul_f32 %0, %1, %2 op_sel:[0,0] op_sel_hi:[0,1]" : "=v"(t) : "v"(a), "v"(w));
  asm("v_pk_fma_f32 %0, %1, %2, %3 op_sel:[1,1,0] op_sel_hi:[1,0,1] neg_lo:[0,1,0]" : "=v"(r) : "v"(a), "v"(w), "v"(t));
  return r;
}
DEV f32x2 cmulc(f32x2 a, f32x2 w) {
  f32x2 t, r;
  asm("v_pk_mul_f32 %0, %1, %2 op_sel:[0,0] op_sel_hi:[0,1] neg_hi:[0,1]" : "=v"(t) : "v"(a), "v"(w));
  asm("v_pk_fma_f32 %0, %1, %2, %3 op_sel:[1,1,0] op_sel_hi:[1,0,1]" : "=v"(r) : "v"(a), "v"(w), "v"(t));
  return r;
}
DEV f32x2 twid(float f) {
  f32x2 r;
  r.x = __builtin_amdgcn_cosf(f);
  r.y = __builtin_amdgcn_sinf(-f);
  return r;
}
DEV void st_wt(f32x2* p, f32x2 v) {
  __hip_atomic_store((unsigned long long*)p, __builtin_bit_cast(unsigned long long, v), __ATOMIC_RELAXED,
                     __HIP_MEMORY_SCOPE_AGENT);
}
DEV void st_wt32(uint32_t* p, uint32_t v) { __hip_atomic_store(p, v, __ATOMIC_RELAXED, __HIP_MEMORY_SCOPE_AGENT); }
DEV void st_wt64(u32x2* p, u32x2 v) {
  __hip_atomic_store((unsigned long long*)p, __builtin_bit_cast(unsigned long long, v), __ATOMIC_RELAXED,
                     __HIP_MEMORY_SCOPE_AGENT);
}
DEV void st_wt128(void* p, u32x4 v) {
  asm volatile("global_store_dwordx4 %0, %1, off sc1\n\ts_nop 1" ::"v"(p), "v"(v) : "memory");
}
#define ST_H(p, v) st_wt32(p, v)
#define ST_H1(p, v) st_wt64(p, v)
template <int N> DEV void pin(f32x2 (&a)[N]) {
#pragma unroll
  for (int i = 0; i < N; ++i) asm volatile("" : "+v"(a[i]));
}
template <bool INV> DEV f32x2 add_rot(f32x2 b, f32x2 d) { return INV ? cadd_pi(b, d) : cadd_mi(b, d); }
template <bool INV> DEV f32x2 sub_rot(f32x2 b, f32x2 d) { return INV ? cadd_mi(b, d) : cadd_pi(b, d); }

template <bool INV> DEV void fft4(f32x2& a0, f32x2& a1, f32x2& a2, f32x2& a3) {
  f32x2 b0 = a0 + a2, b1 = a0 - a2, b2 = a1 + a3, d = a1 - a3;
  a0 = b0 + b2; a2 = b0 - b2;
  a1 = add_rot<INV>(b1, d); a3 = sub_rot<INV>(b1, d);
}
template <bool INV> DEV void fft8(f32x2 (&v)[8]) {
  const f32x2 r = {0.70710678118654752440f, 0.70710678118654752440f};
  const f32x2 nr = {-0.70710678118654752440f, -0.70710678118654752440f};
  f32x2 e0 = v[0] + v[4], e1 = v[1] + v[5], e2 = v[2] + v[6], e3 = v[3] + v[7];
  f32x2 o0 = v[0] - v[4], o1 = v[1] - v[5], o2 = v[2] - v[6], o3 = v[3] - v[7];
  fft4<INV>(e0, e1, e2, e3);
  f32x2 p1 = INV ? cadd_pi(o1, o1) : cadd_mi(o1, o1);
  f32x2 p3 = INV ? cnadd_pi(o3, o3) : cnadd_mi(o3, o3);
  f32x2 s = p1 + p3, d = p1 - p3;
  f32x2 b0 = add_rot<INV>(o0, o2), b1 = sub_rot<INV>(o0, o2);
  v[0] = e0; v[2] = e1; v[4] = e2; v[6] = e3;
  v[1] = pfma(r, s, b0); v[5] = pfma(nr, s, b0);
  v[3] = INV ? cfma_pi(r, d, b1) : cfma_mi(r, d, b1);
  v[7] = INV ? cfma_mi(r, d, b1) : cfma_pi(r, d, b1);
}
template <bool INV> DEV void fft5(f32x2 (&v)[5]) {
  const f32x2 c1 = {0.30901699437494742410f, 0.30901699437494742410f};
  const f32x2 c2 = {-0.80901699437494742410f, -0.80901699437494742410f};
  const f32x2 s1 = {0.95105651629515357212f, 0.95105651629515357212f};
  const f32x2 s2 = {0.58778525229247312917f, 0.58778525229247312917f};
  const f32x2 ns1 = {-0.95105651629515357212f, -0.95105651629515357212f};
  f32x2 t1 = v[1] + v[4], t2 = v[2] + v[3], t3 = v[1] - v[4], t4 = v[2] - v[3];
  f32x2 a1 = pfma(c2, t2, pfma(c1, t1, v[0]));
  f32x2 a2 = pfma(c1, t2, pfma(c2, t1, v[0]));
  f32x2 b1 = pfma(s2, t4, s1 * t3);
  f32x2 b2 = pfma(ns1, t4, s2 * t3);
  v[0] = v[0] + t1 + t2;
  v[1] = add_rot<INV>(a1, b1); v[4] = sub_rot<INV>(a1, b1);
  v[2] = add_rot<INV>(a2, b2); v[3] = sub_rot<INV>(a2, b2);
}

template <int CTRL> DEV float dppmov(float v) {
  return __builtin_bit_cast(float, __builtin_amdgcn_update_dpp(0, __builtin_bit_cast(int, v), CTRL, 0xf, 0xf, false));
}
DEV float wave_sum(float v) {
  v += dppmov<0xB1>(v);
  v += dppmov<0x4E>(v);
  v += dppmov<0x141>(v);
  v += dppmov<0x140>(v);
  int iv = __builtin_bit_cast(int, v);
  float a = __builtin_bit_cast(float, __builtin_amdgcn_readlane(iv, 0));
  float b = __builtin_bit_cast(float, __builtin_amdgcn_readlane(iv, 16));
  float c = __builtin_bit_cast(float, __builtin_amdgcn_readlane(iv, 32));
  float d = __builtin_bit_cast(float, __builtin_amdgcn_readlane(iv, 48));
  return (a + b) + (c + d);
}
DEV void wave_sync() {
  __builtin_amdgcn_fence(__ATOMIC_RELEASE, "wavefront");
  __builtin_amdgcn_wave_barrier();
  __builtin_amdgcn_fence(__ATOMIC_ACQUIRE, "wavefront");
}

DEV void row_layernorm(const f32x2 (&xv)[5], uint32_t (&zp)[5]) {
  f32x2 s2 = (xv[0] + xv[1]) + (xv[2] + xv[3]) + xv[4];
  const float mean = wave_sum(s2.x + s2.y) * (1.0f / 640.0f);
  const f32x2 nm = {-mean, -mean};
  f32x2 q2 = {0.f, 0.f};
#pragma unroll
  for (int i = 0; i < 5; ++i) { f32x2 d = xv[i] + nm; q2 = pfma(d, d, q2); }
  const float var = wave_sum(q2.x + q2.y) * (1.0f / 640.0f);
  f32x2 mv; mv.x = mean; mv.y = var;
  mv = bf_round(mv);
  const float den = bfr(sqrtf(bfr(mv.y + 1e-5f)));
  const float rden = 1.0f / den;
  const f32x2 nmb = {-mv.x, -mv.x}, rd = {rden, rden};
#pragma unroll
  for (int i = 0; i < 5; ++i) zp[i] = bf_pack(bf_round(xv[i] + nmb) * rd);
}

__global__ __launch_bounds__(512) void k_fwd_rows(const uint32_t* __restrict__ x, f32x2* __restrict__ T,
                                                  const uint16_t* __restrict__ wr, const uint16_t* __restrict__ wi,
                                                  const uint32_t* __restrict__ w1, const u32x4* __restrict__ w2,
                                                  u32x4* __restrict__ w2p) {
  __shared__ f32x2 ex[8][360];
  __shared__ f32x2 zz[8][320];
  const int tid = threadIdx.x, lane = tid & 63, j = tid >> 6;
  STAMP(0, 0);
  const int s0 = 32 * (blockIdx.x & 7) + (blockIdx.x >> 3);
  const int row = s0 + 256 * j;
  uint32_t xd[5];
#pragma unroll
  for (int i = 0; i < 5; ++i) xd[i] = x[row * 320 + lane + 64 * i];
  uint32_t pf = 0;
  if (lane < 12) {
    const int t = (blockIdx.x & 7) + 8 * (8 * (blockIdx.x >> 3) + j);
    pf = ((lane < 6) ? wr : wi)[t * DF_ + min(64 * (lane % 6), DF_ - 1)];
  } else if (PF_W && lane >= 56) {
    const int l = j * 8 + (lane - 56);
    pf = w1[(blockIdx.x * 60 + min(l, 59)) * 32];
  }
  const int wid = min((int)blockIdx.x * 512 + tid, 122879);
  const u32x4 wchunk = w2[wid];

  f32x2 tw1[4], tw2[7], twq[7];
#pragma unroll
  for (int r = 1; r < 5; ++r) tw1[r - 1] = twid((float)(lane * r) * (1.0f / 320.0f));
  const int l0 = lane & 7, rr = lane >> 3;
#pragma unroll
  for (int q = 1; q < 8; ++q) tw2[q - 1] = twid((float)(l0 * q) * (1.0f / 64.0f));
#pragma unroll
  for (int q = 1; q < 8; ++q) twq[q - 1] = twid((float)(s0 * q) * (1.0f / 2048.0f));
  f32x2 twk_[1] = {twid((float)tid * (1.0f / 640.0f))};
  pin(tw1); pin(tw2); pin(twq); pin(twk_);
  const f32x2 twk = twk_[0];
  __builtin_amdgcn_sched_barrier(0);
  STAMP(0, 1);

  f32x2 u[5];
  {
    f32x2 xv[5];
    uint32_t zp[5];
#pragma unroll
    for (int i = 0; i < 5; ++i) xv[i] = bf_unpack(xd[i]);
    row_layernorm(xv, zp);
#pragma unroll
    for (int i = 0; i < 5; ++i) u[i] = bf_unpack(zp[i]);
  }
  STAMP(0, 2);

  f32x2* E = ex[j];
  fft5<false>(u);
  E[lane] = u[0];
#pragma unroll
  for (int r = 1; r < 5; ++r) E[r * 72 + lane] = cmul(u[r], tw1[r - 1]);
  wave_sync();
  if (lane < 40) {
    f32x2 v[8];
    f32x2* Er = E + rr * 72;
#pragma unroll
    for (int l1 = 0; l1 < 8; ++l1) v[l1] = Er[l0 + 8 * l1];
    fft8<false>(v);
    wave_sync();
    Er[l0] = v[0];
#pragma unroll
    for (int q = 1; q < 8; ++q) Er[q * 9 + l0] = cmul(v[q], tw2[q - 1]);
    wave_sync();
#pragma unroll
    for (int a = 0; a < 8; ++a) v[a] = Er[l0 * 9 + a];
    fft8<false>(v);
    f32x2* Z = zz[j] + rr + 5 * l0;
#pragma unroll
    for (int p = 0; p < 8; ++p) Z[40 * p] = v[p];
  }
  if (blockIdx.x < 240) {
    const int wk_ = wid / 80, wc_ = wid % 80;
    const int rho = (wk_ & ~12) | ((wk_ & 4) << 1) | ((wk_ & 8) >> 1);
    st_wt128(w2p + ((wc_ / 10) * 1536 + rho) * 10 + (wc_ % 10), wchunk);
  }
  STAMP(0, 3);
  __syncthreads();
  STAMP(0, 4);
  if (j < 6) {
    const int k = tid;
    const int ka = (k >= 320) ? 0 : k, kb = (k == 0 || k >= 320) ? 0 : 320 - k;
    f32x2 v[8];
#pragma unroll
    for (int jj = 0; jj < 8; ++jj) {
      const f32x2 a = zz[jj][ka], b = zz[jj][kb];
      f32x2 e, d;
      e.x = a.x + b.x; e.y = a.y - b.y;
      d.x = a.x - b.x; d.y = a.y + b.y;
      v[jj] = cadd_mi(e, cmul(d, twk));
    }
    fft8<false>(v);
    if (k < DF_) {
      f32x2* dst = T + (size_t)s0 * TP;
      st_wt(dst + k, v[0]);
#pragma unroll
      for (int q = 1; q < 8; ++q) st_wt(dst + (size_t)q * 256 * TP + k, cmul(v[q], twq[q - 1]));
    }
  }
  asm volatile("" ::"v"(pf));
  STAMP(0, 5);
  STAMP_END(0, 6);
}

__global__ __launch_bounds__(256) void k_cols(f32x2* __restrict__ T, const uint16_t* __restrict__ wr,
                                              const uint16_t* __restrict__ wi) {
  __shared__ f32x2 e1[2048];
  __shared__ f32x2 e2[4][576];
  __shared__ __attribute__((aligned(16))) uint32_t wl[2048];
  const int tid = threadIdx.x, lane = tid & 63, wv = tid >> 6;
  STAMP(1, 0);
  const int q = blockIdx.x & 7, kg = blockIdx.x >> 3;
  const int c = tid & 7, a = tid >> 3;
  const int k = kg * 8 + c;
  const bool valid = k < DF_;
  f32x2* colbase = T + (size_t)(q * 256) * TP;
  const uint32_t coff = a * TP + (valid ? k : DF_ - 1);
  f32x2 v[8];
#pragma unroll
  for (int j1 = 0; j1 < 8; ++j1) v[j1] = (colbase + (size_t)j1 * 32 * TP)[coff];
  const int kb = min(8 * kg, DF_ - 8);
  u32x4 wre, wim;
  __builtin_memcpy(&wre, wr + (size_t)(q + 8 * tid) * DF_ + kb, 16);
  __builtin_memcpy(&wim, wi + (size_t)(q + 8 * tid) * DF_ + kb, 16);

  const int b = (lane >> 3) & 3, p1lo = lane >> 5, p1 = 2 * wv + p1lo;
  f32x2 twa[7], twb[7];
#pragma unroll
  for (int p = 1; p < 8; ++p) twa[p - 1] = twid((float)(a * p) * (1.0f / 256.0f));
#pragma unroll
  for (int p = 1; p < 8; ++p) twb[p - 1] = twid((float)(b * p) * (1.0f / 32.0f));
  pin(twa); pin(twb);
  __builtin_amdgcn_sched_barrier(0);
  STAMP(1, 1);

  fft8<false>(v);
  e1[tid] = v[0];
#pragma unroll
  for (int p = 1; p < 8; ++p) e1[p * 256 + tid] = cmul(v[p], twa[p - 1]);
  {
    u32x4 o0, o1;
    o0.x = __builtin_amdgcn_perm(wim.x, wre.x, 0x05040100); o0.y = __builtin_amdgcn_perm(wim.x, wre.x, 0x07060302);
    o0.z = __builtin_amdgcn_perm(wim.y, wre.y, 0x05040100); o0.w = __builtin_amdgcn_perm(wim.y, wre.y, 0x07060302);
    o1.x = __builtin_amdgcn_perm(wim.z, wre.z, 0x05040100); o1.y = __builtin_amdgcn_perm(wim.z, wre.z, 0x07060302);
    o1.z = __builtin_amdgcn_perm(wim.w, wre.w, 0x05040100); o1.w = __builtin_amdgcn_perm(wim.w, wre.w, 0x07060302);
    *(u32x4*)(wl + tid * 8) = o0;
    *(u32x4*)(wl + tid * 8 + 4) = o1;
  }
  STAMP(1, 2);
  __syncthreads();
  STAMP(1, 3);
  uint32_t wf[8];
  {
    const uint32_t* wlp = wl + (p1 + 16 * b) * 8 + min(8 * kg + c - kb, 7);
#pragma unroll
    for (int e = 0; e < 2; ++e)
#pragma unroll
      for (int mm = 0; mm < 4; ++mm) wf[e * 4 + mm] = wlp[(8 * e + 64 * mm) * 8];
  }
  f32x2* E1 = e1 + p1 * 256 + (lane & 31);
#pragma unroll
  for (int j2 = 0; j2 < 8; ++j2) v[j2] = E1[32 * j2];
  fft8<false>(v);
  f32x2* E2w = e2[wv] + p1lo * 288 + (lane & 31);
  E2w[0] = v[0];
#pragma unroll
  for (int p = 1; p < 8; ++p) E2w[p * 36] = cmul(v[p], twb[p - 1]);
  wave_sync();
  f32x2* E2r = e2[wv] + p1lo * 288 + (2 * b) * 36 + c;
#pragma unroll
  for (int e = 0; e < 2; ++e)
#pragma unroll
    for (int bb = 0; bb < 4; ++bb) v[e * 4 + bb] = E2r[e * 36 + bb * 8];
  fft4<false>(v[0], v[1], v[2], v[3]);
  fft4<false>(v[4], v[5], v[6], v[7]);
  STAMP(1, 4);
#pragma unroll
  for (int i = 0; i < 8; ++i) v[i] = cmul(v[i], bf_unpack(wf[i]));
  fft4<true>(v[0], v[1], v[2], v[3]);
  fft4<true>(v[4], v[5], v[6], v[7]);
  STAMP(1, 5);
#pragma unroll
  for (int e = 0; e < 2; ++e)
#pragma unroll
    for (int bb = 0; bb < 4; ++bb) E2r[e * 36 + bb * 8] = v[e * 4 + bb];
  wave_sync();
  v[0] = E2w[0];
#pragma unroll
  for (int p = 1; p < 8; ++p) v[p] = cmulc(E2w[p * 36], twb[p - 1]);
  fft8<true>(v);
#pragma unroll
  for (int j2 = 0; j2 < 8; ++j2) E1[32 * j2] = v[j2];
  STAMP(1, 6);
  __syncthreads();
  STAMP(1, 7);
  v[0] = e1[tid];
#pragma unroll
  for (int p = 1; p < 8; ++p) v[p] = cmulc(e1[p * 256 + tid], twa[p - 1]);
  fft8<true>(v);
  if (valid) {
#pragma unroll
    for (int j1 = 0; j1 < 8; ++j1) st_wt(colbase + (size_t)j1 * 32 * TP + coff, v[j1]);
  }
  STAMP(1, 8);
  STAMP_END(1, 9);
}

__global__ __launch_bounds__(512) void k_inv_rows(const f32x2* __restrict__ T, const uint32_t* __restrict__ x,
                                                  uint32_t* __restrict__ h, uint32_t* __restrict__ hn) {
  __shared__ f32x2 ex[8][360];
  __shared__ f32x2 xx[8][384];
  const int tid = threadIdx.x, lane = tid & 63, j = tid >> 6;
  STAMP(2, 0);
  const int s0 = 32 * (blockIdx.x & 7) + (blockIdx.x >> 3);
  const int row = s0 + 256 * j;
  const int prow = s0 * 8 + j;
  f32x2 v[8];
  {
    const f32x2* src = T + (size_t)s0 * TP;
    const uint32_t kk = min(tid, DF_ - 1);
#pragma unroll
    for (int q = 0; q < 8; ++q) v[q] = (src + (size_t)q * 256 * TP)[kk];
  }
  uint32_t xd[5];
#pragma unroll
  for (int i = 0; i < 5; ++i) xd[i] = x[row * 320 + lane + 64 * i];

  f32x2 tw1[4], tw2[7], twq[7], twk[8];
#pragma unroll
  for (int r = 1; r < 5; ++r) tw1[r - 1] = twid((float)(lane * r) * (1.0f / 320.0f));
  const int l0 = lane & 7, rr = lane >> 3;
#pragma unroll
  for (int q = 1; q < 8; ++q) tw2[q - 1] = twid((float)(l0 * q) * (1.0f / 64.0f));
#pragma unroll
  for (int q = 1; q < 8; ++q) twq[q - 1] = twid((float)(s0 * q) * (1.0f / 2048.0f));
#pragma unroll
  for (int p = 0; p < 8; ++p) twk[p] = twid((float)(rr + 5 * l0 + 40 * p) * (1.0f / 640.0f));
  pin(tw1); pin(tw2); pin(twq); pin(twk);
  __builtin_amdgcn_sched_barrier(0);
  STAMP(2, 1);

  if (j < 6) {
#pragma unroll
    for (int q = 1; q < 8; ++q) v[q] = cmulc(v[q], twq[q - 1]);
    fft8<true>(v);
    f32x2 msk;
    msk.x = 1.0f; msk.y = (tid == 0 || tid == 320) ? 0.0f : 1.0f;
#pragma unroll
    for (int jj = 0; jj < 8; ++jj) xx[jj][tid] = v[jj] * msk;
  }
  STAMP(2, 2);
  __syncthreads();
  STAMP(2, 3);
  f32x2* E = ex[j];
  if (lane < 40) {
    f32x2* Er = E + rr * 72;
    const f32x2* Xa = xx[j] + rr + 5 * l0;
    const f32x2* Xb = xx[j] + 320 - rr - 5 * l0;
#pragma unroll
    for (int p = 0; p < 8; ++p) {
      const f32x2 a = Xa[40 * p], b = Xb[-40 * p];
      f32x2 e, d;
      e.x = a.x + b.x; e.y = a.y - b.y;
      d.x = a.x - b.x; d.y = a.y + b.y;
      v[p] = cadd_pi(e, cmulc(d, twk[p]));
    }
    fft8<true>(v);
    Er[l0 * 9] = v[0];
#pragma unroll
    for (int a = 1; a < 8; ++a) Er[l0 * 9 + a] = cmulc(v[a], tw2[a - 1]);
    wave_sync();
#pragma unroll
    for (int q = 0; q < 8; ++q) v[q] = Er[q * 9 + l0];
    fft8<true>(v);
    wave_sync();
#pragma unroll
    for (int l1 = 0; l1 < 8; ++l1) Er[l0 + 8 * l1] = v[l1];
  }
  wave_sync();
  f32x2 u[5];
  u[0] = E[lane];
#pragma unroll
  for (int r = 1; r < 5; ++r) u[r] = cmulc(E[r * 72 + lane], tw1[r - 1]);
  fft5<true>(u);
  STAMP(2, 4);

  const f32x2 sc = {1.0f / 2621440.0f, 1.0f / 2621440.0f};
  f32x2 hv[5];
  uint32_t zp[5];
#pragma unroll
  for (int i = 0; i < 5; ++i) {
    const uint32_t hp = bf_pack(bf_unpack(xd[i]) + bf_round(u[i] * sc));
    ST_H(h + prow * 320 + lane + 64 * i, hp);
    hv[i] = bf_unpack(hp);
  }
  row_layernorm(hv, zp);
  STAMP(2, 5);
#pragma unroll
  for (int i = 0; i < 5; ++i) ST_H(hn + prow * 320 + lane + 64 * i, zp[i]);
  STAMP(2, 6);
  STAMP_END(2, 7);
}

template <int N> DEV void wait_vm() { asm volatile("s_waitcnt vmcnt(%0)" ::"n"(N) : "memory"); }
#define RAW_BAR() do { asm volatile("s_waitcnt lgkmcnt(0)" ::: "memory"); __builtin_amdgcn_s_barrier(); } while (0)

DEV void glds16(const void* src, char* lds) {
  __builtin_amdgcn_global_load_lds((const unsigned*)src, (unsigned*)lds, 16, 0, 0);
}
DEV uint32_t pk_relu_bf16(uint32_t p) {
  uint32_t r;
  asm("v_pk_max_i16 %0, %1, 0" : "=v"(r) : "v"(p));
  return r;
}

typedef short v4s_t __attribute__((ext_vector_type(4)));
typedef __attribute__((address_space(3))) v4s_t* lds_v4s_ptr;
DEV bf16x8 tr_frag(const char* p0, const char* p1) {
  const v4s_t a = __builtin_amdgcn_ds_read_tr16_b64_v4i16((lds_v4s_ptr)p0);
  const v4s_t b = __builtin_amdgcn_ds_read_tr16_b64_v4i16((lds_v4s_ptr)p1);
  return __builtin_shufflevector(a, b, 0, 1, 2, 3, 4, 5, 6, 7);
}

#undef STAMP_TID
#define STAMP_TID 512
__global__ __launch_bounds__(1024) void k_gemm1(const char* __restrict__ hn, const char* __restrict__ w1,
                                                const uint16_t* __restrict__ b1, u32x2* __restrict__ h1) {
  constexpr int NST = 10, NSLOT = 5, SB = 28 * 1024;
  __shared__ __attribute__((aligned(16))) char lds[NSLOT * SB + 256];
  const int tid = threadIdx.x, lane = tid & 63;
  const int wv = __builtin_amdgcn_readfirstlane(tid >> 6);
  const int xcd = blockIdx.x & 7, idx = blockIdx.x >> 3;
  const int rb = 2 * xcd + (idx >> 4), cb = idx & 15;
  if (wv < 8) {
    const int lw = wv;
    const int arow = lane >> 3;
    const char* asrc = hn + (size_t)(rb * 128 + 16 * lw + arow) * 1280 + (((lane & 7) ^ arow) << 4);
    const int id0 = lw * 64 + lane, r0 = id0 / 12, c0 = (id0 % 12) ^ (((r0 >> 3) & 1) << 1);
    const int id1 = (8 + lw) * 64 + lane, r1 = id1 / 12, c1 = (id1 % 12) ^ (((r1 >> 3) & 1) << 1);
    const char* bsrc0 = w1 + (size_t)r0 * 3072 + cb * 192 + c0 * 16;
    const char* bsrc1 = w1 + (size_t)r1 * 3072 + cb * 192 + c1 * 16;
    auto stage = [&](int st) {
      char* dst = lds + (st % NSLOT) * SB + lane * 16;
      glds16(asrc + st * 128, dst + (2 * lw) * 1024);
      glds16(asrc + st * 128 + 8 * 1280, dst + (2 * lw + 1) * 1024);
      glds16(bsrc0 + (size_t)st * 64 * 3072, dst + (16 + lw) * 1024);
      if (lw < 4) glds16(bsrc1 + (size_t)st * 64 * 3072, dst + (24 + lw) * 1024);
    };
    if (lw == 7 && lane < 12) glds16((const u32x4*)(b1 + cb * 96) + lane, lds + NSLOT * SB + lane * 16);
#pragma unroll
    for (int st = 0; st < NSLOT - 1; ++st) stage(st);
#pragma unroll
    for (int st = 0; st < NST; ++st) {
      constexpr int LAST = NST - 1;
      const int inflight = ((st + NSLOT - 2 < LAST) ? (st + NSLOT - 2) : LAST) - st;
      if (lw < 4) {
        if (inflight == 3) wait_vm<12>(); else if (inflight == 2) wait_vm<8>(); else if (inflight == 1) wait_vm<4>(); else wait_vm<0>();
      } else {
        if (inflight == 3) wait_vm<9>(); else if (inflight == 2) wait_vm<6>(); else if (inflight == 1) wait_vm<3>(); else wait_vm<0>();
      }
      __builtin_amdgcn_s_barrier();
      if (st + NSLOT - 1 < NST) stage(st + NSLOT - 1);
    }
    return;
  }
  STAMP(3, 0);
  const int wm = (wv - 8) >> 1, wn = wv & 1;
  f32x4 acc[2][3];
#pragma unroll
  for (int mi = 0; mi < 2; ++mi)
#pragma unroll
    for (int ni = 0; ni < 3; ++ni) acc[mi][ni] = (f32x4){0.f, 0.f, 0.f, 0.f};
  const int ar = 32 * wm + (lane & 15);
  const int aoff0 = ar * 128 + (((lane >> 4) ^ (ar & 7)) << 4);
  const int aoff1 = ar * 128 + (((4 + (lane >> 4)) ^ (ar & 7)) << 4);
  const int g = lane >> 4, q4 = (lane & 15) >> 2, p4 = lane & 3;
  int boff[3];
#pragma unroll
  for (int ni = 0; ni < 3; ++ni) boff[ni] = 16384 + (8 * g + q4) * 192 + p4 * 8 + (((3 * wn + ni) ^ (g & 1)) << 5);

#pragma unroll
  for (int st = 0; st < NST; ++st) {
    RAW_BAR();
    int slot_off = (st % NSLOT) * SB;
    asm volatile("" : "+s"(slot_off));
    const char* base = lds + slot_off;
    bf16x8 af[2][2], bf[2][3];
#pragma unroll
    for (int ks = 0; ks < 2; ++ks) {
#pragma unroll
      for (int mi = 0; mi < 2; ++mi) af[ks][mi] = *(const bf16x8*)(base + (ks ? aoff1 : aoff0) + mi * 2048);
#pragma unroll
      for (int ni = 0; ni < 3; ++ni) bf[ks][ni] = tr_frag(base + boff[ni] + ks * 6144, base + boff[ni] + ks * 6144 + 768);
    }
#pragma unroll
    for (int ks = 0; ks < 2; ++ks)
#pragma unroll
      for (int mi = 0; mi < 2; ++mi)
#pragma unroll
        for (int ni = 0; ni < 3; ++ni)
          acc[mi][ni] = __builtin_amdgcn_mfma_f32_16x16x32_bf16(bf[ks][ni], af[ks][mi], acc[mi][ni], 0, 0, 0);
  }
  STAMP(3, 6);
#pragma unroll
  for (int ni = 0; ni < 3; ++ni) {
    const u32x2 bb = *(const u32x2*)(lds + NSLOT * SB + ((3 * wn + ni) * 16 + 4 * (lane >> 4)) * 2);
    const f32x2 b01 = bf_unpack(bb.x), b23 = bf_unpack(bb.y);
    const int NT = cb * 6 + 3 * wn + ni;
    const int ks2 = NT >> 1, gg = 2 * (NT & 1) + (lane >> 5), half = (lane >> 4) & 1;
#pragma unroll
    for (int mi = 0; mi < 2; ++mi) {
      f32x2 a01 = {acc[mi][ni][0], acc[mi][ni][1]}, a23 = {acc[mi][ni][2], acc[mi][ni][3]};
      a01 = bf_unpack(pk_relu_bf16(bf_pack(bf_round(a01) + b01)));
      a23 = bf_unpack(pk_relu_bf16(bf_pack(bf_round(a23) + b23)));
      u32x2 ov;
      ov.x = bf_pack(a01 * a01); ov.y = bf_pack(a23 * a23);
      const int mt = rb * 8 + 2 * wm + mi;
      ST_H1(h1 + ((((mt >> 2) * 48 + ks2) * 4 + (mt & 3)) * 64 + (lane & 15) + 16 * gg) * 2 + half, ov);
    }
  }
  STAMP(3, 7);
  STAMP_END(3, 8);
}

__global__ __launch_bounds__(1024) void k_gemm2(const u32x4* __restrict__ h1, const u32x4* __restrict__ w2p,
                                                const char* __restrict__ w2,
                                                const uint16_t* __restrict__ b2, const uint16_t* __restrict__ h,
                                                uint16_t* __restrict__ out) {
  constexpr int NST = 12, NSLOT = 4, SB = 36 * 1024;
  constexpr int RES_OFF = NSLOT * SB, BIAS_OFF = RES_OFF + 10240;
  __shared__ __attribute__((aligned(16))) char lds[BIAS_OFF + 256];
  const int tid = threadIdx.x, lane = tid & 63;
  const int wv = __builtin_amdgcn_readfirstlane(tid >> 6);
  const int xcd = blockIdx.x & 7, idx = blockIdx.x >> 3;
  const int rb2 = 4 * xcd + (idx >> 3), cb2 = idx & 7;
  if (wv < 8) {
    const int lw = wv;
    const u32x4* asrc = h1 + (size_t)rb2 * 48 * 4 * 64 + (2 * lw) * 64 + lane;
    const u32x4* bsrc = w2p + (size_t)cb2 * 1536 * 10 + lw * 64 + lane;
    auto stage = [&](int st) {
      char* dst = lds + (st % NSLOT) * SB + lane * 16;
      glds16(asrc + (size_t)(st * 16) * 64, dst + (2 * lw) * 1024);
      glds16(asrc + (size_t)(st * 16 + 1) * 64, dst + (2 * lw + 1) * 1024);
      glds16(bsrc + st * 1280, dst + (16 + lw) * 1024);
      glds16(bsrc + st * 1280 + 512, dst + (24 + lw) * 1024);
      if (lw < 4) glds16(bsrc + st * 1280 + 1024, dst + (32 + lw) * 1024);
    };
    stage(0);
    if (lw == 7 && lane < 10) glds16((const u32x4*)(b2 + cb2 * 80) + lane, lds + BIAS_OFF + lane * 16);
    {
      const int id = lw * 64 + lane, r = id / 10, ch = id % 10;
      glds16((const u32x4*)(h + (size_t)(rb2 * 64 + r) * D_ + cb2 * 80) + ch, lds + RES_OFF + id * 16);
      if (lw >= 6) {
        const int id2 = (lw + 2) * 64 + lane, r2 = id2 / 10, ch2 = id2 % 10;
        glds16((const u32x4*)(h + (size_t)(rb2 * 64 + r2) * D_ + cb2 * 80) + ch2, lds + RES_OFF + id2 * 16);
      }
    }
#pragma unroll
    for (int st = 1; st < NSLOT - 1; ++st) stage(st);
#pragma unroll
    for (int st = 0; st < NST; ++st) {
      constexpr int LAST = NST - 1;
      const int inflight = ((st + NSLOT - 2 < LAST) ? (st + NSLOT - 2) : LAST) - st;
      if (lw < 4) {
        if (inflight == 2) wait_vm<10>(); else if (inflight == 1) wait_vm<5>(); else wait_vm<0>();
      } else {
        if (inflight == 2) wait_vm<8>(); else if (inflight == 1) wait_vm<4>(); else wait_vm<0>();
      }
      __builtin_amdgcn_s_barrier();
      if (st + NSLOT - 1 < NST) stage(st + NSLOT - 1);
    }
    __builtin_amdgcn_s_barrier();
    return;
  }
  STAMP(4, 0);
  const int wm = wv & 3, wk = (wv >> 2) & 1;
  f32x4 acc[5];
#pragma unroll
  for (int ni = 0; ni < 5; ++ni) acc[ni] = (f32x4){0.f, 0.f, 0.f, 0.f};
  const int g = lane >> 4, q4 = (lane & 15) >> 2, p4 = lane & 3;
  const int boff = 16384 + (16 * (g >> 1) + 4 * (g & 1) + q4 + 32 * wk) * 160 + p4 * 8;
  const int aoff = (wk * 4 + wm) * 1024 + lane * 16;

#pragma unroll
  for (int st = 0; st < NST; ++st) {
    RAW_BAR();
    int slot_off = (st % NSLOT) * SB;
    asm volatile("" : "+s"(slot_off));
    const char* base = lds + slot_off;
    bf16x8 af[2], bf[2][5];
#pragma unroll
    for (int kk = 0; kk < 2; ++kk) {
      af[kk] = *(const bf16x8*)(base + aoff + kk * 8192);
#pragma unroll
      for (int ni = 0; ni < 5; ++ni)
        bf[kk][ni] = tr_frag(base + boff + kk * 10240 + ni * 32, base + boff + kk * 10240 + ni * 32 + 1280);
    }
#pragma unroll
    for (int kk = 0; kk < 2; ++kk)
#pragma unroll
      for (int ni = 0; ni < 5; ++ni) acc[ni] = __builtin_amdgcn_mfma_f32_16x16x32_bf16(bf[kk][ni], af[kk], acc[ni], 0, 0, 0);
  }
  STAMP(4, 6);
  f32x4* red = (f32x4*)lds + (wm * 5) * 64 + lane;
  if (wk == 1) {
#pragma unroll
    for (int ni = 0; ni < 5; ++ni) red[ni * 64] = acc[ni];
  }
  RAW_BAR();
  if (wk == 0) {
    const int prow = rb2 * 64 + wm * 16 + (lane & 15);
    const int mrow = (prow >> 3) + 256 * (prow & 7);
    const int ncol = cb2 * 80 + 4 * (lane >> 4);
#pragma unroll
    for (int ni = 0; ni < 5; ++ni) {
      const f32x4 o = acc[ni] + red[ni * 64];
      const u32x2 bb = *(const u32x2*)(lds + BIAS_OFF + (16 * ni + 4 * (lane >> 4)) * 2);
      const u32x2 rs = *(const u32x2*)(lds + RES_OFF + (wm * 16 + (lane & 15)) * 160 + (16 * ni + 4 * (lane >> 4)) * 2);
      f32x2 a01 = {o[0], o[1]}, a23 = {o[2], o[3]};
      u32x2 ov;
      ov.x = bf_pack(bf_unpack(rs.x) + bf_round(bf_round(a01) + bf_unpack(bb.x)));
      ov.y = bf_pack(bf_unpack(rs.y) + bf_round(bf_round(a23) + bf_unpack(bb.y)));
      *(u32x2*)(out + (size_t)mrow * D_ + ncol + 16 * ni) = ov;
    }
  }
  STAMP(4, 7);
  STAMP_END(4, 8);
}

extern "C" void kernel_launch(void* const* d_in, const int* in_sizes, int n_in, void* d_out, int out_size,
                              void* d_ws, size_t ws_size, hipStream_t stream) {
  const uint32_t* x = (const uint32_t*)d_in[0];
  const uint16_t* wr = (const uint16_t*)d_in[1];
  const uint16_t* wi = (const uint16_t*)d_in[2];
  const uint16_t* w1 = (const uint16_t*)d_in[4];
  const uint16_t* b1 = (const uint16_t*)d_in[5];
  const uint16_t* w2 = (const uint16_t*)d_in[6];
  const uint16_t* b2 = (const uint16_t*)d_in[7];
  char* ws = (char*)d_ws;
  size_t off = 0;
  f32x2* T = (f32x2*)(ws + off); off += (size_t)8 * 256 * TP * 8;
  uint32_t* h = (uint32_t*)(ws + off); off += (size_t)S_ * D_ * 2;
  uint32_t* hn = (uint32_t*)(ws + off); off += (size_t)S_ * D_ * 2;
  u32x4* h1 = (u32x4*)(ws + off); off += (size_t)S_ * F_ * 2;
  u32x4* w2p = (u32x4*)(ws + off); off += (size_t)D_ * F_ * 2;

  k_fwd_rows<<<256, 512, 0, stream>>>(x, T, wr, wi, (const uint32_t*)w1, (const u32x4*)w2, w2p);
  k_cols<<<328, 256, 0, stream>>>(T, wr, wi);
  k_inv_rows<<<256, 512, 0, stream>>>(T, x, h, hn);
  k_gemm1<<<256, 1024, 0, stream>>>((const char*)hn, (const char*)w1, b1, (u32x2*)h1);
  k_gemm2<<<256, 1024, 0, stream>>>(h1, w2p, (const char*)w2, b2, (const uint16_t*)h, (uint16_t*)d_out);
}
```

```cpp
#define PF_CODE 1
#include <hip/hip_runtime.h>
#include <hip/hip_bf16.h>
#include <stdint.h>

typedef float f32x2 __attribute__((ext_vector_type(2)));
typedef float f32x4 __attribute__((ext_vector_type(4)));
typedef short bf16x8 __attribute__((ext_vector_type(8)));
typedef unsigned int u32x4 __attribute__((ext_vector_type(4)));
typedef unsigned int u32x2 __attribute__((ext_vector_type(2)));

#define DEV __device__ __forceinline__

#define STAMP(kern, i)
#define STAMP_END(kern, i)
#define STAMP_TID 0

#define W2_MODE 1
#define WT_C
#define WT_D
#define PF_TLB 0
#define PF_W 1
static constexpr int S_ = 2048, D_ = 640, F_ = 1536, DF_ = 321;
static constexpr int TP = 328;

DEV float bf_lo(uint32_t p) { return __builtin_bit_cast(float, p << 16); }
DEV float bf_hi(uint32_t p) { return __builtin_bit_cast(float, p & 0xffff0000u); }
DEV f32x2 bf_unpack(uint32_t p) { f32x2 r; r.x = bf_lo(p); r.y = bf_hi(p); return r; }
typedef __bf16 bf16x2_t __attribute__((ext_vector_type(2)));
DEV uint32_t bf_pack(f32x2 v) {
  return __builtin_bit_cast(uint32_t, __builtin_convertvector(v, bf16x2_t));
}
DEV f32x2 bf_round(f32x2 v) { return bf_unpack(bf_pack(v)); }
DEV float bfr(float f) { f32x2 v; v.x = f; v.y = f; return bf_lo(bf_pack(v)); }

DEV f32x2 cadd_mi(f32x2 b, f32x2 d) {
  f32x2 r;
  asm("v_pk_add_f32 %0, %1, %2 op_sel:[0,1] op_sel_hi:[1,0] neg_hi:[0,1]" : "=v"(r) : "v"(b), "v"(d));
  return r;
}
DEV f32x2 cadd_pi(f32x2 b, f32x2 d) {
  f32x2 r;
  asm("v_pk_add_f32 %0, %1, %2 op_sel:[0,1] op_sel_hi:[1,0] neg_lo:[0,1]" : "=v"(r) : "v"(b), "v"(d));
  return r;
}
DEV f32x2 cnadd_mi(f32x2 b, f32x2 d) {
  f32x2 r;
  asm("v_pk_add_f32 %0, %1, %2 op_sel:[0,1] op_sel_hi:[1,0] neg_lo:[1,0] neg_hi:[1,1]" : "=v"(r) : "v"(b), "v"(d));
  return r;
}
DEV f32x2 cnadd_pi(f32x2 b, f32x2 d) {
  f32x2 r;
  asm("v_pk_add_f32 %0, %1, %2 op_sel:[0,1] op_sel_hi:[1,0] neg_lo:[1,1] neg_hi:[1,0]" : "=v"(r) : "v"(b), "v"(d));
  return r;
}
DEV f32x2 cfma_mi(f32x2 s, f32x2 d, f32x2 b) {
  f32x2 r;
  asm("v_pk_fma_f32 %0, %1, %2, %3 op_sel:[0,1,0] op_sel_hi:[1,0,1] neg_hi:[0,1,0]" : "=v"(r) : "v"(s), "v"(d), "v"(b));
  return r;
}
DEV f32x2 cfma_pi(f32x2 s, f32x2 d, f32x2 b) {
  f32x2 r;
  asm("v_pk_fma_f32 %0, %1, %2, %3 op_sel:[0,1,0] op_sel_hi:[1,0,1] neg_lo:[0,1,0]" : "=v"(r) : "v"(s), "v"(d), "v"(b));
  return r;
}
DEV f32x2 pfma(f32x2 a, f32x2 b, f32x2 c) { return __builtin_elementwise_fma(a, b, c); }
DEV f32x2 cmul(f32x2 a, f32x2 w) {
  f32x2 t, r;
  asm("v_pk_mul_f32 %0, %1, %2 op_sel:[0,0] op_sel_hi:[0,1]" : "=v"(t) : "v"(a), "v"(w));
  asm("v_pk_fma_f32 %0, %1, %2, %3 op_sel:[1,1,0] op_sel_hi:[1,0,1] neg_lo:[0,1,0]" : "=v"(r) : "v"(a), "v"(w), "v"(t));
  return r;
}
DEV f32x2 cmulc(f32x2 a, f32x2 w) {
  f32x2 t, r;
  asm("v_pk_mul_f32 %0, %1, %2 op_sel:[0,0] op_sel_hi:[0,1] neg_hi:[0,1]" : "=v"(t) : "v"(a), "v"(w));
  asm("v_pk_fma_f32 %0, %1, %2, %3 op_sel:[1,1,0] op_sel_hi:[1,0,1]" : "=v"(r) : "v"(a), "v"(w), "v"(t));
  return r;
}
DEV f32x2 twid(float f) {
  f32x2 r;
  r.x = __builtin_amdgcn_cosf(f);
  r.y = __builtin_amdgcn_sinf(-f);
  return r;
}
DEV void st_wt(f32x2* p, f32x2 v) {
  __hip_atomic_store((unsigned long long*)p, __builtin_bit_cast(unsigned long long, v), __ATOMIC_RELAXED,
                     __HIP_MEMORY_SCOPE_AGENT);
}
DEV void st_wt32(uint32_t* p, uint32_t v) { __hip_atomic_store(p, v, __ATOMIC_RELAXED, __HIP_MEMORY_SCOPE_AGENT); }
DEV void st_wt64(u32x2* p, u32x2 v) {
  __hip_atomic_store((unsigned long long*)p, __builtin_bit_cast(unsigned long long, v), __ATOMIC_RELAXED,
                     __HIP_MEMORY_SCOPE_AGENT);
}
DEV void st_wt128(void* p, u32x4 v) {
  asm volatile("global_store_dwordx4 %0, %1, off sc1\n\ts_nop 1" ::"v"(p), "v"(v) : "memory");
}
#define ST_H(p, v) st_wt32(p, v)
#define ST_H1(p, v) st_wt64(p, v)
template <int N> DEV void pin(f32x2 (&a)[N]) {
#pragma unroll
  for (int i = 0; i < N; ++i) asm volatile("" : "+v"(a[i]));
}
template <bool INV> DEV f32x2 add_rot(f32x2 b, f32x2 d) { return INV ? cadd_pi(b, d) : cadd_mi(b, d); }
template <bool INV> DEV f32x2 sub_rot(f32x2 b, f32x2 d) { return INV ? cadd_mi(b, d) : cadd_pi(b, d); }

template <bool INV> DEV void fft4(f32x2& a0, f32x2& a1, f32x2& a2, f32x2& a3) {
  f32x2 b0 = a0 + a2, b1 = a0 - a2, b2 = a1 + a3, d = a1 - a3;
  a0 = b0 + b2; a2 = b0 - b2;
  a1 = add_rot<INV>(b1, d); a3 = sub_rot<INV>(b1, d);
}
template <bool INV> DEV void fft8(f32x2 (&v)[8]) {
  const f32x2 r = {0.70710678118654752440f, 0.70710678118654752440f};
  const f32x2 nr = {-0.70710678118654752440f, -0.70710678118654752440f};
  f32x2 e0 = v[0] + v[4], e1 = v[1] + v[5], e2 = v[2] + v[6], e3 = v[3] + v[7];
  f32x2 o0 = v[0] - v[4], o1 = v[1] - v[5], o2 = v[2] - v[6], o3 = v[3] - v[7];
  fft4<INV>(e0, e1, e2, e3);
  f32x2 p1 = INV ? cadd_pi(o1, o1) : cadd_mi(o1, o1);
  f32x2 p3 = INV ? cnadd_pi(o3, o3) : cnadd_mi(o3, o3);
  f32x2 s = p1 + p3, d = p1 - p3;
  f32x2 b0 = add_rot<INV>(o0, o2), b1 = sub_rot<INV>(o0, o2);
  v[0] = e0; v[2] = e1; v[4] = e2; v[6] = e3;
  v[1] = pfma(r, s, b0); v[5] = pfma(nr, s, b0);
  v[3] = INV ? cfma_pi(r, d, b1) : cfma_mi(r, d, b1);
  v[7] = INV ? cfma_mi(r, d, b1) : cfma_pi(r, d, b1);
}
template <bool INV> DEV void fft5(f32x2 (&v)[5]) {
  const f32x2 c1 = {0.30901699437494742410f, 0.30901699437494742410f};
  const f32x2 c2 = {-0.80901699437494742410f, -0.80901699437494742410f};
  const f32x2 s1 = {0.95105651629515357212f, 0.95105651629515357212f};
  const f32x2 s2 = {0.58778525229247312917f, 0.58778525229247312917f};
  const f32x2 ns1 = {-0.95105651629515357212f, -0.95105651629515357212f};
  f32x2 t1 = v[1] + v[4], t2 = v[2] + v[3], t3 = v[1] - v[4], t4 = v[2] - v[3];
  f32x2 a1 = pfma(c2, t2, pfma(c1, t1, v[0]));
  f32x2 a2 = pfma(c1, t2, pfma(c2, t1, v[0]));
  f32x2 b1 = pfma(s2, t4, s1 * t3);
  f32x2 b2 = pfma(ns1, t4, s2 * t3);
  v[0] = v[0] + t1 + t2;
  v[1] = add_rot<INV>(a1, b1); v[4] = sub_rot<INV>(a1, b1);
  v[2] = add_rot<INV>(a2, b2); v[3] = sub_rot<INV>(a2, b2);
}

template <int CTRL> DEV float dppmov(float v) {
  return __builtin_bit_cast(float, __builtin_amdgcn_update_dpp(0, __builtin_bit_cast(int, v), CTRL, 0xf, 0xf, false));
}
DEV float wave_sum(float v) {
  v += dppmov<0xB1>(v);
  v += dppmov<0x4E>(v);
  v += dppmov<0x141>(v);
  v += dppmov<0x140>(v);
  int iv = __builtin_bit_cast(int, v);
  float a = __builtin_bit_cast(float, __builtin_amdgcn_readlane(iv, 0));
  float b = __builtin_bit_cast(float, __builtin_amdgcn_readlane(iv, 16));
  float c = __builtin_bit_cast(float, __builtin_amdgcn_readlane(iv, 32));
  float d = __builtin_bit_cast(float, __builtin_amdgcn_readlane(iv, 48));
  return (a + b) + (c + d);
}
DEV void wave_sync() {
  __builtin_amdgcn_fence(__ATOMIC_RELEASE, "wavefront");
  __builtin_amdgcn_wave_barrier();
  __builtin_amdgcn_fence(__ATOMIC_ACQUIRE, "wavefront");
}

DEV void row_layernorm(const f32x2 (&xv)[5], uint32_t (&zp)[5]) {
  f32x2 s2 = (xv[0] + xv[1]) + (xv[2] + xv[3]) + xv[4];
  const float mean = wave_sum(s2.x + s2.y) * (1.0f / 640.0f);
  const f32x2 nm = {-mean, -mean};
  f32x2 q2 = {0.f, 0.f};
#pragma unroll
  for (int i = 0; i < 5; ++i) { f32x2 d = xv[i] + nm; q2 = pfma(d, d, q2); }
  const float var = wave_sum(q2.x + q2.y) * (1.0f / 640.0f);
  f32x2 mv; mv.x = mean; mv.y = var;
  mv = bf_round(mv);
  const float den = bfr(sqrtf(bfr(mv.y + 1e-5f)));
  const float rden = 1.0f / den;
  const f32x2 nmb = {-mv.x, -mv.x}, rd = {rden, rden};
#pragma unroll
  for (int i = 0; i < 5; ++i) zp[i] = bf_pack(bf_round(xv[i] + nmb) * rd);
}

__global__ __launch_bounds__(512) void k_fwd_rows(const uint32_t* __restrict__ x, f32x2* __restrict__ T,
                                                  const uint16_t* __restrict__ wr, const uint16_t* __restrict__ wi,
                                                  const uint32_t* __restrict__ w1, const u32x4* __restrict__ w2,
                                                  u32x4* __restrict__ w2p) {
  __shared__ f32x2 ex[8][360];
  __shared__ f32x2 zz[8][320];
  const int tid = threadIdx.x, lane = tid & 63, j = tid >> 6;
  STAMP(0, 0);
  const int s0 = 32 * (blockIdx.x & 7) + (blockIdx.x >> 3);
  const int row = s0 + 256 * j;
  uint32_t xd[5];
#pragma unroll
  for (int i = 0; i < 5; ++i) xd[i] = x[row * 320 + lane + 64 * i];
  uint32_t pf = 0;
  if (lane < 12) {
    const int t = (blockIdx.x & 7) + 8 * (8 * (blockIdx.x >> 3) + j);
    pf = ((lane < 6) ? wr : wi)[t * DF_ + min(64 * (lane % 6), DF_ - 1)];
  } else if (PF_W && lane >= 56) {
    const int l = j * 8 + (lane - 56);
    pf = w1[(blockIdx.x * 60 + min(l, 59)) * 32];
  }
  uint32_t pfc = 0;
  {
    unsigned long long c0, c1;
    asm volatile("s_getpc_b64 s[40:41]\n\ts_add_u32 s40, s40, _Z6k_colsPDv2_fPKtS2_@rel32@lo+4\n\ts_addc_u32 s41, s41, _Z6k_colsPDv2_fPKtS2_@rel32@hi+12\n\ts_mov_b64 %0, s[40:41]" : "=s"(c0) :: "s40", "s41", "scc");
    asm volatile("s_getpc_b64 s[40:41]\n\ts_add_u32 s40, s40, _Z8k_zz_endv@rel32@lo+4\n\ts_addc_u32 s41, s41, _Z8k_zz_endv@rel32@hi+12\n\ts_mov_b64 %0, s[40:41]" : "=s"(c1) :: "s40", "s41", "scc");
    const long long len = (long long)(c1 - c0);
    const int li = (blockIdx.x >> 3) * 16 + (j * 2 + (lane - 16 >= 16 ? 1 : 0));
    if (len > 0 && len < 65536 && (lane == 16 || lane == 32) && li * 128 < len) pfc = *(const uint32_t*)(c0 + li * 128);
  }
  const int wid = min((int)blockIdx.x * 512 + tid, 122879);
  const u32x4 wchunk = w2[wid];

  f32x2 tw1[4], tw2[7], twq[7];
#pragma unroll
  for (int r = 1; r < 5; ++r) tw1[r - 1] = twid((float)(lane * r) * (1.0f / 320.0f));
  const int l0 = lane & 7, rr = lane >> 3;
#pragma unroll
  for (int q = 1; q < 8; ++q) tw2[q - 1] = twid((float)(l0 * q) * (1.0f / 64.0f));
#pragma unroll
  for (int q = 1; q < 8; ++q) twq[q - 1] = twid((float)(s0 * q) * (1.0f / 2048.0f));
  f32x2 twk_[1] = {twid((float)tid * (1.0f / 640.0f))};
  pin(tw1); pin(tw2); pin(twq); pin(twk_);
  const f32x2 twk = twk_[0];
  __builtin_amdgcn_sched_barrier(0);
  STAMP(0, 1);

  f32x2 u[5];
  {
    f32x2 xv[5];
    uint32_t zp[5];
#pragma unroll
    for (int i = 0; i < 5; ++i) xv[i] = bf_unpack(xd[i]);
    row_layernorm(xv, zp);
#pragma unroll
    for (int i = 0; i < 5; ++i) u[i] = bf_unpack(zp[i]);
  }
  STAMP(0, 2);

  f32x2* E = ex[j];
  fft5<false>(u);
  E[lane] = u[0];
#pragma unroll
  for (int r = 1; r < 5; ++r) E[r * 72 + lane] = cmul(u[r], tw1[r - 1]);
  wave_sync();
  if (lane < 40) {
    f32x2 v[8];
    f32x2* Er = E + rr * 72;
#pragma unroll
    for (int l1 = 0; l1 < 8; ++l1) v[l1] = Er[l0 + 8 * l1];
    fft8<false>(v);
    wave_sync();
    Er[l0] = v[0];
#pragma unroll
    for (int q = 1; q < 8; ++q) Er[q * 9 + l0] = cmul(v[q], tw2[q - 1]);
    wave_sync();
#pragma unroll
    for (int a = 0; a < 8; ++a) v[a] = Er[l0 * 9 + a];
    fft8<false>(v);
    f32x2* Z = zz[j] + rr + 5 * l0;
#pragma unroll
    for (int p = 0; p < 8; ++p) Z[40 * p] = v[p];
  }
  if (blockIdx.x < 240) {
    const int wk_ = wid / 80, wc_ = wid % 80;
    const int rho = (wk_ & ~12) | ((wk_ & 4) << 1) | ((wk_ & 8) >> 1);
    st_wt128(w2p + ((wc_ / 10) * 1536 + rho) * 10 + (wc_ % 10), wchunk);
  }
  STAMP(0, 3);
  __syncthreads();
  STAMP(0, 4);
  if (j < 6) {
    const int k = tid;
    const int ka = (k >= 320) ? 0 : k, kb = (k == 0 || k >= 320) ? 0 : 320 - k;
    f32x2 v[8];
#pragma unroll
    for (int jj = 0; jj < 8; ++jj) {
      const f32x2 a = zz[jj][ka], b = zz[jj][kb];
      f32x2 e, d;
      e.x = a.x + b.x; e.y = a.y - b.y;
      d.x = a.x - b.x; d.y = a.y + b.y;
      v[jj] = cadd_mi(e, cmul(d, twk));
    }
    fft8<false>(v);
    if (k < DF_) {
      f32x2* dst = T + (size_t)s0 * TP;
      st_wt(dst + k, v[0]);
#pragma unroll
      for (int q = 1; q < 8; ++q) st_wt(dst + (size_t)q * 256 * TP + k, cmul(v[q], twq[q - 1]));
    }
  }
  asm volatile("" ::"v"(pf));
  asm volatile("" ::"v"(pfc));
  STAMP(0, 5);
  STAMP_END(0, 6);
}

__global__ __launch_bounds__(256) void k_cols(f32x2* __restrict__ T, const uint16_t* __restrict__ wr,
                                              const uint16_t* __restrict__ wi) {
  __shared__ f32x2 e1[2048];
  __shared__ f32x2 e2[4][576];
  __shared__ __attribute__((aligned(16))) uint32_t wl[2048];
  const int tid = threadIdx.x, lane = tid & 63, wv = tid >> 6;
  STAMP(1, 0);
  const int q = blockIdx.x & 7, kg = blockIdx.x >> 3;
  const int c = tid & 7, a = tid >> 3;
  const int k = kg * 8 + c;
  const bool valid = k < DF_;
  f32x2* colbase = T + (size_t)(q * 256) * TP;
  const uint32_t coff = a * TP + (valid ? k : DF_ - 1);
  f32x2 v[8];
#pragma unroll
  for (int j1 = 0; j1 < 8; ++j1) v[j1] = (colbase + (size_t)j1 * 32 * TP)[coff];
  const int kb = min(8 * kg, DF_ - 8);
  u32x4 wre, wim;
  __builtin_memcpy(&wre, wr + (size_t)(q + 8 * tid) * DF_ + kb, 16);
  __builtin_memcpy(&wim, wi + (size_t)(q + 8 * tid) * DF_ + kb, 16);

  const int b = (lane >> 3) & 3, p1lo = lane >> 5, p1 = 2 * wv + p1lo;
  f32x2 twa[7], twb[7];
#pragma unroll
  for (int p = 1; p < 8; ++p) twa[p - 1] = twid((float)(a * p) * (1.0f / 256.0f));
#pragma unroll
  for (int p = 1; p < 8; ++p) twb[p - 1] = twid((float)(b * p) * (1.0f / 32.0f));
  pin(twa); pin(twb);
  __builtin_amdgcn_sched_barrier(0);
  STAMP(1, 1);

  fft8<false>(v);
  e1[tid] = v[0];
#pragma unroll
  for (int p = 1; p < 8; ++p) e1[p * 256 + tid] = cmul(v[p], twa[p - 1]);
  {
    u32x4 o0, o1;
    o0.x = __builtin_amdgcn_perm(wim.x, wre.x, 0x05040100); o0.y = __builtin_amdgcn_perm(wim.x, wre.x, 0x07060302);
    o0.z = __builtin_amdgcn_perm(wim.y, wre.y, 0x05040100); o0.w = __builtin_amdgcn_perm(wim.y, wre.y, 0x07060302);
    o1.x = __builtin_amdgcn_perm(wim.z, wre.z, 0x05040100); o1.y = __builtin_amdgcn_perm(wim.z, wre.z, 0x07060302);
    o1.z = __builtin_amdgcn_perm(wim.w, wre.w, 0x05040100); o1.w = __builtin_amdgcn_perm(wim.w, wre.w, 0x07060302);
    *(u32x4*)(wl + tid * 8) = o0;
    *(u32x4*)(wl + tid * 8 + 4) = o1;
  }
  STAMP(1, 2);
  __syncthreads();
  STAMP(1, 3);
  uint32_t wf[8];
  {
    const uint32_t* wlp = wl + (p1 + 16 * b) * 8 + min(8 * kg + c - kb, 7);
#pragma unroll
    for (int e = 0; e < 2; ++e)
#pragma unroll
      for (int mm = 0; mm < 4; ++mm) wf[e * 4 + mm] = wlp[(8 * e + 64 * mm) * 8];
  }
  f32x2* E1 = e1 + p1 * 256 + (lane & 31);
#pragma unroll
  for (int j2 = 0; j2 < 8; ++j2) v[j2] = E1[32 * j2];
  fft8<false>(v);
  f32x2* E2w = e2[wv] + p1lo * 288 + (lane & 31);
  E2w[0] = v[0];
#pragma unroll
  for (int p = 1; p < 8; ++p) E2w[p * 36] = cmul(v[p], twb[p - 1]);
  wave_sync();
  f32x2* E2r = e2[wv] + p1lo * 288 + (2 * b) * 36 + c;
#pragma unroll
  for (int e = 0; e < 2; ++e)
#pragma unroll
    for (int bb = 0; bb < 4; ++bb) v[e * 4 + bb] = E2r[e * 36 + bb * 8];
  fft4<false>(v[0], v[1], v[2], v[3]);
  fft4<false>(v[4], v[5], v[6], v[7]);
  STAMP(1, 4);
#pragma unroll
  for (int i = 0; i < 8; ++i) v[i] = cmul(v[i], bf_unpack(wf[i]));
  fft4<true>(v[0], v[1], v[2], v[3]);
  fft4<true>(v[4], v[5], v[6], v[7]);
  STAMP(1, 5);
#pragma unroll
  for (int e = 0; e < 2; ++e)
#pragma unroll
    for (int bb = 0; bb < 4; ++bb) E2r[e * 36 + bb * 8] = v[e * 4 + bb];
  wave_sync();
  v[0] = E2w[0];
#pragma unroll
  for (int p = 1; p < 8; ++p) v[p] = cmulc(E2w[p * 36], twb[p - 1]);
  fft8<true>(v);
#pragma unroll
  for (int j2 = 0; j2 < 8; ++j2) E1[32 * j2] = v[j2];
  STAMP(1, 6);
  __syncthreads();
  STAMP(1, 7);
  v[0] = e1[tid];
#pragma unroll
  for (int p = 1; p < 8; ++p) v[p] = cmulc(e1[p * 256 + tid], twa[p - 1]);
  fft8<true>(v);
  if (valid) {
#pragma unroll
    for (int j1 = 0; j1 < 8; ++j1) st_wt(colbase + (size_t)j1 * 32 * TP + coff, v[j1]);
  }
  STAMP(1, 8);
  STAMP_END(1, 9);
}

__global__ __launch_bounds__(512) void k_inv_rows(const f32x2* __restrict__ T, const uint32_t* __restrict__ x,
                                                  uint32_t* __restrict__ h, uint32_t* __restrict__ hn) {
  __shared__ f32x2 ex[8][360];
  __shared__ f32x2 xx[8][384];
  const int tid = threadIdx.x, lane = tid & 63, j = tid >> 6;
  STAMP(2, 0);
  const int s0 = 32 * (blockIdx.x & 7) + (blockIdx.x >> 3);
  const int row = s0 + 256 * j;
  const int prow = s0 * 8 + j;
  f32x2 v[8];
  {
    const f32x2* src = T + (size_t)s0 * TP;
    const uint32_t kk = min(tid, DF_ - 1);
#pragma unroll
    for (int q = 0; q < 8; ++q) v[q] = (src + (size_t)q * 256 * TP)[kk];
  }
  uint32_t xd[5];
#pragma unroll
  for (int i = 0; i < 5; ++i) xd[i] = x[row * 320 + lane + 64 * i];

  f32x2 tw1[4], tw2[7], twq[7], twk[8];
#pragma unroll
  for (int r = 1; r < 5; ++r) tw1[r - 1] = twid((float)(lane * r) * (1.0f / 320.0f));
  const int l0 = lane & 7, rr = lane >> 3;
#pragma unroll
  for (int q = 1; q < 8; ++q) tw2[q - 1] = twid((float)(l0 * q) * (1.0f / 64.0f));
#pragma unroll
  for (int q = 1; q < 8; ++q) twq[q - 1] = twid((float)(s0 * q) * (1.0f / 2048.0f));
#pragma unroll
  for (int p = 0; p < 8; ++p) twk[p] = twid((float)(rr + 5 * l0 + 40 * p) * (1.0f / 640.0f));
  pin(tw1); pin(tw2); pin(twq); pin(twk);
  __builtin_amdgcn_sched_barrier(0);
  STAMP(2, 1);

  if (j < 6) {
#pragma unroll
    for (int q = 1; q < 8; ++q) v[q] = cmulc(v[q], twq[q - 1]);
    fft8<true>(v);
    f32x2 msk;
    msk.x = 1.0f; msk.y = (tid == 0 || tid == 320) ? 0.0f : 1.0f;
#pragma unroll
    for (int jj = 0; jj < 8; ++jj) xx[jj][tid] = v[jj] * msk;
  }
  STAMP(2, 2);
  __syncthreads();
  STAMP(2, 3);
  f32x2* E = ex[j];
  if (lane < 40) {
    f32x2* Er = E + rr * 72;
    const f32x2* Xa = xx[j] + rr + 5 * l0;
    const f32x2* Xb = xx[j] + 320 - rr - 5 * l0;
#pragma unroll
    for (int p = 0; p < 8; ++p) {
      const f32x2 a = Xa[40 * p], b = Xb[-40 * p];
      f32x2 e, d;
      e.x = a.x + b.x; e.y = a.y - b.y;
      d.x = a.x - b.x; d.y = a.y + b.y;
      v[p] = cadd_pi(e, cmulc(d, twk[p]));
    }
    fft8<true>(v);
    Er[l0 * 9] = v[0];
#pragma unroll
    for (int a = 1; a < 8; ++a) Er[l0 * 9 + a] = cmulc(v[a], tw2[a - 1]);
    wave_sync();
#pragma unroll
    for (int q = 0; q < 8; ++q) v[q] = Er[q * 9 + l0];
    fft8<true>(v);
    wave_sync();
#pragma unroll
    for (int l1 = 0; l1 < 8; ++l1) Er[l0 + 8 * l1] = v[l1];
  }
  wave_sync();
  f32x2 u[5];
  u[0] = E[lane];
#pragma unroll
  for (int r = 1; r < 5; ++r) u[r] = cmulc(E[r * 72 + lane], tw1[r - 1]);
  fft5<true>(u);
  STAMP(2, 4);

  const f32x2 sc = {1.0f / 2621440.0f, 1.0f / 2621440.0f};
  f32x2 hv[5];
  uint32_t zp[5];
#pragma unroll
  for (int i = 0; i < 5; ++i) {
    const uint32_t hp = bf_pack(bf_unpack(xd[i]) + bf_round(u[i] * sc));
    ST_H(h + prow * 320 + lane + 64 * i, hp);
    hv[i] = bf_unpack(hp);
  }
  row_layernorm(hv, zp);
  STAMP(2, 5);
#pragma unroll
  for (int i = 0; i < 5; ++i) ST_H(hn + prow * 320 + lane + 64 * i, zp[i]);
  STAMP(2, 6);
  STAMP_END(2, 7);
}

template <int N> DEV void wait_vm() { asm volatile("s_waitcnt vmcnt(%0)" ::"n"(N) : "memory"); }
#define RAW_BAR() do { asm volatile("s_waitcnt lgkmcnt(0)" ::: "memory"); __builtin_amdgcn_s_barrier(); } while (0)

DEV void glds16(const void* src, char* lds) {
  __builtin_amdgcn_global_load_lds((const unsigned*)src, (unsigned*)lds, 16, 0, 0);
}
DEV uint32_t pk_relu_bf16(uint32_t p) {
  uint32_t r;
  asm("v_pk_max_i16 %0, %1, 0" : "=v"(r) : "v"(p));
  return r;
}

typedef short v4s_t __attribute__((ext_vector_type(4)));
typedef __attribute__((address_space(3))) v4s_t* lds_v4s_ptr;
DEV bf16x8 tr_frag(const char* p0, const char* p1) {
  const v4s_t a = __builtin_amdgcn_ds_read_tr16_b64_v4i16((lds_v4s_ptr)p0);
  const v4s_t b = __builtin_amdgcn_ds_read_tr16_b64_v4i16((lds_v4s_ptr)p1);
  return __builtin_shufflevector(a, b, 0, 1, 2, 3, 4, 5, 6, 7);
}

#undef STAMP_TID
#define STAMP_TID 512
__global__ __launch_bounds__(1024) void k_gemm1(const char* __restrict__ hn, const char* __restrict__ w1,
                                                const uint16_t* __restrict__ b1, u32x2* __restrict__ h1) {
  constexpr int NST = 10, NSLOT = 5, SB = 28 * 1024;
  __shared__ __attribute__((aligned(16))) char lds[NSLOT * SB + 256];
  const int tid = threadIdx.x, lane = tid & 63;
  const int wv = __builtin_amdgcn_readfirstlane(tid >> 6);
  const int xcd = blockIdx.x & 7, idx = blockIdx.x >> 3;
  const int rb = 2 * xcd + (idx >> 4), cb = idx & 15;
  if (wv < 8) {
    const int lw = wv;
    const int arow = lane >> 3;
    const char* asrc = hn + (size_t)(rb * 128 + 16 * lw + arow) * 1280 + (((lane & 7) ^ arow) << 4);
    const int id0 = lw * 64 + lane, r0 = id0 / 12, c0 = (id0 % 12) ^ (((r0 >> 3) & 1) << 1);
    const int id1 = (8 + lw) * 64 + lane, r1 = id1 / 12, c1 = (id1 % 12) ^ (((r1 >> 3) & 1) << 1);
    const char* bsrc0 = w1 + (size_t)r0 * 3072 + cb * 192 + c0 * 16;
    const char* bsrc1 = w1 + (size_t)r1 * 3072 + cb * 192 + c1 * 16;
    auto stage = [&](int st) {
      char* dst = lds + (st % NSLOT) * SB + lane * 16;
      glds16(asrc + st * 128, dst + (2 * lw) * 1024);
      glds16(asrc + st * 128 + 8 * 1280, dst + (2 * lw + 1) * 1024);
      glds16(bsrc0 + (size_t)st * 64 * 3072, dst + (16 + lw) * 1024);
      if (lw < 4) glds16(bsrc1 + (size_t)st * 64 * 3072, dst + (24 + lw) * 1024);
    };
    if (lw == 7 && lane < 12) glds16((const u32x4*)(b1 + cb * 96) + lane, lds + NSLOT * SB + lane * 16);
#pragma unroll
    for (int st = 0; st < NSLOT - 1; ++st) stage(st);
#pragma unroll
    for (int st = 0; st < NST; ++st) {
      constexpr int LAST = NST - 1;
      const int inflight = ((st + NSLOT - 2 < LAST) ? (st + NSLOT - 2) : LAST) - st;
      if (lw < 4) {
        if (inflight == 3) wait_vm<12>(); else if (inflight == 2) wait_vm<8>(); else if (inflight == 1) wait_vm<4>(); else wait_vm<0>();
      } else {
        if (inflight == 3) wait_vm<9>(); else if (inflight == 2) wait_vm<6>(); else if (inflight == 1) wait_vm<3>(); else wait_vm<0>();
      }
      __builtin_amdgcn_s_barrier();
      if (st + NSLOT - 1 < NST) stage(st + NSLOT - 1);
    }
    return;
  }
  STAMP(3, 0);
  const int wm = (wv - 8) >> 1, wn = wv & 1;
  f32x4 acc[2][3];
#pragma unroll
  for (int mi = 0; mi < 2; ++mi)
#pragma unroll
    for (int ni = 0; ni < 3; ++ni) acc[mi][ni] = (f32x4){0.f, 0.f, 0.f, 0.f};
  const int ar = 32 * wm + (lane & 15);
  const int aoff0 = ar * 128 + (((lane >> 4) ^ (ar & 7)) << 4);
  const int aoff1 = ar * 128 + (((4 + (lane >> 4)) ^ (ar & 7)) << 4);
  const int g = lane >> 4, q4 = (lane & 15) >> 2, p4 = lane & 3;
  int boff[3];
#pragma unroll
  for (int ni = 0; ni < 3; ++ni) boff[ni] = 16384 + (8 * g + q4) * 192 + p4 * 8 + (((3 * wn + ni) ^ (g & 1)) << 5);

#pragma unroll
  for (int st = 0; st < NST; ++st) {
    RAW_BAR();
    int slot_off = (st % NSLOT) * SB;
    asm volatile("" : "+s"(slot_off));
    const char* base = lds + slot_off;
    bf16x8 af[2][2], bf[2][3];
#pragma unroll
    for (int ks = 0; ks < 2; ++ks) {
#pragma unroll
      for (int mi = 0; mi < 2; ++mi) af[ks][mi] = *(const bf16x8*)(base + (ks ? aoff1 : aoff0) + mi * 2048);
#pragma unroll
      for (int ni = 0; ni < 3; ++ni) bf[ks][ni] = tr_frag(base + boff[ni] + ks * 6144, base + boff[ni] + ks * 6144 + 768);
    }
#pragma unroll
    for (int ks = 0; ks < 2; ++ks)
#pragma unroll
      for (int mi = 0; mi < 2; ++mi)
#pragma unroll
        for (int ni = 0; ni < 3; ++ni)
          acc[mi][ni] = __builtin_amdgcn_mfma_f32_16x16x32_bf16(bf[ks][ni], af[ks][mi], acc[mi][ni], 0, 0, 0);
  }
  STAMP(3, 6);
#pragma unroll
  for (int ni = 0; ni < 3; ++ni) {
    const u32x2 bb = *(const u32x2*)(lds + NSLOT * SB + ((3 * wn + ni) * 16 + 4 * (lane >> 4)) * 2);
    const f32x2 b01 = bf_unpack(bb.x), b23 = bf_unpack(bb.y);
    const int NT = cb * 6 + 3 * wn + ni;
    const int ks2 = NT >> 1, gg = 2 * (NT & 1) + (lane >> 5), half = (lane >> 4) & 1;
#pragma unroll
    for (int mi = 0; mi < 2; ++mi) {
      f32x2 a01 = {acc[mi][ni][0], acc[mi][ni][1]}, a23 = {acc[mi][ni][2], acc[mi][ni][3]};
      a01 = bf_unpack(pk_relu_bf16(bf_pack(bf_round(a01) + b01)));
      a23 = bf_unpack(pk_relu_bf16(bf_pack(bf_round(a23) + b23)));
      u32x2 ov;
      ov.x = bf_pack(a01 * a01); ov.y = bf_pack(a23 * a23);
      const int mt = rb * 8 + 2 * wm + mi;
      ST_H1(h1 + ((((mt >> 2) * 48 + ks2) * 4 + (mt & 3)) * 64 + (lane & 15) + 16 * gg) * 2 + half, ov);
    }
  }
  STAMP(3, 7);
  STAMP_END(3, 8);
}

__global__ __launch_bounds__(1024) void k_gemm2(const u32x4* __restrict__ h1, const u32x4* __restrict__ w2p,
                                                const char* __restrict__ w2,
                                                const uint16_t* __restrict__ b2, const uint16_t* __restrict__ h,
                                                uint16_t* __restrict__ out) {
  constexpr int NST = 12, NSLOT = 4, SB = 36 * 1024;
  constexpr int RES_OFF = NSLOT * SB, BIAS_OFF = RES_OFF + 10240;
  __shared__ __attribute__((aligned(16))) char lds[BIAS_OFF + 256];
  const int tid = threadIdx.x, lane = tid & 63;
  const int wv = __builtin_amdgcn_readfirstlane(tid >> 6);
  const int xcd = blockIdx.x & 7, idx = blockIdx.x >> 3;
  const int rb2 = 4 * xcd + (idx >> 3), cb2 = idx & 7;
  if (wv < 8) {
    const int lw = wv;
    const u32x4* asrc = h1 + (size_t)rb2 * 48 * 4 * 64 + (2 * lw) * 64 + lane;
    const u32x4* bsrc = w2p + (size_t)cb2 * 1536 * 10 + lw * 64 + lane;
    auto stage = [&](int st) {
      char* dst = lds + (st % NSLOT) * SB + lane * 16;
      glds16(asrc + (size_t)(st * 16) * 64, dst + (2 * lw) * 1024);
      glds16(asrc + (size_t)(st * 16 + 1) * 64, dst + (2 * lw + 1) * 1024);
      glds16(bsrc + st * 1280, dst + (16 + lw) * 1024);
      glds16(bsrc + st * 1280 + 512, dst + (24 + lw) * 1024);
      if (lw < 4) glds16(bsrc + st * 1280 + 1024, dst + (32 + lw) * 1024);
    };
    stage(0);
    if (lw == 7 && lane < 10) glds16((const u32x4*)(b2 + cb2 * 80) + lane, lds + BIAS_OFF + lane * 16);
    {
      const int id = lw * 64 + lane, r = id / 10, ch = id % 10;
      glds16((const u32x4*)(h + (size_t)(rb2 * 64 + r) * D_ + cb2 * 80) + ch, lds + RES_OFF + id * 16);
      if (lw >= 6) {
        const int id2 = (lw + 2) * 64 + lane, r2 = id2 / 10, ch2 = id2 % 10;
        glds16((const u32x4*)(h + (size_t)(rb2 * 64 + r2) * D_ + cb2 * 80) + ch2, lds + RES_OFF + id2 * 16);
      }
    }
#pragma unroll
    for (int st = 1; st < NSLOT - 1; ++st) stage(st);
#pragma unroll
    for (int st = 0; st < NST; ++st) {
      constexpr int LAST = NST - 1;
      const int inflight = ((st + NSLOT - 2 < LAST) ? (st + NSLOT - 2) : LAST) - st;
      if (lw < 4) {
        if (inflight == 2) wait_vm<10>(); else if (inflight == 1) wait_vm<5>(); else wait_vm<0>();
      } else {
        if (inflight == 2) wait_vm<8>(); else if (inflight == 1) wait_vm<4>(); else wait_vm<0>();
      }
      __builtin_amdgcn_s_barrier();
      if (st + NSLOT - 1 < NST) stage(st + NSLOT - 1);
    }
    __builtin_amdgcn_s_barrier();
    return;
  }
  STAMP(4, 0);
  const int wm = wv & 3, wk = (wv >> 2) & 1;
  f32x4 acc[5];
#pragma unroll
  for (int ni = 0; ni < 5; ++ni) acc[ni] = (f32x4){0.f, 0.f, 0.f, 0.f};
  const int g = lane >> 4, q4 = (lane & 15) >> 2, p4 = lane & 3;
  const int boff = 16384 + (16 * (g >> 1) + 4 * (g & 1) + q4 + 32 * wk) * 160 + p4 * 8;
  const int aoff = (wk * 4 + wm) * 1024 + lane * 16;

#pragma unroll
  for (int st = 0; st < NST; ++st) {
    RAW_BAR();
    int slot_off = (st % NSLOT) * SB;
    asm volatile("" : "+s"(slot_off));
    const char* base = lds + slot_off;
    bf16x8 af[2], bf[2][5];
#pragma unroll
    for (int kk = 0; kk < 2; ++kk) {
      af[kk] = *(const bf16x8*)(base + aoff + kk * 8192);
#pragma unroll
      for (int ni = 0; ni < 5; ++ni)
        bf[kk][ni] = tr_frag(base + boff + kk * 10240 + ni * 32, base + boff + kk * 10240 + ni * 32 + 1280);
    }
#pragma unroll
    for (int kk = 0; kk < 2; ++kk)
#pragma unroll
      for (int ni = 0; ni < 5; ++ni) acc[ni] = __builtin_amdgcn_mfma_f32_16x16x32_bf16(bf[kk][ni], af[kk], acc[ni], 0, 0, 0);
  }
  STAMP(4, 6);
  f32x4* red = (f32x4*)lds + (wm * 5) * 64 + lane;
  if (wk == 1) {
#pragma unroll
    for (int ni = 0; ni < 5; ++ni) red[ni * 64] = acc[ni];
  }
  RAW_BAR();
  if (wk == 0) {
    const int prow = rb2 * 64 + wm * 16 + (lane & 15);
    const int mrow = (prow >> 3) + 256 * (prow & 7);
    const int ncol = cb2 * 80 + 4 * (lane >> 4);
#pragma unroll
    for (int ni = 0; ni < 5; ++ni) {
      const f32x4 o = acc[ni] + red[ni * 64];
      const u32x2 bb = *(const u32x2*)(lds + BIAS_OFF + (16 * ni + 4 * (lane >> 4)) * 2);
      const u32x2 rs = *(const u32x2*)(lds + RES_OFF + (wm * 16 + (lane & 15)) * 160 + (16 * ni + 4 * (lane >> 4)) * 2);
      f32x2 a01 = {o[0], o[1]}, a23 = {o[2], o[3]};
      u32x2 ov;
      ov.x = bf_pack(bf_unpack(rs.x) + bf_round(bf_round(a01) + bf_unpack(bb.x)));
      ov.y = bf_pack(bf_unpack(rs.y) + bf_round(bf_round(a23) + bf_unpack(bb.y)));
      *(u32x2*)(out + (size_t)mrow * D_ + ncol + 16 * ni) = ov;
    }
  }
  STAMP(4, 7);
  STAMP_END(4, 8);
}

__global__ void k_zz_end() {}
extern "C" void kernel_launch(void* const* d_in, const int* in_sizes, int n_in, void* d_out, int out_size,
                              void* d_ws, size_t ws_size, hipStream_t stream) {
  const uint32_t* x = (const uint32_t*)d_in[0];
  const uint16_t* wr = (const uint16_t*)d_in[1];
  const uint16_t* wi = (const uint16_t*)d_in[2];
  const uint16_t* w1 = (const uint16_t*)d_in[4];
  const uint16_t* b1 = (const uint16_t*)d_in[5];
  const uint16_t* w2 = (const uint16_t*)d_in[6];
  const uint16_t* b2 = (const uint16_t*)d_in[7];
  char* ws = (char*)d_ws;
  size_t off = 0;
  f32x2* T = (f32x2*)(ws + off); off += (size_t)8 * 256 * TP * 8;
  uint32_t* h = (uint32_t*)(ws + off); off += (size_t)S_ * D_ * 2;
  uint32_t* hn = (uint32_t*)(ws + off); off += (size_t)S_ * D_ * 2;
  u32x4* h1 = (u32x4*)(ws + off); off += (size_t)S_ * F_ * 2;
  u32x4* w2p = (u32x4*)(ws + off); off += (size_t)D_ * F_ * 2;

  k_fwd_rows<<<256, 512, 0, stream>>>(x, T, wr, wi, (const uint32_t*)w1, (const u32x4*)w2, w2p);
  k_cols<<<328, 256, 0, stream>>>(T, wr, wi);
  k_inv_rows<<<256, 512, 0, stream>>>(T, x, h, hn);
  k_gemm1<<<256, 1024, 0, stream>>>((const char*)hn, (const char*)w1, b1, (u32x2*)h1);
  k_gemm2<<<256, 1024, 0, stream>>>(h1, w2p, (const char*)w2, b2, (const uint16_t*)h, (uint16_t*)d_out);
}
```

```cpp
#define MAP_E 1
#include <hip/hip_runtime.h>
#include <hip/hip_bf16.h>
#include <stdint.h>

typedef float f32x2 __attribute__((ext_vector_type(2)));
typedef float f32x4 __attribute__((ext_vector_type(4)));
typedef short bf16x8 __attribute__((ext_vector_type(8)));
typedef unsigned int u32x4 __attribute__((ext_vector_type(4)));
typedef unsigned int u32x2 __attribute__((ext_vector_type(2)));

#define DEV __device__ __forceinline__

#define STAMP(kern, i)
#define STAMP_END(kern, i)
#define STAMP_TID 0

#define W2_MODE 1
#define WT_C
#define WT_D
#define PF_CODE 1
#define MAP_D 0
#define PF_TLB 0
#define PF_W 1
static constexpr int S_ = 2048, D_ = 640, F_ = 1536, DF_ = 321;
static constexpr int TP = 328;

DEV float bf_lo(uint32_t p) { return __builtin_bit_cast(float, p << 16); }
DEV float bf_hi(uint32_t p) { return __builtin_bit_cast(float, p & 0xffff0000u); }
DEV f32x2 bf_unpack(uint32_t p) { f32x2 r; r.x = bf_lo(p); r.y = bf_hi(p); return r; }
typedef __bf16 bf16x2_t __attribute__((ext_vector_type(2)));
DEV uint32_t bf_pack(f32x2 v) {
  return __builtin_bit_cast(uint32_t, __builtin_convertvector(v, bf16x2_t));
}
DEV f32x2 bf_round(f32x2 v) { return bf_unpack(bf_pack(v)); }
DEV float bfr(float f) { f32x2 v; v.x = f; v.y = f; return bf_lo(bf_pack(v)); }

DEV f32x2 cadd_mi(f32x2 b, f32x2 d) {
  f32x2 r;
  asm("v_pk_add_f32 %0, %1, %2 op_sel:[0,1] op_sel_hi:[1,0] neg_hi:[0,1]" : "=v"(r) : "v"(b), "v"(d));
  return r;
}
DEV f32x2 cadd_pi(f32x2 b, f32x2 d) {
  f32x2 r;
  asm("v_pk_add_f32 %0, %1, %2 op_sel:[0,1] op_sel_hi:[1,0] neg_lo:[0,1]" : "=v"(r) : "v"(b), "v"(d));
  return r;
}
DEV f32x2 cnadd_mi(f32x2 b, f32x2 d) {
  f32x2 r;
  asm("v_pk_add_f32 %0, %1, %2 op_sel:[0,1] op_sel_hi:[1,0] neg_lo:[1,0] neg_hi:[1,1]" : "=v"(r) : "v"(b), "v"(d));
  return r;
}
DEV f32x2 cnadd_pi(f32x2 b, f32x2 d) {
  f32x2 r;
  asm("v_pk_add_f32 %0, %1, %2 op_sel:[0,1] op_sel_hi:[1,0] neg_lo:[1,1] neg_hi:[1,0]" : "=v"(r) : "v"(b), "v"(d));
  return r;
}
DEV f32x2 cfma_mi(f32x2 s, f32x2 d, f32x2 b) {
  f32x2 r;
  asm("v_pk_fma_f32 %0, %1, %2, %3 op_sel:[0,1,0] op_sel_hi:[1,0,1] neg_hi:[0,1,0]" : "=v"(r) : "v"(s), "v"(d), "v"(b));
  return r;
}
DEV f32x2 cfma_pi(f32x2 s, f32x2 d, f32x2 b) {
  f32x2 r;
  asm("v_pk_fma_f32 %0, %1, %2, %3 op_sel:[0,1,0] op_sel_hi:[1,0,1] neg_lo:[0,1,0]" : "=v"(r) : "v"(s), "v"(d), "v"(b));
  return r;
}
DEV f32x2 pfma(f32x2 a, f32x2 b, f32x2 c) { return __builtin_elementwise_fma(a, b, c); }
DEV f32x2 cmul(f32x2 a, f32x2 w) {
  f32x2 t, r;
  asm("v_pk_mul_f32 %0, %1, %2 op_sel:[0,0] op_sel_hi:[0,1]" : "=v"(t) : "v"(a), "v"(w));
  asm("v_pk_fma_f32 %0, %1, %2, %3 op_sel:[1,1,0] op_sel_hi:[1,0,1] neg_lo:[0,1,0]" : "=v"(r) : "v"(a), "v"(w), "v"(t));
  return r;
}
DEV f32x2 cmulc(f32x2 a, f32x2 w) {
  f32x2 t, r;
  asm("v_pk_mul_f32 %0, %1, %2 op_sel:[0,0] op_sel_hi:[0,1] neg_hi:[0,1]" : "=v"(t) : "v"(a), "v"(w));
  asm("v_pk_fma_f32 %0, %1, %2, %3 op_sel:[1,1,0] op_sel_hi:[1,0,1]" : "=v"(r) : "v"(a), "v"(w), "v"(t));
  return r;
}
DEV f32x2 twid(float f) {
  f32x2 r;
  r.x = __builtin_amdgcn_cosf(f);
  r.y = __builtin_amdgcn_sinf(-f);
  return r;
}
DEV void st_wt(f32x2* p, f32x2 v) {
  __hip_atomic_store((unsigned long long*)p, __builtin_bit_cast(unsigned long long, v), __ATOMIC_RELAXED,
                     __HIP_MEMORY_SCOPE_AGENT);
}
DEV void st_wt32(uint32_t* p, uint32_t v) { __hip_atomic_store(p, v, __ATOMIC_RELAXED, __HIP_MEMORY_SCOPE_AGENT); }
DEV void st_wt64(u32x2* p, u32x2 v) {
  __hip_atomic_store((unsigned long long*)p, __builtin_bit_cast(unsigned long long, v), __ATOMIC_RELAXED,
                     __HIP_MEMORY_SCOPE_AGENT);
}
DEV void st_wt128(void* p, u32x4 v) {
  asm volatile("global_store_dwordx4 %0, %1, off sc1\n\ts_nop 1" ::"v"(p), "v"(v) : "memory");
}
#define ST_H(p, v) st_wt32(p, v)
#define ST_H1(p, v) st_wt64(p, v)
template <int N> DEV void pin(f32x2 (&a)[N]) {
#pragma unroll
  for (int i = 0; i < N; ++i) asm volatile("" : "+v"(a[i]));
}
template <bool INV> DEV f32x2 add_rot(f32x2 b, f32x2 d) { return INV ? cadd_pi(b, d) : cadd_mi(b, d); }
template <bool INV> DEV f32x2 sub_rot(f32x2 b, f32x2 d) { return INV ? cadd_mi(b, d) : cadd_pi(b, d); }

template <bool INV> DEV void fft4(f32x2& a0, f32x2& a1, f32x2& a2, f32x2& a3) {
  f32x2 b0 = a0 + a2, b1 = a0 - a2, b2 = a1 + a3, d = a1 - a3;
  a0 = b0 + b2; a2 = b0 - b2;
  a1 = add_rot<INV>(b1, d); a3 = sub_rot<INV>(b1, d);
}
template <bool INV> DEV void fft8(f32x2 (&v)[8]) {
  const f32x2 r = {0.70710678118654752440f, 0.70710678118654752440f};
  const f32x2 nr = {-0.70710678118654752440f, -0.70710678118654752440f};
  f32x2 e0 = v[0] + v[4], e1 = v[1] + v[5], e2 = v[2] + v[6], e3 = v[3] + v[7];
  f32x2 o0 = v[0] - v[4], o1 = v[1] - v[5], o2 = v[2] - v[6], o3 = v[3] - v[7];
  fft4<INV>(e0, e1, e2, e3);
  f32x2 p1 = INV ? cadd_pi(o1, o1) : cadd_mi(o1, o1);
  f32x2 p3 = INV ? cnadd_pi(o3, o3) : cnadd_mi(o3, o3);
  f32x2 s = p1 + p3, d = p1 - p3;
  f32x2 b0 = add_rot<INV>(o0, o2), b1 = sub_rot<INV>(o0, o2);
  v[0] = e0; v[2] = e1; v[4] = e2; v[6] = e3;
  v[1] = pfma(r, s, b0); v[5] = pfma(nr, s, b0);
  v[3] = INV ? cfma_pi(r, d, b1) : cfma_mi(r, d, b1);
  v[7] = INV ? cfma_mi(r, d, b1) : cfma_pi(r, d, b1);
}
template <bool INV> DEV void fft5(f32x2 (&v)[5]) {
  const f32x2 c1 = {0.30901699437494742410f, 0.30901699437494742410f};
  const f32x2 c2 = {-0.80901699437494742410f, -0.80901699437494742410f};
  const f32x2 s1 = {0.95105651629515357212f, 0.95105651629515357212f};
  const f32x2 s2 = {0.58778525229247312917f, 0.58778525229247312917f};
  const f32x2 ns1 = {-0.95105651629515357212f, -0.95105651629515357212f};
  f32x2 t1 = v[1] + v[4], t2 = v[2] + v[3], t3 = v[1] - v[4], t4 = v[2] - v[3];
  f32x2 a1 = pfma(c2, t2, pfma(c1, t1, v[0]));
  f32x2 a2 = pfma(c1, t2, pfma(c2, t1, v[0]));
  f32x2 b1 = pfma(s2, t4, s1 * t3);
  f32x2 b2 = pfma(ns1, t4, s2 * t3);
  v[0] = v[0] + t1 + t2;
  v[1] = add_rot<INV>(a1, b1); v[4] = sub_rot<INV>(a1, b1);
  v[2] = add_rot<INV>(a2, b2); v[3] = sub_rot<INV>(a2, b2);
}

template <int CTRL> DEV float dppmov(float v) {
  return __builtin_bit_cast(float, __builtin_amdgcn_update_dpp(0, __builtin_bit_cast(int, v), CTRL, 0xf, 0xf, false));
}
DEV float wave_sum(float v) {
  v += dppmov<0xB1>(v);
  v += dppmov<0x4E>(v);
  v += dppmov<0x141>(v);
  v += dppmov<0x140>(v);
  int iv = __builtin_bit_cast(int, v);
  float a = __builtin_bit_cast(float, __builtin_amdgcn_readlane(iv, 0));
  float b = __builtin_bit_cast(float, __builtin_amdgcn_readlane(iv, 16));
  float c = __builtin_bit_cast(float, __builtin_amdgcn_readlane(iv, 32));
  float d = __builtin_bit_cast(float, __builtin_amdgcn_readlane(iv, 48));
  return (a + b) + (c + d);
}
DEV void wave_sync() {
  __builtin_amdgcn_fence(__ATOMIC_RELEASE, "wavefront");
  __builtin_amdgcn_wave_barrier();
  __builtin_amdgcn_fence(__ATOMIC_ACQUIRE, "wavefront");
}

DEV void row_layernorm(const f32x2 (&xv)[5], uint32_t (&zp)[5]) {
  f32x2 s2 = (xv[0] + xv[1]) + (xv[2] + xv[3]) + xv[4];
  const float mean = wave_sum(s2.x + s2.y) * (1.0f / 640.0f);
  const f32x2 nm = {-mean, -mean};
  f32x2 q2 = {0.f, 0.f};
#pragma unroll
  for (int i = 0; i < 5; ++i) { f32x2 d = xv[i] + nm; q2 = pfma(d, d, q2); }
  const float var = wave_sum(q2.x + q2.y) * (1.0f / 640.0f);
  f32x2 mv; mv.x = mean; mv.y = var;
  mv = bf_round(mv);
  const float den = bfr(sqrtf(bfr(mv.y + 1e-5f)));
  const float rden = 1.0f / den;
  const f32x2 nmb = {-mv.x, -mv.x}, rd = {rden, rden};
#pragma unroll
  for (int i = 0; i < 5; ++i) zp[i] = bf_pack(bf_round(xv[i] + nmb) * rd);
}

__global__ __launch_bounds__(512) void k_fwd_rows(const uint32_t* __restrict__ x, f32x2* __restrict__ T,
                                                  const uint16_t* __restrict__ wr, const uint16_t* __restrict__ wi,
                                                  const uint32_t* __restrict__ w1, const u32x4* __restrict__ w2,
                                                  u32x4* __restrict__ w2p) {
  __shared__ f32x2 ex[8][360];
  __shared__ f32x2 zz[8][320];
  const int tid = threadIdx.x, lane = tid & 63, j = tid >> 6;
  STAMP(0, 0);
  const int s0 = 32 * (blockIdx.x & 7) + (blockIdx.x >> 3);
  const int row = s0 + 256 * j;
  uint32_t xd[5];
#pragma unroll
  for (int i = 0; i < 5; ++i) xd[i] = x[row * 320 + lane + 64 * i];
  uint32_t pf = 0;
  if (lane < 12) {
    const int t = (blockIdx.x & 7) + 8 * (8 * (blockIdx.x >> 3) + j);
    pf = ((lane < 6) ? wr : wi)[t * DF_ + min(64 * (lane % 6), DF_ - 1)];
  } else if (PF_W && lane >= 56) {
    const int l = j * 8 + (lane - 56);
    pf = w1[(blockIdx.x * 60 + min(l, 59)) * 32];
  }
  uint32_t pfc = 0;
  {
    unsigned long long c0, c1;
    asm volatile("s_getpc_b64 s[40:41]\n\ts_add_u32 s40, s40, _Z6k_colsPDv2_fPKtS2_@rel32@lo+4\n\ts_addc_u32 s41, s41, _Z6k_colsPDv2_fPKtS2_@rel32@hi+12\n\ts_mov_b64 %0, s[40:41]" : "=s"(c0) :: "s40", "s41", "scc");
    asm volatile("s_getpc_b64 s[40:41]\n\ts_add_u32 s40, s40, _Z8k_zz_endv@rel32@lo+4\n\ts_addc_u32 s41, s41, _Z8k_zz_endv@rel32@hi+12\n\ts_mov_b64 %0, s[40:41]" : "=s"(c1) :: "s40", "s41", "scc");
    const long long len = (long long)(c1 - c0);
    const int li = (blockIdx.x >> 3) * 16 + (j * 2 + (lane - 16 >= 16 ? 1 : 0));
    if (len > 0 && len < 65536 && (lane == 16 || lane == 32) && li * 128 < len) pfc = *(const uint32_t*)(c0 + li * 128);
  }
  const int wid = min((int)blockIdx.x * 512 + tid, 122879);
  const u32x4 wchunk = w2[wid];

  f32x2 tw1[4], tw2[7], twq[7];
#pragma unroll
  for (int r = 1; r < 5; ++r) tw1[r - 1] = twid((float)(lane * r) * (1.0f / 320.0f));
  const int l0 = lane & 7, rr = lane >> 3;
#pragma unroll
  for (int q = 1; q < 8; ++q) tw2[q - 1] = twid((float)(l0 * q) * (1.0f / 64.0f));
#pragma unroll
  for (int q = 1; q < 8; ++q) twq[q - 1] = twid((float)(s0 * q) * (1.0f / 2048.0f));
  f32x2 twk_[1] = {twid((float)tid * (1.0f / 640.0f))};
  pin(tw1); pin(tw2); pin(twq); pin(twk_);
  const f32x2 twk = twk_[0];
  __builtin_amdgcn_sched_barrier(0);
  STAMP(0, 1);

  f32x2 u[5];
  {
    f32x2 xv[5];
    uint32_t zp[5];
#pragma unroll
    for (int i = 0; i < 5; ++i) xv[i] = bf_unpack(xd[i]);
    row_layernorm(xv, zp);
#pragma unroll
    for (int i = 0; i < 5; ++i) u[i] = bf_unpack(zp[i]);
  }
  STAMP(0, 2);

  f32x2* E = ex[j];
  fft5<false>(u);
  E[lane] = u[0];
#pragma unroll
  for (int r = 1; r < 5; ++r) E[r * 72 + lane] = cmul(u[r], tw1[r - 1]);
  wave_sync();
  if (lane < 40) {
    f32x2 v[8];
    f32x2* Er = E + rr * 72;
#pragma unroll
    for (int l1 = 0; l1 < 8; ++l1) v[l1] = Er[l0 + 8 * l1];
    fft8<false>(v);
    wave_sync();
    Er[l0] = v[0];
#pragma unroll
    for (int q = 1; q < 8; ++q) Er[q * 9 + l0] = cmul(v[q], tw2[q - 1]);
    wave_sync();
#pragma unroll
    for (int a = 0; a < 8; ++a) v[a] = Er[l0 * 9 + a];
    fft8<false>(v);
    f32x2* Z = zz[j] + rr + 5 * l0;
#pragma unroll
    for (int p = 0; p < 8; ++p) Z[40 * p] = v[p];
  }
  if (blockIdx.x < 240) {
    const int wk_ = wid / 80, wc_ = wid % 80;
    const int rho = (wk_ & ~12) | ((wk_ & 4) << 1) | ((wk_ & 8) >> 1);
    st_wt128(w2p + ((wc_ / 10) * 1536 + rho) * 10 + (wc_ % 10), wchunk);
  }
  STAMP(0, 3);
  __syncthreads();
  STAMP(0, 4);
  if (j < 6) {
    const int k = tid;
    const int ka = (k >= 320) ? 0 : k, kb = (k == 0 || k >= 320) ? 0 : 320 - k;
    f32x2 v[8];
#pragma unroll
    for (int jj = 0; jj < 8; ++jj) {
      const f32x2 a = zz[jj][ka], b = zz[jj][kb];
      f32x2 e, d;
      e.x = a.x + b.x; e.y = a.y - b.y;
      d.x = a.x - b.x; d.y = a.y + b.y;
      v[jj] = cadd_mi(e, cmul(d, twk));
    }
    fft8<false>(v);
    if (k < DF_) {
      f32x2* dst = T + (size_t)s0 * TP;
      st_wt(dst + k, v[0]);
#pragma unroll
      for (int q = 1; q < 8; ++q) st_wt(dst + (size_t)q * 256 * TP + k, cmul(v[q], twq[q - 1]));
    }
  }
  asm volatile("" ::"v"(pf));
  asm volatile("" ::"v"(pfc));
  STAMP(0, 5);
  STAMP_END(0, 6);
}

__global__ __launch_bounds__(256) void k_cols(f32x2* __restrict__ T, const uint16_t* __restrict__ wr,
                                              const uint16_t* __restrict__ wi) {
  __shared__ f32x2 e1[2048];
  __shared__ f32x2 e2[4][576];
  __shared__ __attribute__((aligned(16))) uint32_t wl[2048];
  const int tid = threadIdx.x, lane = tid & 63, wv = tid >> 6;
  STAMP(1, 0);
  const int q = blockIdx.x & 7, kg = blockIdx.x >> 3;
  const int c = tid & 7, a = tid >> 3;
  const int k = kg * 8 + c;
  const bool valid = k < DF_;
  f32x2* colbase = T + (size_t)(q * 256) * TP;
  const uint32_t coff = a * TP + (valid ? k : DF_ - 1);
  f32x2 v[8];
#pragma unroll
  for (int j1 = 0; j1 < 8; ++j1) v[j1] = (colbase + (size_t)j1 * 32 * TP)[coff];
  const int kb = min(8 * kg, DF_ - 8);
  u32x4 wre, wim;
  __builtin_memcpy(&wre, wr + (size_t)(q + 8 * tid) * DF_ + kb, 16);
  __builtin_memcpy(&wim, wi + (size_t)(q + 8 * tid) * DF_ + kb, 16);

  const int b = (lane >> 3) & 3, p1lo = lane >> 5, p1 = 2 * wv + p1lo;
  f32x2 twa[7], twb[7];
#pragma unroll
  for (int p = 1; p < 8; ++p) twa[p - 1] = twid((float)(a * p) * (1.0f / 256.0f));
#pragma unroll
  for (int p = 1; p < 8; ++p) twb[p - 1] = twid((float)(b * p) * (1.0f / 32.0f));
  pin(twa); pin(twb);
  __builtin_amdgcn_sched_barrier(0);
  STAMP(1, 1);

  fft8<false>(v);
  e1[tid] = v[0];
#pragma unroll
  for (int p = 1; p < 8; ++p) e1[p * 256 + tid] = cmul(v[p], twa[p - 1]);
  {
    u32x4 o0, o1;
    o0.x = __builtin_amdgcn_perm(wim.x, wre.x, 0x05040100); o0.y = __builtin_amdgcn_perm(wim.x, wre.x, 0x07060302);
    o0.z = __builtin_amdgcn_perm(wim.y, wre.y, 0x05040100); o0.w = __builtin_amdgcn_perm(wim.y, wre.y, 0x07060302);
    o1.x = __builtin_amdgcn_perm(wim.z, wre.z, 0x05040100); o1.y = __builtin_amdgcn_perm(wim.z, wre.z, 0x07060302);
    o1.z = __builtin_amdgcn_perm(wim.w, wre.w, 0x05040100); o1.w = __builtin_amdgcn_perm(wim.w, wre.w, 0x07060302);
    *(u32x4*)(wl + tid * 8) = o0;
    *(u32x4*)(wl + tid * 8 + 4) = o1;
  }
  STAMP(1, 2);
  __syncthreads();
  STAMP(1, 3);
  uint32_t wf[8];
  {
    const uint32_t* wlp = wl + (p1 + 16 * b) * 8 + min(8 * kg + c - kb, 7);
#pragma unroll
    for (int e = 0; e < 2; ++e)
#pragma unroll
      for (int mm = 0; mm < 4; ++mm) wf[e * 4 + mm] = wlp[(8 * e + 64 * mm) * 8];
  }
  f32x2* E1 = e1 + p1 * 256 + (lane & 31);
#pragma unroll
  for (int j2 = 0; j2 < 8; ++j2) v[j2] = E1[32 * j2];
  fft8<false>(v);
  f32x2* E2w = e2[wv] + p1lo * 288 + (lane & 31);
  E2w[0] = v[0];
#pragma unroll
  for (int p = 1; p < 8; ++p) E2w[p * 36] = cmul(v[p], twb[p - 1]);
  wave_sync();
  f32x2* E2r = e2[wv] + p1lo * 288 + (2 * b) * 36 + c;
#pragma unroll
  for (int e = 0; e < 2; ++e)
#pragma unroll
    for (int bb = 0; bb < 4; ++bb) v[e * 4 + bb] = E2r[e * 36 + bb * 8];
  fft4<false>(v[0], v[1], v[2], v[3]);
  fft4<false>(v[4], v[5], v[6], v[7]);
  STAMP(1, 4);
#pragma unroll
  for (int i = 0; i < 8; ++i) v[i] = cmul(v[i], bf_unpack(wf[i]));
  fft4<true>(v[0], v[1], v[2], v[3]);
  fft4<true>(v[4], v[5], v[6], v[7]);
  STAMP(1, 5);
#pragma unroll
  for (int e = 0; e < 2; ++e)
#pragma unroll
    for (int bb = 0; bb < 4; ++bb) E2r[e * 36 + bb * 8] = v[e * 4 + bb];
  wave_sync();
  v[0] = E2w[0];
#pragma unroll
  for (int p = 1; p < 8; ++p) v[p] = cmulc(E2w[p * 36], twb[p - 1]);
  fft8<true>(v);
#pragma unroll
  for (int j2 = 0; j2 < 8; ++j2) E1[32 * j2] = v[j2];
  STAMP(1, 6);
  __syncthreads();
  STAMP(1, 7);
  v[0] = e1[tid];
#pragma unroll
  for (int p = 1; p < 8; ++p) v[p] = cmulc(e1[p * 256 + tid], twa[p - 1]);
  fft8<true>(v);
  if (valid) {
#pragma unroll
    for (int j1 = 0; j1 < 8; ++j1) st_wt(colbase + (size_t)j1 * 32 * TP + coff, v[j1]);
  }
  STAMP(1, 8);
  STAMP_END(1, 9);
}

__global__ __launch_bounds__(512) void k_inv_rows(const f32x2* __restrict__ T, const uint32_t* __restrict__ x,
                                                  uint32_t* __restrict__ h, uint32_t* __restrict__ hn) {
  __shared__ f32x2 ex[8][360];
  __shared__ f32x2 xx[8][384];
  const int tid = threadIdx.x, lane = tid & 63, j = tid >> 6;
  STAMP(2, 0);
  const int s0 = 32 * (blockIdx.x & 7) + (blockIdx.x >> 3);
  const int row = s0 + 256 * j;
  const int prow = s0 * 8 + j;
  f32x2 v[8];
  {
    const f32x2* src = T + (size_t)s0 * TP;
    const uint32_t kk = min(tid, DF_ - 1);
#pragma unroll
    for (int q = 0; q < 8; ++q) v[q] = (src + (size_t)q * 256 * TP)[kk];
  }
  uint32_t xd[5];
#pragma unroll
  for (int i = 0; i < 5; ++i) xd[i] = x[row * 320 + lane + 64 * i];

  f32x2 tw1[4], tw2[7], twq[7], twk[8];
#pragma unroll
  for (int r = 1; r < 5; ++r) tw1[r - 1] = twid((float)(lane * r) * (1.0f / 320.0f));
  const int l0 = lane & 7, rr = lane >> 3;
#pragma unroll
  for (int q = 1; q < 8; ++q) tw2[q - 1] = twid((float)(l0 * q) * (1.0f / 64.0f));
#pragma unroll
  for (int q = 1; q < 8; ++q) twq[q - 1] = twid((float)(s0 * q) * (1.0f / 2048.0f));
#pragma unroll
  for (int p = 0; p < 8; ++p) twk[p] = twid((float)(rr + 5 * l0 + 40 * p) * (1.0f / 640.0f));
  pin(tw1); pin(tw2); pin(twq); pin(twk);
  __builtin_amdgcn_sched_barrier(0);
  STAMP(2, 1);

  if (j < 6) {
#pragma unroll
    for (int q = 1; q < 8; ++q) v[q] = cmulc(v[q], twq[q - 1]);
    fft8<true>(v);
    f32x2 msk;
    msk.x = 1.0f; msk.y = (tid == 0 || tid == 320) ? 0.0f : 1.0f;
#pragma unroll
    for (int jj = 0; jj < 8; ++jj) xx[jj][tid] = v[jj] * msk;
  }
  STAMP(2, 2);
  __syncthreads();
  STAMP(2, 3);
  f32x2* E = ex[j];
  if (lane < 40) {
    f32x2* Er = E + rr * 72;
    const f32x2* Xa = xx[j] + rr + 5 * l0;
    const f32x2* Xb = xx[j] + 320 - rr - 5 * l0;
#pragma unroll
    for (int p = 0; p < 8; ++p) {
      const f32x2 a = Xa[40 * p], b = Xb[-40 * p];
      f32x2 e, d;
      e.x = a.x + b.x; e.y = a.y - b.y;
      d.x = a.x - b.x; d.y = a.y + b.y;
      v[p] = cadd_pi(e, cmulc(d, twk[p]));
    }
    fft8<true>(v);
    Er[l0 * 9] = v[0];
#pragma unroll
    for (int a = 1; a < 8; ++a) Er[l0 * 9 + a] = cmulc(v[a], tw2[a - 1]);
    wave_sync();
#pragma unroll
    for (int q = 0; q < 8; ++q) v[q] = Er[q * 9 + l0];
    fft8<true>(v);
    wave_sync();
#pragma unroll
    for (int l1 = 0; l1 < 8; ++l1) Er[l0 + 8 * l1] = v[l1];
  }
  wave_sync();
  f32x2 u[5];
  u[0] = E[lane];
#pragma unroll
  for (int r = 1; r < 5; ++r) u[r] = cmulc(E[r * 72 + lane], tw1[r - 1]);
  fft5<true>(u);
  STAMP(2, 4);

  const f32x2 sc = {1.0f / 2621440.0f, 1.0f / 2621440.0f};
  f32x2 hv[5];
  uint32_t zp[5];
#pragma unroll
  for (int i = 0; i < 5; ++i) {
    const uint32_t hp = bf_pack(bf_unpack(xd[i]) + bf_round(u[i] * sc));
    ST_H(h + prow * 320 + lane + 64 * i, hp);
    hv[i] = bf_unpack(hp);
  }
  row_layernorm(hv, zp);
  STAMP(2, 5);
#pragma unroll
  for (int i = 0; i < 5; ++i) ST_H(hn + prow * 320 + lane + 64 * i, zp[i]);
  STAMP(2, 6);
  STAMP_END(2, 7);
}

template <int N> DEV void wait_vm() { asm volatile("s_waitcnt vmcnt(%0)" ::"n"(N) : "memory"); }
#define RAW_BAR() do { asm volatile("s_waitcnt lgkmcnt(0)" ::: "memory"); __builtin_amdgcn_s_barrier(); } while (0)

DEV void glds16(const void* src, char* lds) {
  __builtin_amdgcn_global_load_lds((const unsigned*)src, (unsigned*)lds, 16, 0, 0);
}
DEV uint32_t pk_relu_bf16(uint32_t p) {
  uint32_t r;
  asm("v_pk_max_i16 %0, %1, 0" : "=v"(r) : "v"(p));
  return r;
}

typedef short v4s_t __attribute__((ext_vector_type(4)));
typedef __attribute__((address_space(3))) v4s_t* lds_v4s_ptr;
DEV bf16x8 tr_frag(const char* p0, const char* p1) {
  const v4s_t a = __builtin_amdgcn_ds_read_tr16_b64_v4i16((lds_v4s_ptr)p0);
  const v4s_t b = __builtin_amdgcn_ds_read_tr16_b64_v4i16((lds_v4s_ptr)p1);
  return __builtin_shufflevector(a, b, 0, 1, 2, 3, 4, 5, 6, 7);
}

#undef STAMP_TID
#define STAMP_TID 512
__global__ __launch_bounds__(1024) void k_gemm1(const char* __restrict__ hn, const char* __restrict__ w1,
                                                const uint16_t* __restrict__ b1, u32x2* __restrict__ h1) {
  constexpr int NST = 10, NSLOT = 5, SB = 28 * 1024;
  __shared__ __attribute__((aligned(16))) char lds[NSLOT * SB + 256];
  const int tid = threadIdx.x, lane = tid & 63;
  const int wv = __builtin_amdgcn_readfirstlane(tid >> 6);
  const int xcd = blockIdx.x & 7, idx = blockIdx.x >> 3;
  const int rb = MAP_D ? 4 * (xcd >> 1) + (idx >> 3) : 2 * xcd + (idx >> 4);
  const int cb = MAP_D ? 8 * (xcd & 1) + (idx & 7) : (idx & 15);
  if (wv < 8) {
    const int lw = wv;
    const int arow = lane >> 3;
    const char* asrc = hn + (size_t)(rb * 128 + 16 * lw + arow) * 1280 + (((lane & 7) ^ arow) << 4);
    const int id0 = lw * 64 + lane, r0 = id0 / 12, c0 = (id0 % 12) ^ (((r0 >> 3) & 1) << 1);
    const int id1 = (8 + lw) * 64 + lane, r1 = id1 / 12, c1 = (id1 % 12) ^ (((r1 >> 3) & 1) << 1);
    const char* bsrc0 = w1 + (size_t)r0 * 3072 + cb * 192 + c0 * 16;
    const char* bsrc1 = w1 + (size_t)r1 * 3072 + cb * 192 + c1 * 16;
    auto stage = [&](int st) {
      char* dst = lds + (st % NSLOT) * SB + lane * 16;
      glds16(asrc + st * 128, dst + (2 * lw) * 1024);
      glds16(asrc + st * 128 + 8 * 1280, dst + (2 * lw + 1) * 1024);
      glds16(bsrc0 + (size_t)st * 64 * 3072, dst + (16 + lw) * 1024);
      if (lw < 4) glds16(bsrc1 + (size_t)st * 64 * 3072, dst + (24 + lw) * 1024);
    };
    if (lw == 7 && lane < 12) glds16((const u32x4*)(b1 + cb * 96) + lane, lds + NSLOT * SB + lane * 16);
#pragma unroll
    for (int st = 0; st < NSLOT - 1; ++st) stage(st);
#pragma unroll
    for (int st = 0; st < NST; ++st) {
      constexpr int LAST = NST - 1;
      const int inflight = ((st + NSLOT - 2 < LAST) ? (st + NSLOT - 2) : LAST) - st;
      if (lw < 4) {
        if (inflight == 3) wait_vm<12>(); else if (inflight == 2) wait_vm<8>(); else if (inflight == 1) wait_vm<4>(); else wait_vm<0>();
      } else {
        if (inflight == 3) wait_vm<9>(); else if (inflight == 2) wait_vm<6>(); else if (inflight == 1) wait_vm<3>(); else wait_vm<0>();
      }
      __builtin_amdgcn_s_barrier();
      if (st + NSLOT - 1 < NST) stage(st + NSLOT - 1);
    }
    return;
  }
  STAMP(3, 0);
  const int wm = (wv - 8) >> 1, wn = wv & 1;
  f32x4 acc[2][3];
#pragma unroll
  for (int mi = 0; mi < 2; ++mi)
#pragma unroll
    for (int ni = 0; ni < 3; ++ni) acc[mi][ni] = (f32x4){0.f, 0.f, 0.f, 0.f};
  const int ar = 32 * wm + (lane & 15);
  const int aoff0 = ar * 128 + (((lane >> 4) ^ (ar & 7)) << 4);
  const int aoff1 = ar * 128 + (((4 + (lane >> 4)) ^ (ar & 7)) << 4);
  const int g = lane >> 4, q4 = (lane & 15) >> 2, p4 = lane & 3;
  int boff[3];
#pragma unroll
  for (int ni = 0; ni < 3; ++ni) boff[ni] = 16384 + (8 * g + q4) * 192 + p4 * 8 + (((3 * wn + ni) ^ (g & 1)) << 5);

#pragma unroll
  for (int st = 0; st < NST; ++st) {
    RAW_BAR();
    int slot_off = (st % NSLOT) * SB;
    asm volatile("" : "+s"(slot_off));
    const char* base = lds + slot_off;
    bf16x8 af[2][2], bf[2][3];
#pragma unroll
    for (int ks = 0; ks < 2; ++ks) {
#pragma unroll
      for (int mi = 0; mi < 2; ++mi) af[ks][mi] = *(const bf16x8*)(base + (ks ? aoff1 : aoff0) + mi * 2048);
#pragma unroll
      for (int ni = 0; ni < 3; ++ni) bf[ks][ni] = tr_frag(base + boff[ni] + ks * 6144, base + boff[ni] + ks * 6144 + 768);
    }
#pragma unroll
    for (int ks = 0; ks < 2; ++ks)
#pragma unroll
      for (int mi = 0; mi < 2; ++mi)
#pragma unroll
        for (int ni = 0; ni < 3; ++ni)
          acc[mi][ni] = __builtin_amdgcn_mfma_f32_16x16x32_bf16(bf[ks][ni], af[ks][mi], acc[mi][ni], 0, 0, 0);
  }
  STAMP(3, 6);
#pragma unroll
  for (int ni = 0; ni < 3; ++ni) {
    const u32x2 bb = *(const u32x2*)(lds + NSLOT * SB + ((3 * wn + ni) * 16 + 4 * (lane >> 4)) * 2);
    const f32x2 b01 = bf_unpack(bb.x), b23 = bf_unpack(bb.y);
    const int NT = cb * 6 + 3 * wn + ni;
    const int ks2 = NT >> 1, gg = 2 * (NT & 1) + (lane >> 5), half = (lane >> 4) & 1;
#pragma unroll
    for (int mi = 0; mi < 2; ++mi) {
      f32x2 a01 = {acc[mi][ni][0], acc[mi][ni][1]}, a23 = {acc[mi][ni][2], acc[mi][ni][3]};
      a01 = bf_unpack(pk_relu_bf16(bf_pack(bf_round(a01) + b01)));
      a23 = bf_unpack(pk_relu_bf16(bf_pack(bf_round(a23) + b23)));
      u32x2 ov;
      ov.x = bf_pack(a01 * a01); ov.y = bf_pack(a23 * a23);
      const int mt = rb * 8 + 2 * wm + mi;
      ST_H1(h1 + ((((mt >> 2) * 48 + ks2) * 4 + (mt & 3)) * 64 + (lane & 15) + 16 * gg) * 2 + half, ov);
    }
  }
  STAMP(3, 7);
  STAMP_END(3, 8);
}

__global__ __launch_bounds__(1024) void k_gemm2(const u32x4* __restrict__ h1, const u32x4* __restrict__ w2p,
                                                const char* __restrict__ w2,
                                                const uint16_t* __restrict__ b2, const uint16_t* __restrict__ h,
                                                uint16_t* __restrict__ out) {
  constexpr int NST = 12, NSLOT = 4, SB = 36 * 1024;
  constexpr int RES_OFF = NSLOT * SB, BIAS_OFF = RES_OFF + 10240;
  __shared__ __attribute__((aligned(16))) char lds[BIAS_OFF + 256];
  const int tid = threadIdx.x, lane = tid & 63;
  const int wv = __builtin_amdgcn_readfirstlane(tid >> 6);
  const int xcd = blockIdx.x & 7, idx = blockIdx.x >> 3;
  const int rb2 = MAP_E ? 8 * (xcd >> 1) + (idx >> 2) : 4 * xcd + (idx >> 3);
  const int cb2 = MAP_E ? 4 * (xcd & 1) + (idx & 3) : (idx & 7);
  if (wv < 8) {
    const int lw = wv;
    const u32x4* asrc = h1 + (size_t)rb2 * 48 * 4 * 64 + (2 * lw) * 64 + lane;
    const u32x4* bsrc = w2p + (size_t)cb2 * 1536 * 10 + lw * 64 + lane;
    auto stage = [&](int st) {
      char* dst = lds + (st % NSLOT) * SB + lane * 16;
      glds16(asrc + (size_t)(st * 16) * 64, dst + (2 * lw) * 1024);
      glds16(asrc + (size_t)(st * 16 + 1) * 64, dst + (2 * lw + 1) * 1024);
      glds16(bsrc + st * 1280, dst + (16 + lw) * 1024);
      glds16(bsrc + st * 1280 + 512, dst + (24 + lw) * 1024);
      if (lw < 4) glds16(bsrc + st * 1280 + 1024, dst + (32 + lw) * 1024);
    };
    stage(0);
    if (lw == 7 && lane < 10) glds16((const u32x4*)(b2 + cb2 * 80) + lane, lds + BIAS_OFF + lane * 16);
    {
      const int id = lw * 64 + lane, r = id / 10, ch = id % 10;
      glds16((const u32x4*)(h + (size_t)(rb2 * 64 + r) * D_ + cb2 * 80) + ch, lds + RES_OFF + id * 16);
      if (lw >= 6) {
        const int id2 = (lw + 2) * 64 + lane, r2 = id2 / 10, ch2 = id2 % 10;
        glds16((const u32x4*)(h + (size_t)(rb2 * 64 + r2) * D_ + cb2 * 80) + ch2, lds + RES_OFF + id2 * 16);
      }
    }
#pragma unroll
    for (int st = 1; st < NSLOT - 1; ++st) stage(st);
#pragma unroll
    for (int st = 0; st < NST; ++st) {
      constexpr int LAST = NST - 1;
      const int inflight = ((st + NSLOT - 2 < LAST) ? (st + NSLOT - 2) : LAST) - st;
      if (lw < 4) {
        if (inflight == 2) wait_vm<10>(); else if (inflight == 1) wait_vm<5>(); else wait_vm<0>();
      } else {
        if (inflight == 2) wait_vm<8>(); else if (inflight == 1) wait_vm<4>(); else wait_vm<0>();
      }
      __builtin_amdgcn_s_barrier();
      if (st + NSLOT - 1 < NST) stage(st + NSLOT - 1);
    }
    __builtin_amdgcn_s_barrier();
    return;
  }
  STAMP(4, 0);
  const int wm = wv & 3, wk = (wv >> 2) & 1;
  f32x4 acc[5];
#pragma unroll
  for (int ni = 0; ni < 5; ++ni) acc[ni] = (f32x4){0.f, 0.f, 0.f, 0.f};
  const int g = lane >> 4, q4 = (lane & 15) >> 2, p4 = lane & 3;
  const int boff = 16384 + (16 * (g >> 1) + 4 * (g & 1) + q4 + 32 * wk) * 160 + p4 * 8;
  const int aoff = (wk * 4 + wm) * 1024 + lane * 16;

#pragma unroll
  for (int st = 0; st < NST; ++st) {
    RAW_BAR();
    int slot_off = (st % NSLOT) * SB;
    asm volatile("" : "+s"(slot_off));
    const char* base = lds + slot_off;
    bf16x8 af[2], bf[2][5];
#pragma unroll
    for (int kk = 0; kk < 2; ++kk) {
      af[kk] = *(const bf16x8*)(base + aoff + kk * 8192);
#pragma unroll
      for (int ni = 0; ni < 5; ++ni)
        bf[kk][ni] = tr_frag(base + boff + kk * 10240 + ni * 32, base + boff + kk * 10240 + ni * 32 + 1280);
    }
#pragma unroll
    for (int kk = 0; kk < 2; ++kk)
#pragma unroll
      for (int ni = 0; ni < 5; ++ni) acc[ni] = __builtin_amdgcn_mfma_f32_16x16x32_bf16(bf[kk][ni], af[kk], acc[ni], 0, 0, 0);
  }
  STAMP(4, 6);
  f32x4* red = (f32x4*)lds + (wm * 5) * 64 + lane;
  if (wk == 1) {
#pragma unroll
    for (int ni = 0; ni < 5; ++ni) red[ni * 64] = acc[ni];
  }
  RAW_BAR();
  if (wk == 0) {
    const int prow = rb2 * 64 + wm * 16 + (lane & 15);
    const int mrow = (prow >> 3) + 256 * (prow & 7);
    const int ncol = cb2 * 80 + 4 * (lane >> 4);
#pragma unroll
    for (int ni = 0; ni < 5; ++ni) {
      const f32x4 o = acc[ni] + red[ni * 64];
      const u32x2 bb = *(const u32x2*)(lds + BIAS_OFF + (16 * ni + 4 * (lane >> 4)) * 2);
      const u32x2 rs = *(const u32x2*)(lds + RES_OFF + (wm * 16 + (lane & 15)) * 160 + (16 * ni + 4 * (lane >> 4)) * 2);
      f32x2 a01 = {o[0], o[1]}, a23 = {o[2], o[3]};
      u32x2 ov;
      ov.x = bf_pack(bf_unpack(rs.x) + bf_round(bf_round(a01) + bf_unpack(bb.x)));
      ov.y = bf_pack(bf_unpack(rs.y) + bf_round(bf_round(a23) + bf_unpack(bb.y)));
      *(u32x2*)(out + (size_t)mrow * D_ + ncol + 16 * ni) = ov;
    }
  }
  STAMP(4, 7);
  STAMP_END(4, 8);
}

__global__ void k_zz_end() {}
extern "C" void kernel_launch(void* const* d_in, const int* in_sizes, int n_in, void* d_out, int out_size,
                              void* d_ws, size_t ws_size, hipStream_t stream) {
  const uint32_t* x = (const uint32_t*)d_in[0];
  const uint16_t* wr = (const uint16_t*)d_in[1];
  const uint16_t* wi = (const uint16_t*)d_in[2];
  const uint16_t* w1 = (const uint16_t*)d_in[4];
  const uint16_t* b1 = (const uint16_t*)d_in[5];
  const uint16_t* w2 = (const uint16_t*)d_in[6];
  const uint16_t* b2 = (const uint16_t*)d_in[7];
  char* ws = (char*)d_ws;
  size_t off = 0;
  f32x2* T = (f32x2*)(ws + off); off += (size_t)8 * 256 * TP * 8;
  uint32_t* h = (uint32_t*)(ws + off); off += (size_t)S_ * D_ * 2;
  uint32_t* hn = (uint32_t*)(ws + off); off += (size_t)S_ * D_ * 2;
  u32x4* h1 = (u32x4*)(ws + off); off += (size_t)S_ * F_ * 2;
  u32x4* w2p = (u32x4*)(ws + off); off += (size_t)D_ * F_ * 2;

  k_fwd_rows<<<256, 512, 0, stream>>>(x, T, wr, wi, (const uint32_t*)w1, (const u32x4*)w2, w2p);
  k_cols<<<328, 256, 0, stream>>>(T, wr, wi);
  k_inv_rows<<<256, 512, 0, stream>>>(T, x, h, hn);
  k_gemm1<<<256, 1024, 0, stream>>>((const char*)hn, (const char*)w1, b1, (u32x2*)h1);
  k_gemm2<<<256, 1024, 0, stream>>>(h1, w2p, (const char*)w2, b2, (const uint16_t*)h, (uint16_t*)d_out);
}
```

```cpp
#define PF_SELF 0
#include <hip/hip_runtime.h>
#include <hip/hip_bf16.h>
#include <stdint.h>

typedef float f32x2 __attribute__((ext_vector_type(2)));
typedef float f32x4 __attribute__((ext_vector_type(4)));
typedef short bf16x8 __attribute__((ext_vector_type(8)));
typedef unsigned int u32x4 __attribute__((ext_vector_type(4)));
typedef unsigned int u32x2 __attribute__((ext_vector_type(2)));

#define DEV __device__ __forceinline__

#define STAMP(kern, i)
#define STAMP_END(kern, i)
#define STAMP_TID 0

#define W2_MODE 1
#define WT_C
#define WT_D
#define PF_CODE 1
#define MAP_D 0
#define MAP_E 0
#define PF_W 1
static constexpr int S_ = 2048, D_ = 640, F_ = 1536, DF_ = 321;
static constexpr int TP = 328;

DEV float bf_lo(uint32_t p) { return __builtin_bit_cast(float, p << 16); }
DEV float bf_hi(uint32_t p) { return __builtin_bit_cast(float, p & 0xffff0000u); }
DEV f32x2 bf_unpack(uint32_t p) { f32x2 r; r.x = bf_lo(p); r.y = bf_hi(p); return r; }
typedef __bf16 bf16x2_t __attribute__((ext_vector_type(2)));
DEV uint32_t bf_pack(f32x2 v) {
  return __builtin_bit_cast(uint32_t, __builtin_convertvector(v, bf16x2_t));
}
DEV f32x2 bf_round(f32x2 v) { return bf_unpack(bf_pack(v)); }
DEV float bfr(float f) { f32x2 v; v.x = f; v.y = f; return bf_lo(bf_pack(v)); }

DEV f32x2 cadd_mi(f32x2 b, f32x2 d) {
  f32x2 r;
  asm("v_pk_add_f32 %0, %1, %2 op_sel:[0,1] op_sel_hi:[1,0] neg_hi:[0,1]" : "=v"(r) : "v"(b), "v"(d));
  return r;
}
DEV f32x2 cadd_pi(f32x2 b, f32x2 d) {
  f32x2 r;
  asm("v_pk_add_f32 %0, %1, %2 op_sel:[0,1] op_sel_hi:[1,0] neg_lo:[0,1]" : "=v"(r) : "v"(b), "v"(d));
  return r;
}
DEV f32x2 cnadd_mi(f32x2 b, f32x2 d) {
  f32x2 r;
  asm("v_pk_add_f32 %0, %1, %2 op_sel:[0,1] op_sel_hi:[1,0] neg_lo:[1,0] neg_hi:[1,1]" : "=v"(r) : "v"(b), "v"(d));
  return r;
}
DEV f32x2 cnadd_pi(f32x2 b, f32x2 d) {
  f32x2 r;
  asm("v_pk_add_f32 %0, %1, %2 op_sel:[0,1] op_sel_hi:[1,0] neg_lo:[1,1] neg_hi:[1,0]" : "=v"(r) : "v"(b), "v"(d));
  return r;
}
DEV f32x2 cfma_mi(f32x2 s, f32x2 d, f32x2 b) {
  f32x2 r;
  asm("v_pk_fma_f32 %0, %1, %2, %3 op_sel:[0,1,0] op_sel_hi:[1,0,1] neg_hi:[0,1,0]" : "=v"(r) : "v"(s), "v"(d), "v"(b));
  return r;
}
DEV f32x2 cfma_pi(f32x2 s, f32x2 d, f32x2 b) {
  f32x2 r;
  asm("v_pk_fma_f32 %0, %1, %2, %3 op_sel:[0,1,0] op_sel_hi:[1,0,1] neg_lo:[0,1,0]" : "=v"(r) : "v"(s), "v"(d), "v"(b));
  return r;
}
DEV f32x2 pfma(f32x2 a, f32x2 b, f32x2 c) { return __builtin_elementwise_fma(a, b, c); }
DEV f32x2 cmul(f32x2 a, f32x2 w) {
  f32x2 t, r;
  asm("v_pk_mul_f32 %0, %1, %2 op_sel:[0,0] op_sel_hi:[0,1]" : "=v"(t) : "v"(a), "v"(w));
  asm("v_pk_fma_f32 %0, %1, %2, %3 op_sel:[1,1,0] op_sel_hi:[1,0,1] neg_lo:[0,1,0]" : "=v"(r) : "v"(a), "v"(w), "v"(t));
  return r;
}
DEV f32x2 cmulc(f32x2 a, f32x2 w) {
  f32x2 t, r;
  asm("v_pk_mul_f32 %0, %1, %2 op_sel:[0,0] op_sel_hi:[0,1] neg_hi:[0,1]" : "=v"(t) : "v"(a), "v"(w));
  asm("v_pk_fma_f32 %0, %1, %2, %3 op_sel:[1,1,0] op_sel_hi:[1,0,1]" : "=v"(r) : "v"(a), "v"(w), "v"(t));
  return r;
}
DEV f32x2 twid(float f) {
  f32x2 r;
  r.x = __builtin_amdgcn_cosf(f);
  r.y = __builtin_amdgcn_sinf(-f);
  return r;
}
DEV void st_wt(f32x2* p, f32x2 v) {
  __hip_atomic_store((unsigned long long*)p, __builtin_bit_cast(unsigned long long, v), __ATOMIC_RELAXED,
                     __HIP_MEMORY_SCOPE_AGENT);
}
DEV void st_wt32(uint32_t* p, uint32_t v) { __hip_atomic_store(p, v, __ATOMIC_RELAXED, __HIP_MEMORY_SCOPE_AGENT); }
DEV void st_wt64(u32x2* p, u32x2 v) {
  __hip_atomic_store((unsigned long long*)p, __builtin_bit_cast(unsigned long long, v), __ATOMIC_RELAXED,
                     __HIP_MEMORY_SCOPE_AGENT);
}
DEV void st_wt128(void* p, u32x4 v) {
  asm volatile("global_store_dwordx4 %0, %1, off sc1\n\ts_nop 1" ::"v"(p), "v"(v) : "memory");
}
#define ST_H(p, v) st_wt32(p, v)
#define ST_H1(p, v) st_wt64(p, v)
template <int N> DEV void pin(f32x2 (&a)[N]) {
#pragma unroll
  for (int i = 0; i < N; ++i) asm volatile("" : "+v"(a[i]));
}
template <bool INV> DEV f32x2 add_rot(f32x2 b, f32x2 d) { return INV ? cadd_pi(b, d) : cadd_mi(b, d); }
template <bool INV> DEV f32x2 sub_rot(f32x2 b, f32x2 d) { return INV ? cadd_mi(b, d) : cadd_pi(b, d); }

template <bool INV> DEV void fft4(f32x2& a0, f32x2& a1, f32x2& a2, f32x2& a3) {
  f32x2 b0 = a0 + a2, b1 = a0 - a2, b2 = a1 + a3, d = a1 - a3;
  a0 = b0 + b2; a2 = b0 - b2;
  a1 = add_rot<INV>(b1, d); a3 = sub_rot<INV>(b1, d);
}
template <bool INV> DEV void fft8(f32x2 (&v)[8]) {
  const f32x2 r = {0.70710678118654752440f, 0.70710678118654752440f};
  const f32x2 nr = {-0.70710678118654752440f, -0.70710678118654752440f};
  f32x2 e0 = v[0] + v[4], e1 = v[1] + v[5], e2 = v[2] + v[6], e3 = v[3] + v[7];
  f32x2 o0 = v[0] - v[4], o1 = v[1] - v[5], o2 = v[2] - v[6], o3 = v[3] - v[7];
  fft4<INV>(e0, e1, e2, e3);
  f32x2 p1 = INV ? cadd_pi(o1, o1) : cadd_mi(o1, o1);
  f32x2 p3 = INV ? cnadd_pi(o3, o3) : cnadd_mi(o3, o3);
  f32x2 s = p1 + p3, d = p1 - p3;
  f32x2 b0 = add_rot<INV>(o0, o2), b1 = sub_rot<INV>(o0, o2);
  v[0] = e0; v[2] = e1; v[4] = e2; v[6] = e3;
  v[1] = pfma(r, s, b0); v[5] = pfma(nr, s, b0);
  v[3] = INV ? cfma_pi(r, d, b1) : cfma_mi(r, d, b1);
  v[7] = INV ? cfma_mi(r, d, b1) : cfma_pi(r, d, b1);
}
template <bool INV> DEV void fft5(f32x2 (&v)[5]) {
  const f32x2 c1 = {0.30901699437494742410f, 0.30901699437494742410f};
  const f32x2 c2 = {-0.80901699437494742410f, -0.80901699437494742410f};
  const f32x2 s1 = {0.95105651629515357212f, 0.95105651629515357212f};
  const f32x2 s2 = {0.58778525229247312917f, 0.58778525229247312917f};
  const f32x2 ns1 = {-0.95105651629515357212f, -0.95105651629515357212f};
  f32x2 t1 = v[1] + v[4], t2 = v[2] + v[3], t3 = v[1] - v[4], t4 = v[2] - v[3];
  f32x2 a1 = pfma(c2, t2, pfma(c1, t1, v[0]));
  f32x2 a2 = pfma(c1, t2, pfma(c2, t1, v[0]));
  f32x2 b1 = pfma(s2, t4, s1 * t3);
  f32x2 b2 = pfma(ns1, t4, s2 * t3);
  v[0] = v[0] + t1 + t2;
  v[1] = add_rot<INV>(a1, b1); v[4] = sub_rot<INV>(a1, b1);
  v[2] = add_rot<INV>(a2, b2); v[3] = sub_rot<INV>(a2, b2);
}

template <int CTRL> DEV float dppmov(float v) {
  return __builtin_bit_cast(float, __builtin_amdgcn_update_dpp(0, __builtin_bit_cast(int, v), CTRL, 0xf, 0xf, false));
}
DEV float wave_sum(float v) {
  v += dppmov<0xB1>(v);
  v += dppmov<0x4E>(v);
  v += dppmov<0x141>(v);
  v += dppmov<0x140>(v);
  int iv = __builtin_bit_cast(int, v);
  float a = __builtin_bit_cast(float, __builtin_amdgcn_readlane(iv, 0));
  float b = __builtin_bit_cast(float, __builtin_amdgcn_readlane(iv, 16));
  float c = __builtin_bit_cast(float, __builtin_amdgcn_readlane(iv, 32));
  float d = __builtin_bit_cast(float, __builtin_amdgcn_readlane(iv, 48));
  return (a + b) + (c + d);
}
DEV void wave_sync() {
  __builtin_amdgcn_fence(__ATOMIC_RELEASE, "wavefront");
  __builtin_amdgcn_wave_barrier();
  __builtin_amdgcn_fence(__ATOMIC_ACQUIRE, "wavefront");
}

DEV void row_layernorm(const f32x2 (&xv)[5], uint32_t (&zp)[5]) {
  f32x2 s2 = (xv[0] + xv[1]) + (xv[2] + xv[3]) + xv[4];
  const float mean = wave_sum(s2.x + s2.y) * (1.0f / 640.0f);
  const f32x2 nm = {-mean, -mean};
  f32x2 q2 = {0.f, 0.f};
#pragma unroll
  for (int i = 0; i < 5; ++i) { f32x2 d = xv[i] + nm; q2 = pfma(d, d, q2); }
  const float var = wave_sum(q2.x + q2.y) * (1.0f / 640.0f);
  f32x2 mv; mv.x = mean; mv.y = var;
  mv = bf_round(mv);
  const float den = bfr(sqrtf(bfr(mv.y + 1e-5f)));
  const float rden = 1.0f / den;
  const f32x2 nmb = {-mv.x, -mv.x}, rd = {rden, rden};
#pragma unroll
  for (int i = 0; i < 5; ++i) zp[i] = bf_pack(bf_round(xv[i] + nmb) * rd);
}

__global__ __launch_bounds__(512) void k_fwd_rows(const uint32_t* __restrict__ x, f32x2* __restrict__ T,
                                                  const uint16_t* __restrict__ wr, const uint16_t* __restrict__ wi,
                                                  const uint32_t* __restrict__ w1, const u32x4* __restrict__ w2,
                                                  u32x4* __restrict__ w2p, uint32_t* __restrict__ cnt) {
  __shared__ f32x2 ex[8][360];
  __shared__ f32x2 zz[8][320];
  const int tid = threadIdx.x, lane = tid & 63, j = tid >> 6;
  STAMP(0, 0);
  if (blockIdx.x == 0 && tid < 32) st_wt32(cnt + tid * 64, 0u);
  const int s0 = 32 * (blockIdx.x & 7) + (blockIdx.x >> 3);
  const int row = s0 + 256 * j;
  uint32_t xd[5];
#pragma unroll
  for (int i = 0; i < 5; ++i) xd[i] = x[row * 320 + lane + 64 * i];
  uint32_t pf = 0;
  {
    const char* pa = nullptr;
    if (lane < 12) {
      const int t = (blockIdx.x & 7) + 8 * (8 * (blockIdx.x >> 3) + j);
      pa = (const char*)(((lane < 6) ? wr : wi) + (t * DF_ + min(64 * (lane % 6), DF_ - 2)));
      pa = (const char*)((uintptr_t)pa & ~(uintptr_t)3);
    } else if (lane >= 56) {
      pa = (const char*)(w1 + (blockIdx.x * 60 + min(j * 8 + (lane - 56), 59)) * 32);
    }
    else if (lane < 18) {
      unsigned long long c0, c1;
      asm volatile("s_getpc_b64 s[40:41]\n\ts_add_u32 s40, s40, _Z6k_colsPDv2_fPKtS2_@rel32@lo+4\n\ts_addc_u32 s41, s41, _Z6k_colsPDv2_fPKtS2_@rel32@hi+12\n\ts_mov_b64 %0, s[40:41]" : "=s"(c0) :: "s40", "s41", "scc");
      asm volatile("s_getpc_b64 s[40:41]\n\ts_add_u32 s40, s40, _Z8k_zz_endv@rel32@lo+4\n\ts_addc_u32 s41, s41, _Z8k_zz_endv@rel32@hi+12\n\ts_mov_b64 %0, s[40:41]" : "=s"(c1) :: "s40", "s41", "scc");
      const long long len = (long long)(c1 - c0);
      const int li = ((blockIdx.x >> 3) * 8 + j) * 2 + (lane - 16);
      if (lane >= 16 && len > 0 && len < 65536 && li * 128 < len) pa = (const char*)(c0 + li * 128);
    }
    if (pa) pf = *(const uint32_t*)pa;
  }
  const int wid = min((int)blockIdx.x * 512 + tid, 122879);
  const u32x4 wchunk = w2[wid];

  f32x2 tw1[4], tw2[7], twq[7];
#pragma unroll
  for (int r = 1; r < 5; ++r) tw1[r - 1] = twid((float)(lane * r) * (1.0f / 320.0f));
  const int l0 = lane & 7, rr = lane >> 3;
#pragma unroll
  for (int q = 1; q < 8; ++q) tw2[q - 1] = twid((float)(l0 * q) * (1.0f / 64.0f));
#pragma unroll
  for (int q = 1; q < 8; ++q) twq[q - 1] = twid((float)(s0 * q) * (1.0f / 2048.0f));
  f32x2 twk_[1] = {twid((float)tid * (1.0f / 640.0f))};
  pin(tw1); pin(tw2); pin(twq); pin(twk_);
  const f32x2 twk = twk_[0];
  __builtin_amdgcn_sched_barrier(0);
  STAMP(0, 1);

  f32x2 u[5];
  {
    f32x2 xv[5];
    uint32_t zp[5];
#pragma unroll
    for (int i = 0; i < 5; ++i) xv[i] = bf_unpack(xd[i]);
    row_layernorm(xv, zp);
#pragma unroll
    for (int i = 0; i < 5; ++i) u[i] = bf_unpack(zp[i]);
  }
  STAMP(0, 2);

  f32x2* E = ex[j];
  fft5<false>(u);
  E[lane] = u[0];
#pragma unroll
  for (int r = 1; r < 5; ++r) E[r * 72 + lane] = cmul(u[r], tw1[r - 1]);
  wave_sync();
  if (lane < 40) {
    f32x2 v[8];
    f32x2* Er = E + rr * 72;
#pragma unroll
    for (int l1 = 0; l1 < 8; ++l1) v[l1] = Er[l0 + 8 * l1];
    fft8<false>(v);
    wave_sync();
    Er[l0] = v[0];
#pragma unroll
    for (int q = 1; q < 8; ++q) Er[q * 9 + l0] = cmul(v[q], tw2[q - 1]);
    wave_sync();
#pragma unroll
    for (int a = 0; a < 8; ++a) v[a] = Er[l0 * 9 + a];
    fft8<false>(v);
    f32x2* Z = zz[j] + rr + 5 * l0;
#pragma unroll
    for (int p = 0; p < 8; ++p) Z[40 * p] = v[p];
  }
  if (blockIdx.x < 240) {
    const int wk_ = wid / 80, wc_ = wid % 80;
    const int rho = (wk_ & ~12) | ((wk_ & 4) << 1) | ((wk_ & 8) >> 1);
    st_wt128(w2p + ((wc_ / 10) * 1536 + rho) * 10 + (wc_ % 10), wchunk);
  }
  STAMP(0, 3);
  __syncthreads();
  STAMP(0, 4);
  if (j < 6) {
    const int k = tid;
    const int ka = (k >= 320) ? 0 : k, kb = (k == 0 || k >= 320) ? 0 : 320 - k;
    f32x2 v[8];
#pragma unroll
    for (int jj = 0; jj < 8; ++jj) {
      const f32x2 a = zz[jj][ka], b = zz[jj][kb];
      f32x2 e, d;
      e.x = a.x + b.x; e.y = a.y - b.y;
      d.x = a.x - b.x; d.y = a.y + b.y;
      v[jj] = cadd_mi(e, cmul(d, twk));
    }
    fft8<false>(v);
    if (k < DF_) {
      f32x2* dst = T + (size_t)s0 * TP;
      st_wt(dst + k, v[0]);
#pragma unroll
      for (int q = 1; q < 8; ++q) st_wt(dst + (size_t)q * 256 * TP + k, cmul(v[q], twq[q - 1]));
    }
  }
  asm volatile("" ::"v"(pf));
  STAMP(0, 5);
  STAMP_END(0, 6);
}

__global__ __launch_bounds__(256) void k_cols(f32x2* __restrict__ T, const uint16_t* __restrict__ wr,
                                              const uint16_t* __restrict__ wi) {
  __shared__ f32x2 e1[2048];
  __shared__ f32x2 e2[4][576];
  __shared__ __attribute__((aligned(16))) uint32_t wl[2048];
  const int tid = threadIdx.x, lane = tid & 63, wv = tid >> 6;
  STAMP(1, 0);
  const int q = blockIdx.x & 7, kg = blockIdx.x >> 3;
  const int c = tid & 7, a = tid >> 3;
  const int k = kg * 8 + c;
  const bool valid = k < DF_;
  f32x2* colbase = T + (size_t)(q * 256) * TP;
  const uint32_t coff = a * TP + (valid ? k : DF_ - 1);
  f32x2 v[8];
#pragma unroll
  for (int j1 = 0; j1 < 8; ++j1) v[j1] = (colbase + (size_t)j1 * 32 * TP)[coff];
  const int kb = min(8 * kg, DF_ - 8);
  u32x4 wre, wim;
  __builtin_memcpy(&wre, wr + (size_t)(q + 8 * tid) * DF_ + kb, 16);
  __builtin_memcpy(&wim, wi + (size_t)(q + 8 * tid) * DF_ + kb, 16);

  const int b = (lane >> 3) & 3, p1lo = lane >> 5, p1 = 2 * wv + p1lo;
  f32x2 twa[7], twb[7];
#pragma unroll
  for (int p = 1; p < 8; ++p) twa[p - 1] = twid((float)(a * p) * (1.0f / 256.0f));
#pragma unroll
  for (int p = 1; p < 8; ++p) twb[p - 1] = twid((float)(b * p) * (1.0f / 32.0f));
  pin(twa); pin(twb);
  __builtin_amdgcn_sched_barrier(0);
  STAMP(1, 1);

  fft8<false>(v);
  e1[tid] = v[0];
#pragma unroll
  for (int p = 1; p < 8; ++p) e1[p * 256 + tid] = cmul(v[p], twa[p - 1]);
  {
    u32x4 o0, o1;
    o0.x = __builtin_amdgcn_perm(wim.x, wre.x, 0x05040100); o0.y = __builtin_amdgcn_perm(wim.x, wre.x, 0x07060302);
    o0.z = __builtin_amdgcn_perm(wim.y, wre.y, 0x05040100); o0.w = __builtin_amdgcn_perm(wim.y, wre.y, 0x07060302);
    o1.x = __builtin_amdgcn_perm(wim.z, wre.z, 0x05040100); o1.y = __builtin_amdgcn_perm(wim.z, wre.z, 0x07060302);
    o1.z = __builtin_amdgcn_perm(wim.w, wre.w, 0x05040100); o1.w = __builtin_amdgcn_perm(wim.w, wre.w, 0x07060302);
    *(u32x4*)(wl + tid * 8) = o0;
    *(u32x4*)(wl + tid * 8 + 4) = o1;
  }
  STAMP(1, 2);
  __syncthreads();
  STAMP(1, 3);
  uint32_t wf[8];
  {
    const uint32_t* wlp = wl + (p1 + 16 * b) * 8 + min(8 * kg + c - kb, 7);
#pragma unroll
    for (int e = 0; e < 2; ++e)
#pragma unroll
      for (int mm = 0; mm < 4; ++mm) wf[e * 4 + mm] = wlp[(8 * e + 64 * mm) * 8];
  }
  f32x2* E1 = e1 + p1 * 256 + (lane & 31);
#pragma unroll
  for (int j2 = 0; j2 < 8; ++j2) v[j2] = E1[32 * j2];
  fft8<false>(v);
  f32x2* E2w = e2[wv] + p1lo * 288 + (lane & 31);
  E2w[0] = v[0];
#pragma unroll
  for (int p = 1; p < 8; ++p) E2w[p * 36] = cmul(v[p], twb[p - 1]);
  wave_sync();
  f32x2* E2r = e2[wv] + p1lo * 288 + (2 * b) * 36 + c;
#pragma unroll
  for (int e = 0; e < 2; ++e)
#pragma unroll
    for (int bb = 0; bb < 4; ++bb) v[e * 4 + bb] = E2r[e * 36 + bb * 8];
  fft4<false>(v[0], v[1], v[2], v[3]);
  fft4<false>(v[4], v[5], v[6], v[7]);
  STAMP(1, 4);
#pragma unroll
  for (int i = 0; i < 8; ++i) v[i] = cmul(v[i], bf_unpack(wf[i]));
  fft4<true>(v[0], v[1], v[2], v[3]);
  fft4<true>(v[4], v[5], v[6], v[7]);
  STAMP(1, 5);
#pragma unroll
  for (int e = 0; e < 2; ++e)
#pragma unroll
    for (int bb = 0; bb < 4; ++bb) E2r[e * 36 + bb * 8] = v[e * 4 + bb];
  wave_sync();
  v[0] = E2w[0];
#pragma unroll
  for (int p = 1; p < 8; ++p) v[p] = cmulc(E2w[p * 36], twb[p - 1]);
  fft8<true>(v);
#pragma unroll
  for (int j2 = 0; j2 < 8; ++j2) E1[32 * j2] = v[j2];
  STAMP(1, 6);
  __syncthreads();
  STAMP(1, 7);
  v[0] = e1[tid];
#pragma unroll
  for (int p = 1; p < 8; ++p) v[p] = cmulc(e1[p * 256 + tid], twa[p - 1]);
  fft8<true>(v);
  if (valid) {
#pragma unroll
    for (int j1 = 0; j1 < 8; ++j1) st_wt(colbase + (size_t)j1 * 32 * TP + coff, v[j1]);
  }
  STAMP(1, 8);
  STAMP_END(1, 9);
}

template <int N> DEV void wait_vm() { asm volatile("s_waitcnt vmcnt(%0)" ::"n"(N) : "memory"); }
#define RAW_BAR() do { asm volatile("s_waitcnt lgkmcnt(0)" ::: "memory"); __builtin_amdgcn_s_barrier(); } while (0)

DEV void glds16(const void* src, char* lds) {
  __builtin_amdgcn_global_load_lds((const unsigned*)src, (unsigned*)lds, 16, 0, 0);
}
DEV uint32_t pk_relu_bf16(uint32_t p) {
  uint32_t r;
  asm("v_pk_max_i16 %0, %1, 0" : "=v"(r) : "v"(p));
  return r;
}

typedef short v4s_t __attribute__((ext_vector_type(4)));
typedef __attribute__((address_space(3))) v4s_t* lds_v4s_ptr;
DEV bf16x8 tr_frag(const char* p0, const char* p1) {
  const v4s_t a = __builtin_amdgcn_ds_read_tr16_b64_v4i16((lds_v4s_ptr)p0);
  const v4s_t b = __builtin_amdgcn_ds_read_tr16_b64_v4i16((lds_v4s_ptr)p1);
  return __builtin_shufflevector(a, b, 0, 1, 2, 3, 4, 5, 6, 7);
}

#define SGB(mask, n) __builtin_amdgcn_sched_group_barrier(mask, n, 0)
template <int NM, int ND> DEV void interleave_mfma_ds() {
  if constexpr (NM == 6) {
    SGB(0x008, 1); SGB(0x100, 2); SGB(0x008, 1); SGB(0x100, 2); SGB(0x008, 1); SGB(0x100, 2);
    SGB(0x008, 1); SGB(0x100, 2); SGB(0x008, 2);
  } else {
    SGB(0x008, 1); SGB(0x100, 3); SGB(0x008, 1); SGB(0x100, 2); SGB(0x008, 1); SGB(0x100, 2);
    SGB(0x008, 1); SGB(0x100, 2); SGB(0x008, 1); SGB(0x100, 2);
  }
}
DEV void wait_vm_rt(int n) {
  switch (n) {
    case 0: wait_vm<0>(); break;   case 1: wait_vm<1>(); break;   case 2: wait_vm<2>(); break;
    case 3: wait_vm<3>(); break;   case 4: wait_vm<4>(); break;   case 5: wait_vm<5>(); break;
    case 6: wait_vm<6>(); break;   case 7: wait_vm<7>(); break;   case 8: wait_vm<8>(); break;
    case 9: wait_vm<9>(); break;   case 10: wait_vm<10>(); break; case 11: wait_vm<11>(); break;
    case 12: wait_vm<12>(); break; case 13: wait_vm<13>(); break; case 14: wait_vm<14>(); break;
    default: wait_vm<0>(); break;
  }
}
DEV void glds16_dev(const void* src, char* lds) {
  __builtin_amdgcn_global_load_lds((const unsigned*)src, (unsigned*)lds, 16, 0, 16);
}
DEV void lds_spin(uint32_t* w, uint32_t want) {
  for (int i = 0; i < (1 << 24); ++i) {
    const uint32_t v = __hip_atomic_load(w, __ATOMIC_RELAXED, __HIP_MEMORY_SCOPE_WORKGROUP);
    if ((uint32_t)__builtin_amdgcn_readfirstlane(v) >= want) break;
    __builtin_amdgcn_s_sleep(1);
  }
  asm volatile("" ::: "memory");
}
DEV void wait_cluster(uint32_t* lcnt, uint32_t* lflag, uint32_t* cnt, int lw, int lane) {
  if (lw == 0) {
    lds_spin(lcnt, 8u);
    for (int i = 0; i < (1 << 22); ++i) {
      const uint32_t v = __hip_atomic_load(cnt, __ATOMIC_RELAXED, __HIP_MEMORY_SCOPE_AGENT);
      if ((uint32_t)__builtin_amdgcn_readfirstlane(v) >= 16u) break;
      __builtin_amdgcn_s_sleep(2);
    }
    asm volatile("" ::: "memory");
    if (lane == 0) __hip_atomic_store(lflag, 1u, __ATOMIC_RELAXED, __HIP_MEMORY_SCOPE_WORKGROUP);
  } else {
    lds_spin(lflag, 1u);
  }
}
DEV void publish(uint32_t* lcnt, uint32_t* gcnt, int lane) {
  asm volatile("s_waitcnt vmcnt(0)" ::: "memory");
  if (lane == 0) {
    const uint32_t old = __hip_atomic_fetch_add(lcnt, 1u, __ATOMIC_RELAXED, __HIP_MEMORY_SCOPE_WORKGROUP);
    if (old == 7u) __hip_atomic_fetch_add(gcnt, 1u, __ATOMIC_RELAXED, __HIP_MEMORY_SCOPE_AGENT);
  }
}

#undef STAMP_TID
#define STAMP_TID 0
__global__ __launch_bounds__(1024) void k_tail(const f32x2* __restrict__ T, const uint32_t* __restrict__ x,
                                               uint32_t* h, uint32_t* hn, u32x2* h1,
                                               const char* __restrict__ w1, const uint16_t* __restrict__ b1,
                                               const u32x4* __restrict__ w2p, const uint16_t* __restrict__ b2,
                                               uint16_t* __restrict__ out, uint32_t* cnt) {
  constexpr int BD_OFF = 0, AD_OFF = 6 * 12288;
  constexpr int BE_OFF = 0, AE_OFF = 4 * 20480, RES_OFF = AE_OFF + 4 * 16384;
  constexpr int MISC_OFF = 163840 - 1024, B1_OFF = MISC_OFF, LC_OFF = MISC_OFF + 256, B2_OFF = MISC_OFF + 512;
  __shared__ __attribute__((aligned(16))) char lds[163840];
  const int tid = threadIdx.x, lane = tid & 63;
  const int wv = __builtin_amdgcn_readfirstlane(tid >> 6);
  const int xcd = blockIdx.x & 7, idx = blockIdx.x >> 3;
  const int cl = 2 * xcd + (idx >> 4), mi_ = idx & 15;
  const int cb = mi_, rb2 = 2 * cl + (mi_ >> 3), cb2 = mi_ & 7;
  uint32_t* cnt1 = cnt + cl * 64;
  uint32_t* cnt2 = cnt + (16 + cl) * 64;
  uint32_t* lcnt = (uint32_t*)(lds + LC_OFF);

  if (wv >= 8) {
    const int lw = wv - 8;
    const int nb = (lw < 4) ? 2 : 1, nb2 = (lw < 4) ? 3 : 2;
    const int arow = lane >> 3;
    const char* asrc = (const char*)hn + (size_t)(cl * 128 + 16 * lw + arow) * 1280 + (((lane & 7) ^ arow) << 4);
    const int id0 = lw * 64 + lane, r0 = id0 / 12, c0 = (id0 % 12) ^ (((r0 >> 3) & 1) << 1);
    const int id1 = (8 + lw) * 64 + lane, r1 = id1 / 12, c1 = (id1 % 12) ^ (((r1 >> 3) & 1) << 1);
    const char* bsrc0 = w1 + (size_t)r0 * 3072 + cb * 192 + c0 * 16;
    const char* bsrc1 = w1 + (size_t)r1 * 3072 + cb * 192 + c1 * 16;
    auto stageB = [&](int st) {
      char* dst = lds + BD_OFF + (st % 6) * 12288 + lane * 16;
      glds16(bsrc0 + (size_t)st * 64 * 3072, dst + lw * 1024);
      if (lw < 4) glds16(bsrc1 + (size_t)st * 64 * 3072, dst + (8 + lw) * 1024);
    };
    auto stageA = [&](int st) {
      char* dst = lds + AD_OFF + (st % 5) * 16384 + lane * 16;
      glds16_dev(asrc + st * 128, dst + (2 * lw) * 1024);
      glds16_dev(asrc + st * 128 + 8 * 1280, dst + (2 * lw + 1) * 1024);
    };
    if (lw == 7 && lane < 12) glds16((const u32x4*)(b1 + cb * 96) + lane, lds + B1_OFF + lane * 16);
    if (lw == 6 && lane < 10) glds16((const u32x4*)(b2 + cb2 * 80) + lane, lds + B2_OFF + lane * 16);
#pragma unroll
    for (int st = 0; st < 6; ++st) stageB(st);
    __builtin_amdgcn_s_barrier();
    wait_cluster(lcnt, lcnt + 2, cnt1, lw, lane);
#pragma unroll
    for (int st = 0; st < 5; ++st) stageA(st);
#pragma unroll
    for (int st = 0; st < 10; ++st) {
      const int ka = (st == 0) ? 8 : (st <= 6) ? 6 : (st == 7) ? 4 : (st == 8) ? 2 : 0;
      const int kb = (st <= 1) ? 0 : (st <= 5) ? st - 1 : (st == 6) ? 3 : (st == 7) ? 2 : (st == 8) ? 1 : 0;
      if (lw < 4) wait_vm_rt(ka + 2 * kb); else wait_vm_rt(ka + kb);
      __builtin_amdgcn_s_barrier();
      if (st >= 1 && st + 4 < 10) stageA(st + 4);
      if (st >= 1 && st + 5 < 10) stageB(st + 5);
    }
    __builtin_amdgcn_s_barrier();
    const u32x4* asrc2 = (const u32x4*)h1 + (size_t)rb2 * 48 * 4 * 64 + (2 * lw) * 64 + lane;
    const u32x4* bsrc2 = w2p + (size_t)cb2 * 1536 * 10 + lw * 64 + lane;
    auto stageB2 = [&](int st) {
      char* dst = lds + BE_OFF + (st % 4) * 20480 + lane * 16;
      glds16(bsrc2 + st * 1280, dst + lw * 1024);
      glds16(bsrc2 + st * 1280 + 512, dst + (8 + lw) * 1024);
      if (lw < 4) glds16(bsrc2 + st * 1280 + 1024, dst + (16 + lw) * 1024);
    };
    auto stageA2 = [&](int st) {
      char* dst = lds + AE_OFF + (st % 4) * 16384 + lane * 16;
      glds16_dev(asrc2 + (size_t)(st * 16) * 64, dst + (2 * lw) * 1024);
      glds16_dev(asrc2 + (size_t)(st * 16 + 1) * 64, dst + (2 * lw + 1) * 1024);
    };
#pragma unroll
    for (int st = 0; st < 4; ++st) stageB2(st);
    {
      const int id = lw * 64 + lane, r = id / 10, ch = id % 10;
      glds16_dev((const u32x4*)((const uint16_t*)h + (size_t)(rb2 * 64 + r) * D_ + cb2 * 80) + ch, lds + RES_OFF + id * 16);
      if (lw >= 6) {
        const int id2 = (lw + 2) * 64 + lane, r2 = id2 / 10, ch2 = id2 % 10;
        glds16_dev((const u32x4*)((const uint16_t*)h + (size_t)(rb2 * 64 + r2) * D_ + cb2 * 80) + ch2, lds + RES_OFF + id2 * 16);
      }
    }
    wait_cluster(lcnt + 1, lcnt + 3, cnt2, lw, lane);
#pragma unroll
    for (int st = 0; st < 4; ++st) stageA2(st);
#pragma unroll
    for (int st = 0; st < 12; ++st) {
      const int ka = (st == 0) ? 6 : (st <= 9) ? 4 : (st == 10) ? 2 : 0;
      const int kb = (st <= 1) ? 0 : (st == 2) ? 1 : (st <= 9) ? 2 : (st == 10) ? 1 : 0;
      if (lw < 4) wait_vm_rt(ka + 3 * kb); else wait_vm_rt(ka + 2 * kb);
      __builtin_amdgcn_s_barrier();
      if (st >= 1 && st + 3 < 12) { stageA2(st + 3); stageB2(st + 3); }
    }
    __builtin_amdgcn_s_barrier();
    (void)nb; (void)nb2;
    return;
  }

  STAMP(2, 0);
  {
    f32x2(*ex)[360] = (f32x2(*)[360])(lds + AD_OFF);
    f32x2(*xx)[384] = (f32x2(*)[384])(lds + AD_OFF + 23040);
    const int j = wv;
    const int s0 = 32 * xcd + idx;
    const int row = s0 + 256 * j;
    const int prow = s0 * 8 + j;
    f32x2 v[8];
    {
      const f32x2* src = T + (size_t)s0 * TP;
      const uint32_t kk = min(tid, DF_ - 1);
#pragma unroll
      for (int q = 0; q < 8; ++q) v[q] = (src + (size_t)q * 256 * TP)[kk];
    }
    uint32_t xd[5];
#pragma unroll
    for (int i = 0; i < 5; ++i) xd[i] = x[row * 320 + lane + 64 * i];
    if (tid < 4) lcnt[tid] = 0;

    f32x2 tw1[4], tw2[7], twq[7], twk[8];
#pragma unroll
    for (int r = 1; r < 5; ++r) tw1[r - 1] = twid((float)(lane * r) * (1.0f / 320.0f));
    const int l0 = lane & 7, rr = lane >> 3;
#pragma unroll
    for (int q = 1; q < 8; ++q) tw2[q - 1] = twid((float)(l0 * q) * (1.0f / 64.0f));
#pragma unroll
    for (int q = 1; q < 8; ++q) twq[q - 1] = twid((float)(s0 * q) * (1.0f / 2048.0f));
#pragma unroll
    for (int p = 0; p < 8; ++p) twk[p] = twid((float)(rr + 5 * l0 + 40 * p) * (1.0f / 640.0f));
    pin(tw1); pin(tw2); pin(twq); pin(twk);
    __builtin_amdgcn_sched_barrier(0);
    STAMP(2, 1);

    if (j < 6) {
#pragma unroll
      for (int q = 1; q < 8; ++q) v[q] = cmulc(v[q], twq[q - 1]);
      fft8<true>(v);
      f32x2 msk;
      msk.x = 1.0f; msk.y = (tid == 0 || tid == 320) ? 0.0f : 1.0f;
#pragma unroll
      for (int jj = 0; jj < 8; ++jj) xx[jj][tid] = v[jj] * msk;
    }
    RAW_BAR();
    asm volatile("" ::: "memory");
    f32x2* E = ex[j];
    if (lane < 40) {
      f32x2* Er = E + rr * 72;
      const f32x2* Xa = xx[j] + rr + 5 * l0;
      const f32x2* Xb = xx[j] + 320 - rr - 5 * l0;
#pragma unroll
      for (int p = 0; p < 8; ++p) {
        const f32x2 a = Xa[40 * p], b = Xb[-40 * p];
        f32x2 e, d;
        e.x = a.x + b.x; e.y = a.y - b.y;
        d.x = a.x - b.x; d.y = a.y + b.y;
        v[p] = cadd_pi(e, cmulc(d, twk[p]));
      }
      fft8<true>(v);
      Er[l0 * 9] = v[0];
#pragma unroll
      for (int a = 1; a < 8; ++a) Er[l0 * 9 + a] = cmulc(v[a], tw2[a - 1]);
      wave_sync();
#pragma unroll
      for (int q = 0; q < 8; ++q) v[q] = Er[q * 9 + l0];
      fft8<true>(v);
      wave_sync();
#pragma unroll
      for (int l1 = 0; l1 < 8; ++l1) Er[l0 + 8 * l1] = v[l1];
    }
    wave_sync();
    f32x2 u[5];
    u[0] = E[lane];
#pragma unroll
    for (int r = 1; r < 5; ++r) u[r] = cmulc(E[r * 72 + lane], tw1[r - 1]);
    fft5<true>(u);

    const f32x2 sc = {1.0f / 2621440.0f, 1.0f / 2621440.0f};
    f32x2 hv[5];
    uint32_t zp[5];
#pragma unroll
    for (int i = 0; i < 5; ++i) {
      const uint32_t hp = bf_pack(bf_unpack(xd[i]) + bf_round(u[i] * sc));
      st_wt32(h + prow * 320 + lane + 64 * i, hp);
      hv[i] = bf_unpack(hp);
    }
    row_layernorm(hv, zp);
#pragma unroll
    for (int i = 0; i < 5; ++i) st_wt32(hn + prow * 320 + lane + 64 * i, zp[i]);
    STAMP(2, 2);
    publish(lcnt, cnt1, lane);
    STAMP(2, 3);
  }
  const int g = lane >> 4, q4 = (lane & 15) >> 2, p4 = lane & 3;
  {
    const int wm = wv >> 1, wn = wv & 1;
    f32x4 acc[2][3];
#pragma unroll
    for (int mi = 0; mi < 2; ++mi)
#pragma unroll
      for (int ni = 0; ni < 3; ++ni) acc[mi][ni] = (f32x4){0.f, 0.f, 0.f, 0.f};
    const int ar = 32 * wm + (lane & 15);
    const int aoff0 = AD_OFF + ar * 128 + (((lane >> 4) ^ (ar & 7)) << 4);
    const int aoff1 = AD_OFF + ar * 128 + (((4 + (lane >> 4)) ^ (ar & 7)) << 4);
    int boff[3];
#pragma unroll
    for (int ni = 0; ni < 3; ++ni) boff[ni] = BD_OFF + (8 * g + q4) * 192 + p4 * 8 + (((3 * wn + ni) ^ (g & 1)) << 5);
    bf16x8 af0[2], bf0[3], af1[2], bf1[3];
    auto rdD = [&](bf16x8 (&af)[2], bf16x8 (&bf)[3], int st, int ks) {
      int sa = (st % 5) * 16384, sb = (st % 6) * 12288;
      asm volatile("" : "+s"(sa), "+s"(sb));
#pragma unroll
      for (int mi = 0; mi < 2; ++mi) af[mi] = *(const bf16x8*)(lds + sa + (ks ? aoff1 : aoff0) + mi * 2048);
#pragma unroll
      for (int ni = 0; ni < 3; ++ni)
        bf[ni] = tr_frag(lds + sb + boff[ni] + ks * 6144, lds + sb + boff[ni] + ks * 6144 + 768);
    };
    auto mmD = [&](const bf16x8 (&af)[2], const bf16x8 (&bf)[3]) {
#pragma unroll
      for (int mi = 0; mi < 2; ++mi)
#pragma unroll
        for (int ni = 0; ni < 3; ++ni)
          acc[mi][ni] = __builtin_amdgcn_mfma_f32_16x16x32_bf16(bf[ni], af[mi], acc[mi][ni], 0, 0, 0);
    };
    RAW_BAR();
    STAMP(2, 4);
    rdD(af0, bf0, 0, 0);
#pragma unroll
    for (int st = 0; st < 10; ++st) {
      rdD(af1, bf1, st, 1);
      mmD(af0, bf0);
      interleave_mfma_ds<6, 8>();
      __builtin_amdgcn_sched_barrier(0);
      RAW_BAR();
      __builtin_amdgcn_sched_barrier(0);
      if (st < 9) rdD(af0, bf0, st + 1, 0);
      mmD(af1, bf1);
      if (st < 9) interleave_mfma_ds<6, 8>();
      __builtin_amdgcn_sched_barrier(0);
    }
    STAMP(2, 5);
#pragma unroll
    for (int ni = 0; ni < 3; ++ni) {
      const u32x2 bb = *(const u32x2*)(lds + B1_OFF + ((3 * wn + ni) * 16 + 4 * (lane >> 4)) * 2);
      const f32x2 b01 = bf_unpack(bb.x), b23 = bf_unpack(bb.y);
      const int NT = cb * 6 + 3 * wn + ni;
      const int ks2 = NT >> 1, gg = 2 * (NT & 1) + (lane >> 5), half = (lane >> 4) & 1;
#pragma unroll
      for (int mi = 0; mi < 2; ++mi) {
        f32x2 a01 = {acc[mi][ni][0], acc[mi][ni][1]}, a23 = {acc[mi][ni][2], acc[mi][ni][3]};
        a01 = bf_unpack(pk_relu_bf16(bf_pack(bf_round(a01) + b01)));
        a23 = bf_unpack(pk_relu_bf16(bf_pack(bf_round(a23) + b23)));
        u32x2 ov;
        ov.x = bf_pack(a01 * a01); ov.y = bf_pack(a23 * a23);
        const int mt = cl * 8 + 2 * wm + mi;
        st_wt64(h1 + ((((mt >> 2) * 48 + ks2) * 4 + (mt & 3)) * 64 + (lane & 15) + 16 * gg) * 2 + half, ov);
      }
    }
    publish(lcnt + 1, cnt2, lane);
    STAMP(2, 6);
  }
  {
    const int wm = wv & 3, wk = wv >> 2;
    f32x4 acc[5];
#pragma unroll
    for (int ni = 0; ni < 5; ++ni) acc[ni] = (f32x4){0.f, 0.f, 0.f, 0.f};
    const int boff = BE_OFF + (16 * (g >> 1) + 4 * (g & 1) + q4 + 32 * wk) * 160 + p4 * 8;
    const int aoff = AE_OFF + (wk * 4 + wm) * 1024 + lane * 16;
    bf16x8 af0, bf0[5], af1, bf1[5];
    auto rdE = [&](bf16x8& af, bf16x8 (&bf)[5], int st, int kk) {
      int sa = (st % 4) * 16384, sb = (st % 4) * 20480;
      asm volatile("" : "+s"(sa), "+s"(sb));
      af = *(const bf16x8*)(lds + sa + aoff + kk * 8192);
#pragma unroll
      for (int ni = 0; ni < 5; ++ni)
        bf[ni] = tr_frag(lds + sb + boff + kk * 10240 + ni * 32, lds + sb + boff + kk * 10240 + ni * 32 + 1280);
    };
    auto mmE = [&](const bf16x8& af, const bf16x8 (&bf)[5]) {
#pragma unroll
      for (int ni = 0; ni < 5; ++ni) acc[ni] = __builtin_amdgcn_mfma_f32_16x16x32_bf16(bf[ni], af, acc[ni], 0, 0, 0);
    };
    RAW_BAR();
    STAMP(2, 7);
    rdE(af0, bf0, 0, 0);
#pragma unroll
    for (int st = 0; st < 12; ++st) {
      rdE(af1, bf1, st, 1);
      mmE(af0, bf0);
      interleave_mfma_ds<5, 11>();
      __builtin_amdgcn_sched_barrier(0);
      if (st < 11) {
        RAW_BAR();
        __builtin_amdgcn_sched_barrier(0);
        rdE(af0, bf0, st + 1, 0);
      }
      mmE(af1, bf1);
      if (st < 11) interleave_mfma_ds<5, 11>();
      __builtin_amdgcn_sched_barrier(0);
    }
    STAMP(2, 8);
    f32x4* red = (f32x4*)(lds + BE_OFF) + (wm * 5) * 64 + lane;
    if (wk == 1) {
#pragma unroll
      for (int ni = 0; ni < 5; ++ni) red[ni * 64] = acc[ni];
    }
    RAW_BAR();
    asm volatile("" ::: "memory");
    if (wk == 0) {
      const int prow = rb2 * 64 + wm * 16 + (lane & 15);
      const int mrow = (prow >> 3) + 256 * (prow & 7);
      const int ncol = cb2 * 80 + 4 * (lane >> 4);
#pragma unroll
      for (int ni = 0; ni < 5; ++ni) {
        const f32x4 o = acc[ni] + red[ni * 64];
        const u32x2 bb = *(const u32x2*)(lds + B2_OFF + (16 * ni + 4 * (lane >> 4)) * 2);
        const u32x2 rs = *(const u32x2*)(lds + RES_OFF + (wm * 16 + (lane & 15)) * 160 + (16 * ni + 4 * (lane >> 4)) * 2);
        f32x2 a01 = {o[0], o[1]}, a23 = {o[2], o[3]};
        u32x2 ov;
        ov.x = bf_pack(bf_unpack(rs.x) + bf_round(bf_round(a01) + bf_unpack(bb.x)));
        ov.y = bf_pack(bf_unpack(rs.y) + bf_round(bf_round(a23) + bf_unpack(bb.y)));
        *(u32x2*)(out + (size_t)mrow * D_ + ncol + 16 * ni) = ov;
      }
    }
  }
  STAMP_END(2, 9);
}

__global__ void k_zz_end() {}
extern "C" void kernel_launch(void* const* d_in, const int* in_sizes, int n_in, void* d_out, int out_size,
                              void* d_ws, size_t ws_size, hipStream_t stream) {
  const uint32_t* x = (const uint32_t*)d_in[0];
  const uint16_t* wr = (const uint16_t*)d_in[1];
  const uint16_t* wi = (const uint16_t*)d_in[2];
  const uint16_t* w1 = (const uint16_t*)d_in[4];
  const uint16_t* b1 = (const uint16_t*)d_in[5];
  const uint16_t* w2 = (const uint16_t*)d_in[6];
  const uint16_t* b2 = (const uint16_t*)d_in[7];
  char* ws = (char*)d_ws;
  size_t off = 0;
  f32x2* T = (f32x2*)(ws + off); off += (size_t)8 * 256 * TP * 8;
  uint32_t* h = (uint32_t*)(ws + off); off += (size_t)S_ * D_ * 2;
  uint32_t* hn = (uint32_t*)(ws + off); off += (size_t)S_ * D_ * 2;
  u32x4* h1 = (u32x4*)(ws + off); off += (size_t)S_ * F_ * 2;
  u32x4* w2p = (u32x4*)(ws + off); off += (size_t)D_ * F_ * 2;

  uint32_t* cnt = (uint32_t*)(ws + off); off += 32 * 256;
  k_fwd_rows<<<256, 512, 0, stream>>>(x, T, wr, wi, (const uint32_t*)w1, (const u32x4*)w2, w2p, cnt);
  k_cols<<<328, 256, 0, stream>>>(T, wr, wi);
  k_tail<<<256, 1024, 0, stream>>>(T, x, h, hn, (u32x2*)h1, (const char*)w1, b1, W2_MODE == 1 ? (const u32x4*)w2p : (const u32x4*)w2, b2, (uint16_t*)d_out, cnt);
}
```

```cpp
#include <hip/hip_runtime.h>
#include <hip/hip_bf16.h>
#include <stdint.h>

typedef float f32x2 __attribute__((ext_vector_type(2)));
typedef float f32x4 __attribute__((ext_vector_type(4)));
typedef short bf16x8 __attribute__((ext_vector_type(8)));
typedef unsigned int u32x4 __attribute__((ext_vector_type(4)));
typedef unsigned int u32x2 __attribute__((ext_vector_type(2)));

#define DEV __device__ __forceinline__

#define STAMP(kern, i)
#define STAMP_END(kern, i)
#define STAMP_TID 0

#define W2_MODE 1
#define WT_C
#define WT_D
#define PF_CODE 1
#define MAP_D 0
#define MAP_E 0
#define PF_SELF 1
#define PF_W 1
static constexpr int S_ = 2048, D_ = 640, F_ = 1536, DF_ = 321;
static constexpr int TP = 328;

DEV float bf_lo(uint32_t p) { return __builtin_bit_cast(float, p << 16); }
DEV float bf_hi(uint32_t p) { return __builtin_bit_cast(float, p & 0xffff0000u); }
DEV f32x2 bf_unpack(uint32_t p) { f32x2 r; r.x = bf_lo(p); r.y = bf_hi(p); return r; }
typedef __bf16 bf16x2_t __attribute__((ext_vector_type(2)));
DEV uint32_t bf_pack(f32x2 v) {
  return __builtin_bit_cast(uint32_t, __builtin_convertvector(v, bf16x2_t));
}
DEV f32x2 bf_round(f32x2 v) { return bf_unpack(bf_pack(v)); }
DEV float bfr(float f) { f32x2 v; v.x = f; v.y = f; return bf_lo(bf_pack(v)); }

DEV f32x2 cadd_mi(f32x2 b, f32x2 d) {
  f32x2 r;
  asm("v_pk_add_f32 %0, %1, %2 op_sel:[0,1] op_sel_hi:[1,0] neg_hi:[0,1]" : "=v"(r) : "v"(b), "v"(d));
  return r;
}
DEV f32x2 cadd_pi(f32x2 b, f32x2 d) {
  f32x2 r;
  asm("v_pk_add_f32 %0, %1, %2 op_sel:[0,1] op_sel_hi:[1,0] neg_lo:[0,1]" : "=v"(r) : "v"(b), "v"(d));
  return r;
}
DEV f32x2 cnadd_mi(f32x2 b, f32x2 d) {
  f32x2 r;
  asm("v_pk_add_f32 %0, %1, %2 op_sel:[0,1] op_sel_hi:[1,0] neg_lo:[1,0] neg_hi:[1,1]" : "=v"(r) : "v"(b), "v"(d));
  return r;
}
DEV f32x2 cnadd_pi(f32x2 b, f32x2 d) {
  f32x2 r;
  asm("v_pk_add_f32 %0, %1, %2 op_sel:[0,1] op_sel_hi:[1,0] neg_lo:[1,1] neg_hi:[1,0]" : "=v"(r) : "v"(b), "v"(d));
  return r;
}
DEV f32x2 cfma_mi(f32x2 s, f32x2 d, f32x2 b) {
  f32x2 r;
  asm("v_pk_fma_f32 %0, %1, %2, %3 op_sel:[0,1,0] op_sel_hi:[1,0,1] neg_hi:[0,1,0]" : "=v"(r) : "v"(s), "v"(d), "v"(b));
  return r;
}
DEV f32x2 cfma_pi(f32x2 s, f32x2 d, f32x2 b) {
  f32x2 r;
  asm("v_pk_fma_f32 %0, %1, %2, %3 op_sel:[0,1,0] op_sel_hi:[1,0,1] neg_lo:[0,1,0]" : "=v"(r) : "v"(s), "v"(d), "v"(b));
  return r;
}
DEV f32x2 pfma(f32x2 a, f32x2 b, f32x2 c) { return __builtin_elementwise_fma(a, b, c); }
DEV f32x2 cmul(f32x2 a, f32x2 w) {
  f32x2 t, r;
  asm("v_pk_mul_f32 %0, %1, %2 op_sel:[0,0] op_sel_hi:[0,1]" : "=v"(t) : "v"(a), "v"(w));
  asm("v_pk_fma_f32 %0, %1, %2, %3 op_sel:[1,1,0] op_sel_hi:[1,0,1] neg_lo:[0,1,0]" : "=v"(r) : "v"(a), "v"(w), "v"(t));
  return r;
}
DEV f32x2 cmulc(f32x2 a, f32x2 w) {
  f32x2 t, r;
  asm("v_pk_mul_f32 %0, %1, %2 op_sel:[0,0] op_sel_hi:[0,1] neg_hi:[0,1]" : "=v"(t) : "v"(a), "v"(w));
  asm("v_pk_fma_f32 %0, %1, %2, %3 op_sel:[1,1,0] op_sel_hi:[1,0,1]" : "=v"(r) : "v"(a), "v"(w), "v"(t));
  return r;
}
DEV f32x2 twid(float f) {
  f32x2 r;
  r.x = __builtin_amdgcn_cosf(f);
  r.y = __builtin_amdgcn_sinf(-f);
  return r;
}
DEV void st_wt(f32x2* p, f32x2 v) {
  __hip_atomic_store((unsigned long long*)p, __builtin_bit_cast(unsigned long long, v), __ATOMIC_RELAXED,
                     __HIP_MEMORY_SCOPE_AGENT);
}
DEV void st_wt32(uint32_t* p, uint32_t v) { __hip_atomic_store(p, v, __ATOMIC_RELAXED, __HIP_MEMORY_SCOPE_AGENT); }
DEV void st_wt64(u32x2* p, u32x2 v) {
  __hip_atomic_store((unsigned long long*)p, __builtin_bit_cast(unsigned long long, v), __ATOMIC_RELAXED,
                     __HIP_MEMORY_SCOPE_AGENT);
}
DEV void st_wt128(void* p, u32x4 v) {
  asm volatile("global_store_dwordx4 %0, %1, off sc1\n\ts_nop 1" ::"v"(p), "v"(v) : "memory");
}
#define ST_H(p, v) st_wt32(p, v)
#define ST_H1(p, v) st_wt64(p, v)
template <int N> DEV void pin(f32x2 (&a)[N]) {
#pragma unroll
  for (int i = 0; i < N; ++i) asm volatile("" : "+v"(a[i]));
}
template <bool INV> DEV f32x2 add_rot(f32x2 b, f32x2 d) { return INV ? cadd_pi(b, d) : cadd_mi(b, d); }
template <bool INV> DEV f32x2 sub_rot(f32x2 b, f32x2 d) { return INV ? cadd_mi(b, d) : cadd_pi(b, d); }

template <bool INV> DEV void fft4(f32x2& a0, f32x2& a1, f32x2& a2, f32x2& a3) {
  f32x2 b0 = a0 + a2, b1 = a0 - a2, b2 = a1 + a3, d = a1 - a3;
  a0 = b0 + b2; a2 = b0 - b2;
  a1 = add_rot<INV>(b1, d); a3 = sub_rot<INV>(b1, d);
}
template <bool INV> DEV void fft8(f32x2 (&v)[8]) {
  const f32x2 r = {0.70710678118654752440f, 0.70710678118654752440f};
  const f32x2 nr = {-0.70710678118654752440f, -0.70710678118654752440f};
  f32x2 e0 = v[0] + v[4], e1 = v[1] + v[5], e2 = v[2] + v[6], e3 = v[3] + v[7];
  f32x2 o0 = v[0] - v[4], o1 = v[1] - v[5], o2 = v[2] - v[6], o3 = v[3] - v[7];
  fft4<INV>(e0, e1, e2, e3);
  f32x2 p1 = INV ? cadd_pi(o1, o1) : cadd_mi(o1, o1);
  f32x2 p3 = INV ? cnadd_pi(o3, o3) : cnadd_mi(o3, o3);
  f32x2 s = p1 + p3, d = p1 - p3;
  f32x2 b0 = add_rot<INV>(o0, o2), b1 = sub_rot<INV>(o0, o2);
  v[0] = e0; v[2] = e1; v[4] = e2; v[6] = e3;
  v[1] = pfma(r, s, b0); v[5] = pfma(nr, s, b0);
  v[3] = INV ? cfma_pi(r, d, b1) : cfma_mi(r, d, b1);
  v[7] = INV ? cfma_mi(r, d, b1) : cfma_pi(r, d, b1);
}
template <bool INV> DEV void fft5(f32x2 (&v)[5]) {
  const f32x2 c1 = {0.30901699437494742410f, 0.30901699437494742410f};
  const f32x2 c2 = {-0.80901699437494742410f, -0.80901699437494742410f};
  const f32x2 s1 = {0.95105651629515357212f, 0.95105651629515357212f};
  const f32x2 s2 = {0.58778525229247312917f, 0.58778525229247312917f};
  const f32x2 ns1 = {-0.95105651629515357212f, -0.95105651629515357212f};
  f32x2 t1 = v[1] + v[4], t2 = v[2] + v[3], t3 = v[1] - v[4], t4 = v[2] - v[3];
  f32x2 a1 = pfma(c2, t2, pfma(c1, t1, v[0]));
  f32x2 a2 = pfma(c1, t2, pfma(c2, t1, v[0]));
  f32x2 b1 = pfma(s2, t4, s1 * t3);
  f32x2 b2 = pfma(ns1, t4, s2 * t3);
  v[0] = v[0] + t1 + t2;
  v[1] = add_rot<INV>(a1, b1); v[4] = sub_rot<INV>(a1, b1);
  v[2] = add_rot<INV>(a2, b2); v[3] = sub_rot<INV>(a2, b2);
}

template <int CTRL> DEV float dppmov(float v) {
  return __builtin_bit_cast(float, __builtin_amdgcn_update_dpp(0, __builtin_bit_cast(int, v), CTRL, 0xf, 0xf, false));
}
DEV float wave_sum(float v) {
  v += dppmov<0xB1>(v);
  v += dppmov<0x4E>(v);
  v += dppmov<0x141>(v);
  v += dppmov<0x140>(v);
  int iv = __builtin_bit_cast(int, v);
  float a = __builtin_bit_cast(float, __builtin_amdgcn_readlane(iv, 0));
  float b = __builtin_bit_cast(float, __builtin_amdgcn_readlane(iv, 16));
  float c = __builtin_bit_cast(float, __builtin_amdgcn_readlane(iv, 32));
  float d = __builtin_bit_cast(float, __builtin_amdgcn_readlane(iv, 48));
  return (a + b) + (c + d);
}
DEV void wave_sync() {
  __builtin_amdgcn_fence(__ATOMIC_RELEASE, "wavefront");
  __builtin_amdgcn_wave_barrier();
  __builtin_amdgcn_fence(__ATOMIC_ACQUIRE, "wavefront");
}

DEV void row_layernorm(const f32x2 (&xv)[5], uint32_t (&zp)[5]) {
  f32x2 s2 = (xv[0] + xv[1]) + (xv[2] + xv[3]) + xv[4];
  const float mean = wave_sum(s2.x + s2.y) * (1.0f / 640.0f);
  const f32x2 nm = {-mean, -mean};
  f32x2 q2 = {0.f, 0.f};
#pragma unroll
  for (int i = 0; i < 5; ++i) { f32x2 d = xv[i] + nm; q2 = pfma(d, d, q2); }
  const float var = wave_sum(q2.x + q2.y) * (1.0f / 640.0f);
  f32x2 mv; mv.x = mean; mv.y = var;
  mv = bf_round(mv);
  const float den = bfr(sqrtf(bfr(mv.y + 1e-5f)));
  const float rden = 1.0f / den;
  const f32x2 nmb = {-mv.x, -mv.x}, rd = {rden, rden};
#pragma unroll
  for (int i = 0; i < 5; ++i) zp[i] = bf_pack(bf_round(xv[i] + nmb) * rd);
}

__global__ __launch_bounds__(512) void k_fwd_rows(const uint32_t* __restrict__ x, f32x2* __restrict__ T,
                                                  const uint16_t* __restrict__ wr, const uint16_t* __restrict__ wi,
                                                  const uint32_t* __restrict__ w1, const u32x4* __restrict__ w2,
                                                  u32x4* __restrict__ w2p, uint32_t* __restrict__ cnt) {
  __shared__ f32x2 ex[8][360];
  __shared__ f32x2 zz[8][320];
  const int tid = threadIdx.x, lane = tid & 63, j = tid >> 6;
  STAMP(0, 0);
  uint32_t pfs = 0;
  if (j == 0 && lane < 40) {
    unsigned long long pc;
    asm volatile("s_getpc_b64 %0" : "=s"(pc));
    pfs = *(const uint32_t*)((pc & ~127ull) + 128 + lane * 128);
  }
  if (blockIdx.x == 0 && tid < 32) st_wt32(cnt + tid * 64, 0u);
  const int s0 = 32 * (blockIdx.x & 7) + (blockIdx.x >> 3);
  const int row = s0 + 256 * j;
  uint32_t xd[5];
#pragma unroll
  for (int i = 0; i < 5; ++i) xd[i] = x[row * 320 + lane + 64 * i];
  uint32_t pf = 0;
  {
    const char* pa = nullptr;
    if (lane < 12) {
      const int t = (blockIdx.x & 7) + 8 * (8 * (blockIdx.x >> 3) + j);
      pa = (const char*)(((lane < 6) ? wr : wi) + (t * DF_ + min(64 * (lane % 6), DF_ - 2)));
      pa = (const char*)((uintptr_t)pa & ~(uintptr_t)3);
    } else if (lane >= 56) {
      pa = (const char*)(w1 + (blockIdx.x * 60 + min(j * 8 + (lane - 56), 59)) * 32);
    }
    else if (lane < 18) {
      unsigned long long c0, c1;
      asm volatile("s_getpc_b64 s[40:41]\n\ts_add_u32 s40, s40, _Z6k_colsPDv2_fPKtS2_PKDv4_jPS3_@rel32@lo+4\n\ts_addc_u32 s41, s41, _Z6k_colsPDv2_fPKtS2_PKDv4_jPS3_@rel32@hi+12\n\ts_mov_b64 %0, s[40:41]" : "=s"(c0) :: "s40", "s41", "scc");
      asm volatile("s_getpc_b64 s[40:41]\n\ts_add_u32 s40, s40, _Z8k_zz_endv@rel32@lo+4\n\ts_addc_u32 s41, s41, _Z8k_zz_endv@rel32@hi+12\n\ts_mov_b64 %0, s[40:41]" : "=s"(c1) :: "s40", "s41", "scc");
      const long long len = (long long)(c1 - c0);
      const int li = ((blockIdx.x >> 3) * 8 + j) * 2 + (lane - 16);
      if (lane >= 16 && len > 0 && len < 65536 && li * 128 < len) pa = (const char*)(c0 + li * 128);
    }
    if (pa) pf = *(const uint32_t*)pa;
  }
  const int wid = min((int)blockIdx.x * 512 + tid, 122879);
  const u32x4 wchunk = w2[wid];

  f32x2 tw1[4], tw2[7], twq[7];
#pragma unroll
  for (int r = 1; r < 5; ++r) tw1[r - 1] = twid((float)(lane * r) * (1.0f / 320.0f));
  const int l0 = lane & 7, rr = lane >> 3;
#pragma unroll
  for (int q = 1; q < 8; ++q) tw2[q - 1] = twid((float)(l0 * q) * (1.0f / 64.0f));
#pragma unroll
  for (int q = 1; q < 8; ++q) twq[q - 1] = twid((float)(s0 * q) * (1.0f / 2048.0f));
  f32x2 twk_[1] = {twid((float)tid * (1.0f / 640.0f))};
  pin(tw1); pin(tw2); pin(twq); pin(twk_);
  const f32x2 twk = twk_[0];
  __builtin_amdgcn_sched_barrier(0);
  STAMP(0, 1);

  f32x2 u[5];
  {
    f32x2 xv[5];
    uint32_t zp[5];
#pragma unroll
    for (int i = 0; i < 5; ++i) xv[i] = bf_unpack(xd[i]);
    row_layernorm(xv, zp);
#pragma unroll
    for (int i = 0; i < 5; ++i) u[i] = bf_unpack(zp[i]);
  }
  STAMP(0, 2);

  f32x2* E = ex[j];
  fft5<false>(u);
  E[lane] = u[0];
#pragma unroll
  for (int r = 1; r < 5; ++r) E[r * 72 + lane] = cmul(u[r], tw1[r - 1]);
  wave_sync();
  if (lane < 40) {
    f32x2 v[8];
    f32x2* Er = E + rr * 72;
#pragma unroll
    for (int l1 = 0; l1 < 8; ++l1) v[l1] = Er[l0 + 8 * l1];
    fft8<false>(v);
    wave_sync();
    Er[l0] = v[0];
#pragma unroll
    for (int q = 1; q < 8; ++q) Er[q * 9 + l0] = cmul(v[q], tw2[q - 1]);
    wave_sync();
#pragma unroll
    for (int a = 0; a < 8; ++a) v[a] = Er[l0 * 9 + a];
    fft8<false>(v);
    f32x2* Z = zz[j] + rr + 5 * l0;
#pragma unroll
    for (int p = 0; p < 8; ++p) Z[40 * p] = v[p];
  }
  if (blockIdx.x < 240) {
    const int wk_ = wid / 80, wc_ = wid % 80;
    const int rho = (wk_ & ~12) | ((wk_ & 4) << 1) | ((wk_ & 8) >> 1);
    st_wt128(w2p + ((wc_ / 10) * 1536 + rho) * 10 + (wc_ % 10), wchunk);
  }
  STAMP(0, 3);
  __syncthreads();
  STAMP(0, 4);
  if (j < 6) {
    const int k = tid;
    const int ka = (k >= 320) ? 0 : k, kb = (k == 0 || k >= 320) ? 0 : 320 - k;
    f32x2 v[8];
#pragma unroll
    for (int jj = 0; jj < 8; ++jj) {
      const f32x2 a = zz[jj][ka], b = zz[jj][kb];
      f32x2 e, d;
      e.x = a.x + b.x; e.y = a.y - b.y;
      d.x = a.x - b.x; d.y = a.y + b.y;
      v[jj] = cadd_mi(e, cmul(d, twk));
    }
    fft8<false>(v);
    if (k < DF_) {
      f32x2* dst = T + (size_t)s0 * TP;
      st_wt(dst + k, v[0]);
#pragma unroll
      for (int q = 1; q < 8; ++q) st_wt(dst + (size_t)q * 256 * TP + k, cmul(v[q], twq[q - 1]));
    }
  }
  asm volatile("" ::"v"(pf));
  asm volatile("" ::"v"(pfs));
  STAMP(0, 5);
  STAMP_END(0, 6);
}

__global__ __launch_bounds__(256) void k_cols(f32x2* __restrict__ T, const uint16_t* __restrict__ wr,
                                              const uint16_t* __restrict__ wi, const u32x4* __restrict__ w2,
                                              u32x4* __restrict__ w2p) {
  __shared__ f32x2 e1[2048];
  __shared__ f32x2 e2[4][576];
  __shared__ __attribute__((aligned(16))) uint32_t wl[2048];
  const int tid = threadIdx.x, lane = tid & 63, wv = tid >> 6;
  STAMP(1, 0);
  const int q = blockIdx.x & 7, kg = blockIdx.x >> 3;
  const int c = tid & 7, a = tid >> 3;
  const int k = kg * 8 + c;
  const bool valid = k < DF_;
  f32x2* colbase = T + (size_t)(q * 256) * TP;
  const uint32_t coff = a * TP + (valid ? k : DF_ - 1);
  f32x2 v[8];
#pragma unroll
  for (int j1 = 0; j1 < 8; ++j1) v[j1] = (colbase + (size_t)j1 * 32 * TP)[coff];
  const int kb = min(8 * kg, DF_ - 8);
  u32x4 wre, wim;
  __builtin_memcpy(&wre, wr + (size_t)(q + 8 * tid) * DF_ + kb, 16);
  __builtin_memcpy(&wim, wi + (size_t)(q + 8 * tid) * DF_ + kb, 16);

  const int b = (lane >> 3) & 3, p1lo = lane >> 5, p1 = 2 * wv + p1lo;
  f32x2 twa[7], twb[7];
#pragma unroll
  for (int p = 1; p < 8; ++p) twa[p - 1] = twid((float)(a * p) * (1.0f / 256.0f));
#pragma unroll
  for (int p = 1; p < 8; ++p) twb[p - 1] = twid((float)(b * p) * (1.0f / 32.0f));
  pin(twa); pin(twb);
  __builtin_amdgcn_sched_barrier(0);
  STAMP(1, 1);

  fft8<false>(v);
  e1[tid] = v[0];
#pragma unroll
  for (int p = 1; p < 8; ++p) e1[p * 256 + tid] = cmul(v[p], twa[p - 1]);
  {
    u32x4 o0, o1;
    o0.x = __builtin_amdgcn_perm(wim.x, wre.x, 0x05040100); o0.y = __builtin_amdgcn_perm(wim.x, wre.x, 0x07060302);
    o0.z = __builtin_amdgcn_perm(wim.y, wre.y, 0x05040100); o0.w = __builtin_amdgcn_perm(wim.y, wre.y, 0x07060302);
    o1.x = __builtin_amdgcn_perm(wim.z, wre.z, 0x05040100); o1.y = __builtin_amdgcn_perm(wim.z, wre.z, 0x07060302);
    o1.z = __builtin_amdgcn_perm(wim.w, wre.w, 0x05040100); o1.w = __builtin_amdgcn_perm(wim.w, wre.w, 0x07060302);
    *(u32x4*)(wl + tid * 8) = o0;
    *(u32x4*)(wl + tid * 8 + 4) = o1;
  }
  STAMP(1, 2);
  __syncthreads();
  STAMP(1, 3);
  uint32_t wf[8];
  {
    const uint32_t* wlp = wl + (p1 + 16 * b) * 8 + min(8 * kg + c - kb, 7);
#pragma unroll
    for (int e = 0; e < 2; ++e)
#pragma unroll
      for (int mm = 0; mm < 4; ++mm) wf[e * 4 + mm] = wlp[(8 * e + 64 * mm) * 8];
  }
  f32x2* E1 = e1 + p1 * 256 + (lane & 31);
#pragma unroll
  for (int j2 = 0; j2 < 8; ++j2) v[j2] = E1[32 * j2];
  fft8<false>(v);
  f32x2* E2w = e2[wv] + p1lo * 288 + (lane & 31);
  E2w[0] = v[0];
#pragma unroll
  for (int p = 1; p < 8; ++p) E2w[p * 36] = cmul(v[p], twb[p - 1]);
  wave_sync();
  f32x2* E2r = e2[wv] + p1lo * 288 + (2 * b) * 36 + c;
#pragma unroll
  for (int e = 0; e < 2; ++e)
#pragma unroll
    for (int bb = 0; bb < 4; ++bb) v[e * 4 + bb] = E2r[e * 36 + bb * 8];
  fft4<false>(v[0], v[1], v[2], v[3]);
  fft4<false>(v[4], v[5], v[6], v[7]);
  STAMP(1, 4);
#pragma unroll
  for (int i = 0; i < 8; ++i) v[i] = cmul(v[i], bf_unpack(wf[i]));
  fft4<true>(v[0], v[1], v[2], v[3]);
  fft4<true>(v[4], v[5], v[6], v[7]);
  STAMP(1, 5);
#pragma unroll
  for (int e = 0; e < 2; ++e)
#pragma unroll
    for (int bb = 0; bb < 4; ++bb) E2r[e * 36 + bb * 8] = v[e * 4 + bb];
  wave_sync();
  v[0] = E2w[0];
#pragma unroll
  for (int p = 1; p < 8; ++p) v[p] = cmulc(E2w[p * 36], twb[p - 1]);
  fft8<true>(v);
#pragma unroll
  for (int j2 = 0; j2 < 8; ++j2) E1[32 * j2] = v[j2];
  STAMP(1, 6);
  __syncthreads();
  STAMP(1, 7);
  v[0] = e1[tid];
#pragma unroll
  for (int p = 1; p < 8; ++p) v[p] = cmulc(e1[p * 256 + tid], twa[p - 1]);
  fft8<true>(v);
  if (valid) {
#pragma unroll
    for (int j1 = 0; j1 < 8; ++j1) st_wt(colbase + (size_t)j1 * 32 * TP + coff, v[j1]);
  }
  STAMP(1, 8);
  STAMP_END(1, 9);
}

template <int N> DEV void wait_vm() { asm volatile("s_waitcnt vmcnt(%0)" ::"n"(N) : "memory"); }
#define RAW_BAR() do { asm volatile("s_waitcnt lgkmcnt(0)" ::: "memory"); __builtin_amdgcn_s_barrier(); } while (0)

DEV void glds16(const void* src, char* lds) {
  __builtin_amdgcn_global_load_lds((const unsigned*)src, (unsigned*)lds, 16, 0, 0);
}
DEV uint32_t pk_relu_bf16(uint32_t p) {
  uint32_t r;
  asm("v_pk_max_i16 %0, %1, 0" : "=v"(r) : "v"(p));
  return r;
}

typedef short v4s_t __attribute__((ext_vector_type(4)));
typedef __attribute__((address_space(3))) v4s_t* lds_v4s_ptr;
DEV bf16x8 tr_frag(const char* p0, const char* p1) {
  const v4s_t a = __builtin_amdgcn_ds_read_tr16_b64_v4i16((lds_v4s_ptr)p0);
  const v4s_t b = __builtin_amdgcn_ds_read_tr16_b64_v4i16((lds_v4s_ptr)p1);
  return __builtin_shufflevector(a, b, 0, 1, 2, 3, 4, 5, 6, 7);
}

#define SGB(mask, n) __builtin_amdgcn_sched_group_barrier(mask, n, 0)
template <int NM, int ND> DEV void interleave_mfma_ds() {
  if constexpr (NM == 6) {
    SGB(0x008, 1); SGB(0x100, 2); SGB(0x008, 1); SGB(0x100, 2); SGB(0x008, 1); SGB(0x100, 2);
    SGB(0x008, 1); SGB(0x100, 2); SGB(0x008, 2);
  } else {
    SGB(0x008, 1); SGB(0x100, 3); SGB(0x008, 1); SGB(0x100, 2); SGB(0x008, 1); SGB(0x100, 2);
    SGB(0x008, 1); SGB(0x100, 2); SGB(0x008, 1); SGB(0x100, 2);
  }
}
DEV void wait_vm_rt(int n) {
  switch (n) {
    case 0: wait_vm<0>(); break;   case 1: wait_vm<1>(); break;   case 2: wait_vm<2>(); break;
    case 3: wait_vm<3>(); break;   case 4: wait_vm<4>(); break;   case 5: wait_vm<5>(); break;
    case 6: wait_vm<6>(); break;   case 7: wait_vm<7>(); break;   case 8: wait_vm<8>(); break;
    case 9: wait_vm<9>(); break;   case 10: wait_vm<10>(); break; case 11: wait_vm<11>(); break;
    case 12: wait_vm<12>(); break; case 13: wait_vm<13>(); break; case 14: wait_vm<14>(); break;
    default: wait_vm<0>(); break;
  }
}
DEV void glds16_dev(const void* src, char* lds) {
  __builtin_amdgcn_global_load_lds((const unsigned*)src, (unsigned*)lds, 16, 0, 16);
}
DEV void lds_spin(uint32_t* w, uint32_t want) {
  for (int i = 0; i < (1 << 24); ++i) {
    const uint32_t v = __hip_atomic_load(w, __ATOMIC_RELAXED, __HIP_MEMORY_SCOPE_WORKGROUP);
    if ((uint32_t)__builtin_amdgcn_readfirstlane(v) >= want) break;
    __builtin_amdgcn_s_sleep(1);
  }
  asm volatile("" ::: "memory");
}
DEV void wait_cluster(uint32_t* lcnt, uint32_t* lflag, uint32_t* cnt, int lw, int lane) {
  if (lw == 0) {
    lds_spin(lcnt, 8u);
    for (int i = 0; i < (1 << 22); ++i) {
      const uint32_t v = __hip_atomic_load(cnt, __ATOMIC_RELAXED, __HIP_MEMORY_SCOPE_AGENT);
      if ((uint32_t)__builtin_amdgcn_readfirstlane(v) >= 16u) break;
      __builtin_amdgcn_s_sleep(2);
    }
    asm volatile("" ::: "memory");
    if (lane == 0) __hip_atomic_store(lflag, 1u, __ATOMIC_RELAXED, __HIP_MEMORY_SCOPE_WORKGROUP);
  } else {
    lds_spin(lflag, 1u);
  }
}
DEV void publish(uint32_t* lcnt, uint32_t* gcnt, int lane) {
  asm volatile("s_waitcnt vmcnt(0)" ::: "memory");
  if (lane == 0) {
    const uint32_t old = __hip_atomic_fetch_add(lcnt, 1u, __ATOMIC_RELAXED, __HIP_MEMORY_SCOPE_WORKGROUP);
    if (old == 7u) __hip_atomic_fetch_add(gcnt, 1u, __ATOMIC_RELAXED, __HIP_MEMORY_SCOPE_AGENT);
  }
}

#undef STAMP_TID
#define STAMP_TID 0
__global__ __launch_bounds__(1024) void k_tail(const f32x2* __restrict__ T, const uint32_t* __restrict__ x,
                                               uint32_t* h, uint32_t* hn, u32x2* h1,
                                               const char* __restrict__ w1, const uint16_t* __restrict__ b1,
                                               const u32x4* __restrict__ w2p, const uint16_t* __restrict__ b2,
                                               uint16_t* __restrict__ out, uint32_t* cnt) {
  constexpr int BD_OFF = 0, AD_OFF = 6 * 12288;
  constexpr int BE_OFF = 0, AE_OFF = 4 * 20480, RES_OFF = AE_OFF + 4 * 16384;
  constexpr int MISC_OFF = 163840 - 1024, B1_OFF = MISC_OFF, LC_OFF = MISC_OFF + 256, B2_OFF = MISC_OFF + 512;
  __shared__ __attribute__((aligned(16))) char lds[163840];
  const int tid = threadIdx.x, lane = tid & 63;
  const int wv = __builtin_amdgcn_readfirstlane(tid >> 6);
  const int xcd = blockIdx.x & 7, idx = blockIdx.x >> 3;
  const int cl = 2 * xcd + (idx >> 4), mi_ = idx & 15;
  const int cb = mi_, rb2 = 2 * cl + (mi_ >> 3), cb2 = mi_ & 7;
  uint32_t* cnt1 = cnt + cl * 64;
  uint32_t* cnt2 = cnt + (16 + cl) * 64;
  uint32_t* lcnt = (uint32_t*)(lds + LC_OFF);

  if (wv >= 8) {
    const int lw = wv - 8;
    const int nb = (lw < 4) ? 2 : 1, nb2 = (lw < 4) ? 3 : 2;
    const int arow = lane >> 3;
    const char* asrc = (const char*)hn + (size_t)(cl * 128 + 16 * lw + arow) * 1280 + (((lane & 7) ^ arow) << 4);
    const int id0 = lw * 64 + lane, r0 = id0 / 12, c0 = (id0 % 12) ^ (((r0 >> 3) & 1) << 1);
    const int id1 = (8 + lw) * 64 + lane, r1 = id1 / 12, c1 = (id1 % 12) ^ (((r1 >> 3) & 1) << 1);
    const char* bsrc0 = w1 + (size_t)r0 * 3072 + cb * 192 + c0 * 16;
    const char* bsrc1 = w1 + (size_t)r1 * 3072 + cb * 192 + c1 * 16;
    auto stageB = [&](int st) {
      char* dst = lds + BD_OFF + (st % 6) * 12288 + lane * 16;
      glds16(bsrc0 + (size_t)st * 64 * 3072, dst + lw * 1024);
      if (lw < 4) glds16(bsrc1 + (size_t)st * 64 * 3072, dst + (8 + lw) * 1024);
    };
    auto stageA = [&](int st) {
      char* dst = lds + AD_OFF + (st % 5) * 16384 + lane * 16;
      glds16_dev(asrc + st * 128, dst + (2 * lw) * 1024);
      glds16_dev(asrc + st * 128 + 8 * 1280, dst + (2 * lw + 1) * 1024);
    };
    if (lw == 7 && lane < 12) glds16((const u32x4*)(b1 + cb * 96) + lane, lds + B1_OFF + lane * 16);
    if (lw == 6 && lane < 10) glds16((const u32x4*)(b2 + cb2 * 80) + lane, lds + B2_OFF + lane * 16);
#pragma unroll
    for (int st = 0; st < 6; ++st) stageB(st);
    __builtin_amdgcn_s_barrier();
    wait_cluster(lcnt, lcnt + 2, cnt1, lw, lane);
#pragma unroll
    for (int st = 0; st < 5; ++st) stageA(st);
#pragma unroll
    for (int st = 0; st < 10; ++st) {
      const int ka = (st == 0) ? 8 : (st <= 6) ? 6 : (st == 7) ? 4 : (st == 8) ? 2 : 0;
      const int kb = (st <= 1) ? 0 : (st <= 5) ? st - 1 : (st == 6) ? 3 : (st == 7) ? 2 : (st == 8) ? 1 : 0;
      if (lw < 4) wait_vm_rt(ka + 2 * kb); else wait_vm_rt(ka + kb);
      __builtin_amdgcn_s_barrier();
      if (st >= 1 && st + 4 < 10) stageA(st + 4);
      if (st >= 1 && st + 5 < 10) stageB(st + 5);
    }
    __builtin_amdgcn_s_barrier();
    const u32x4* asrc2 = (const u32x4*)h1 + (size_t)rb2 * 48 * 4 * 64 + (2 * lw) * 64 + lane;
    const u32x4* bsrc2 = w2p + (size_t)cb2 * 1536 * 10 + lw * 64 + lane;
    auto stageB2 = [&](int st) {
      char* dst = lds + BE_OFF + (st % 4) * 20480 + lane * 16;
      glds16(bsrc2 + st * 1280, dst + lw * 1024);
      glds16(bsrc2 + st * 1280 + 512, dst + (8 + lw) * 1024);
      if (lw < 4) glds16(bsrc2 + st * 1280 + 1024, dst + (16 + lw) * 1024);
    };
    auto stageA2 = [&](int st) {
      char* dst = lds + AE_OFF + (st % 4) * 16384 + lane * 16;
      glds16_dev(asrc2 + (size_t)(st * 16) * 64, dst + (2 * lw) * 1024);
      glds16_dev(asrc2 + (size_t)(st * 16 + 1) * 64, dst + (2 * lw + 1) * 1024);
    };
#pragma unroll
    for (int st = 0; st < 4; ++st) stageB2(st);
    {
      const int id = lw * 64 + lane, r = id / 10, ch = id % 10;
      glds16_dev((const u32x4*)((const uint16_t*)h + (size_t)(rb2 * 64 + r) * D_ + cb2 * 80) + ch, lds + RES_OFF + id * 16);
      if (lw >= 6) {
        const int id2 = (lw + 2) * 64 + lane, r2 = id2 / 10, ch2 = id2 % 10;
        glds16_dev((const u32x4*)((const uint16_t*)h + (size_t)(rb2 * 64 + r2) * D_ + cb2 * 80) + ch2, lds + RES_OFF + id2 * 16);
      }
    }
    wait_cluster(lcnt + 1, lcnt + 3, cnt2, lw, lane);
#pragma unroll
    for (int st = 0; st < 4; ++st) stageA2(st);
#pragma unroll
    for (int st = 0; st < 12; ++st) {
      const int ka = (st == 0) ? 6 : (st <= 9) ? 4 : (st == 10) ? 2 : 0;
      const int kb = (st <= 1) ? 0 : (st == 2) ? 1 : (st <= 9) ? 2 : (st == 10) ? 1 : 0;
      if (lw < 4) wait_vm_rt(ka + 3 * kb); else wait_vm_rt(ka + 2 * kb);
      __builtin_amdgcn_s_barrier();
      if (st >= 1 && st + 3 < 12) { stageA2(st + 3); stageB2(st + 3); }
    }
    __builtin_amdgcn_s_barrier();
    (void)nb; (void)nb2;
    return;
  }

  STAMP(2, 0);
  {
    f32x2(*ex)[360] = (f32x2(*)[360])(lds + AD_OFF);
    f32x2(*xx)[384] = (f32x2(*)[384])(lds + AD_OFF + 23040);
    const int j = wv;
    const int s0 = 32 * xcd + idx;
    const int row = s0 + 256 * j;
    const int prow = s0 * 8 + j;
    f32x2 v[8];
    {
      const f32x2* src = T + (size_t)s0 * TP;
      const uint32_t kk = min(tid, DF_ - 1);
#pragma unroll
      for (int q = 0; q < 8; ++q) v[q] = (src + (size_t)q * 256 * TP)[kk];
    }
    uint32_t xd[5];
#pragma unroll
    for (int i = 0; i < 5; ++i) xd[i] = x[row * 320 + lane + 64 * i];
    if (tid < 4) lcnt[tid] = 0;

    f32x2 tw1[4], tw2[7], twq[7], twk[8];
#pragma unroll
    for (int r = 1; r < 5; ++r) tw1[r - 1] = twid((float)(lane * r) * (1.0f / 320.0f));
    const int l0 = lane & 7, rr = lane >> 3;
#pragma unroll
    for (int q = 1; q < 8; ++q) tw2[q - 1] = twid((float)(l0 * q) * (1.0f / 64.0f));
#pragma unroll
    for (int q = 1; q < 8; ++q) twq[q - 1] = twid((float)(s0 * q) * (1.0f / 2048.0f));
#pragma unroll
    for (int p = 0; p < 8; ++p) twk[p] = twid((float)(rr + 5 * l0 + 40 * p) * (1.0f / 640.0f));
    pin(tw1); pin(tw2); pin(twq); pin(twk);
    __builtin_amdgcn_sched_barrier(0);
    STAMP(2, 1);

    if (j < 6) {
#pragma unroll
      for (int q = 1; q < 8; ++q) v[q] = cmulc(v[q], twq[q - 1]);
      fft8<true>(v);
      f32x2 msk;
      msk.x = 1.0f; msk.y = (tid == 0 || tid == 320) ? 0.0f : 1.0f;
#pragma unroll
      for (int jj = 0; jj < 8; ++jj) xx[jj][tid] = v[jj] * msk;
    }
    RAW_BAR();
    asm volatile("" ::: "memory");
    f32x2* E = ex[j];
    if (lane < 40) {
      f32x2* Er = E + rr * 72;
      const f32x2* Xa = xx[j] + rr + 5 * l0;
      const f32x2* Xb = xx[j] + 320 - rr - 5 * l0;
#pragma unroll
      for (int p = 0; p < 8; ++p) {
        const f32x2 a = Xa[40 * p], b = Xb[-40 * p];
        f32x2 e, d;
        e.x = a.x + b.x; e.y = a.y - b.y;
        d.x = a.x - b.x; d.y = a.y + b.y;
        v[p] = cadd_pi(e, cmulc(d, twk[p]));
      }
      fft8<true>(v);
      Er[l0 * 9] = v[0];
#pragma unroll
      for (int a = 1; a < 8; ++a) Er[l0 * 9 + a] = cmulc(v[a], tw2[a - 1]);
      wave_sync();
#pragma unroll
      for (int q = 0; q < 8; ++q) v[q] = Er[q * 9 + l0];
      fft8<true>(v);
      wave_sync();
#pragma unroll
      for (int l1 = 0; l1 < 8; ++l1) Er[l0 + 8 * l1] = v[l1];
    }
    wave_sync();
    f32x2 u[5];
    u[0] = E[lane];
#pragma unroll
    for (int r = 1; r < 5; ++r) u[r] = cmulc(E[r * 72 + lane], tw1[r - 1]);
    fft5<true>(u);

    const f32x2 sc = {1.0f / 2621440.0f, 1.0f / 2621440.0f};
    f32x2 hv[5];
    uint32_t zp[5];
#pragma unroll
    for (int i = 0; i < 5; ++i) {
      const uint32_t hp = bf_pack(bf_unpack(xd[i]) + bf_round(u[i] * sc));
      st_wt32(h + prow * 320 + lane + 64 * i, hp);
      hv[i] = bf_unpack(hp);
    }
    row_layernorm(hv, zp);
#pragma unroll
    for (int i = 0; i < 5; ++i) st_wt32(hn + prow * 320 + lane + 64 * i, zp[i]);
    STAMP(2, 2);
    publish(lcnt, cnt1, lane);
    STAMP(2, 3);
  }
  const int g = lane >> 4, q4 = (lane & 15) >> 2, p4 = lane & 3;
  {
    const int wm = wv >> 1, wn = wv & 1;
    f32x4 acc[2][3];
#pragma unroll
    for (int mi = 0; mi < 2; ++mi)
#pragma unroll
      for (int ni = 0; ni < 3; ++ni) acc[mi][ni] = (f32x4){0.f, 0.f, 0.f, 0.f};
    const int ar = 32 * wm + (lane & 15);
    const int aoff0 = AD_OFF + ar * 128 + (((lane >> 4) ^ (ar & 7)) << 4);
    const int aoff1 = AD_OFF + ar * 128 + (((4 + (lane >> 4)) ^ (ar & 7)) << 4);
    int boff[3];
#pragma unroll
    for (int ni = 0; ni < 3; ++ni) boff[ni] = BD_OFF + (8 * g + q4) * 192 + p4 * 8 + (((3 * wn + ni) ^ (g & 1)) << 5);
    bf16x8 af0[2], bf0[3], af1[2], bf1[3];
    auto rdD = [&](bf16x8 (&af)[2], bf16x8 (&bf)[3], int st, int ks) {
      int sa = (st % 5) * 16384, sb = (st % 6) * 12288;
      asm volatile("" : "+s"(sa), "+s"(sb));
#pragma unroll
      for (int mi = 0; mi < 2; ++mi) af[mi] = *(const bf16x8*)(lds + sa + (ks ? aoff1 : aoff0) + mi * 2048);
#pragma unroll
      for (int ni = 0; ni < 3; ++ni)
        bf[ni] = tr_frag(lds + sb + boff[ni] + ks * 6144, lds + sb + boff[ni] + ks * 6144 + 768);
    };
    auto mmD = [&](const bf16x8 (&af)[2], const bf16x8 (&bf)[3]) {
#pragma unroll
      for (int mi = 0; mi < 2; ++mi)
#pragma unroll
        for (int ni = 0; ni < 3; ++ni)
          acc[mi][ni] = __builtin_amdgcn_mfma_f32_16x16x32_bf16(bf[ni], af[mi], acc[mi][ni], 0, 0, 0);
    };
    RAW_BAR();
    STAMP(2, 4);
    rdD(af0, bf0, 0, 0);
#pragma unroll
    for (int st = 0; st < 10; ++st) {
      rdD(af1, bf1, st, 1);
      mmD(af0, bf0);
      interleave_mfma_ds<6, 8>();
      __builtin_amdgcn_sched_barrier(0);
      RAW_BAR();
      __builtin_amdgcn_sched_barrier(0);
      if (st < 9) rdD(af0, bf0, st + 1, 0);
      mmD(af1, bf1);
      if (st < 9) interleave_mfma_ds<6, 8>();
      __builtin_amdgcn_sched_barrier(0);
    }
    STAMP(2, 5);
#pragma unroll
    for (int ni = 0; ni < 3; ++ni) {
      const u32x2 bb = *(const u32x2*)(lds + B1_OFF + ((3 * wn + ni) * 16 + 4 * (lane >> 4)) * 2);
      const f32x2 b01 = bf_unpack(bb.x), b23 = bf_unpack(bb.y);
      const int NT = cb * 6 + 3 * wn + ni;
      const int ks2 = NT >> 1, gg = 2 * (NT & 1) + (lane >> 5), half = (lane >> 4) & 1;
#pragma unroll
      for (int mi = 0; mi < 2; ++mi) {
        f32x2 a01 = {acc[mi][ni][0], acc[mi][ni][1]}, a23 = {acc[mi][ni][2], acc[mi][ni][3]};
        a01 = bf_unpack(pk_relu_bf16(bf_pack(bf_round(a01) + b01)));
        a23 = bf_unpack(pk_relu_bf16(bf_pack(bf_round(a23) + b23)));
        u32x2 ov;
        ov.x = bf_pack(a01 * a01); ov.y = bf_pack(a23 * a23);
        const int mt = cl * 8 + 2 * wm + mi;
        st_wt64(h1 + ((((mt >> 2) * 48 + ks2) * 4 + (mt & 3)) * 64 + (lane & 15) + 16 * gg) * 2 + half, ov);
      }
    }
    publish(lcnt + 1, cnt2, lane);
    STAMP(2, 6);
  }
  {
    const int wm = wv & 3, wk = wv >> 2;
    f32x4 acc[5];
#pragma unroll
    for (int ni = 0; ni < 5; ++ni) acc[ni] = (f32x4){0.f, 0.f, 0.f, 0.f};
    const int boff = BE_OFF + (16 * (g >> 1) + 4 * (g & 1) + q4 + 32 * wk) * 160 + p4 * 8;
    const int aoff = AE_OFF + (wk * 4 + wm) * 1024 + lane * 16;
    bf16x8 af0, bf0[5], af1, bf1[5];
    auto rdE = [&](bf16x8& af, bf16x8 (&bf)[5], int st, int kk) {
      int sa = (st % 4) * 16384, sb = (st % 4) * 20480;
      asm volatile("" : "+s"(sa), "+s"(sb));
      af = *(const bf16x8*)(lds + sa + aoff + kk * 8192);
#pragma unroll
      for (int ni = 0; ni < 5; ++ni)
        bf[ni] = tr_frag(lds + sb + boff + kk * 10240 + ni * 32, lds + sb + boff + kk * 10240 + ni * 32 + 1280);
    };
    auto mmE = [&](const bf16x8& af, const bf16x8 (&bf)[5]) {
#pragma unroll
      for (int ni = 0; ni < 5; ++ni) acc[ni] = __builtin_amdgcn_mfma_f32_16x16x32_bf16(bf[ni], af, acc[ni], 0, 0, 0);
    };
    RAW_BAR();
    STAMP(2, 7);
    rdE(af0, bf0, 0, 0);
#pragma unroll
    for (int st = 0; st < 12; ++st) {
      rdE(af1, bf1, st, 1);
      mmE(af0, bf0);
      interleave_mfma_ds<5, 11>();
      __builtin_amdgcn_sched_barrier(0);
      if (st < 11) {
        RAW_BAR();
        __builtin_amdgcn_sched_barrier(0);
        rdE(af0, bf0, st + 1, 0);
      }
      mmE(af1, bf1);
      if (st < 11) interleave_mfma_ds<5, 11>();
      __builtin_amdgcn_sched_barrier(0);
    }
    STAMP(2, 8);
    f32x4* red = (f32x4*)(lds + BE_OFF) + (wm * 5) * 64 + lane;
    if (wk == 1) {
#pragma unroll
      for (int ni = 0; ni < 5; ++ni) red[ni * 64] = acc[ni];
    }
    RAW_BAR();
    asm volatile("" ::: "memory");
    if (wk == 0) {
      const int prow = rb2 * 64 + wm * 16 + (lane & 15);
      const int mrow = (prow >> 3) + 256 * (prow & 7);
      const int ncol = cb2 * 80 + 4 * (lane >> 4);
#pragma unroll
      for (int ni = 0; ni < 5; ++ni) {
        const f32x4 o = acc[ni] + red[ni * 64];
        const u32x2 bb = *(const u32x2*)(lds + B2_OFF + (16 * ni + 4 * (lane >> 4)) * 2);
        const u32x2 rs = *(const u32x2*)(lds + RES_OFF + (wm * 16 + (lane & 15)) * 160 + (16 * ni + 4 * (lane >> 4)) * 2);
        f32x2 a01 = {o[0], o[1]}, a23 = {o[2], o[3]};
        u32x2 ov;
        ov.x = bf_pack(bf_unpack(rs.x) + bf_round(bf_round(a01) + bf_unpack(bb.x)));
        ov.y = bf_pack(bf_unpack(rs.y) + bf_round(bf_round(a23) + bf_unpack(bb.y)));
        *(u32x2*)(out + (size_t)mrow * D_ + ncol + 16 * ni) = ov;
      }
    }
  }
  STAMP_END(2, 9);
}

__global__ void k_zz_end() {}
extern "C" void kernel_launch(void* const* d_in, const int* in_sizes, int n_in, void* d_out, int out_size,
                              void* d_ws, size_t ws_size, hipStream_t stream) {
  const uint32_t* x = (const uint32_t*)d_in[0];
  const uint16_t* wr = (const uint16_t*)d_in[1];
  const uint16_t* wi = (const uint16_t*)d_in[2];
  const uint16_t* w1 = (const uint16_t*)d_in[4];
  const uint16_t* b1 = (const uint16_t*)d_in[5];
  const uint16_t* w2 = (const uint16_t*)d_in[6];
  const uint16_t* b2 = (const uint16_t*)d_in[7];
  char* ws = (char*)d_ws;
  size_t off = 0;
  f32x2* T = (f32x2*)(ws + off); off += (size_t)8 * 256 * TP * 8;
  uint32_t* h = (uint32_t*)(ws + off); off += (size_t)S_ * D_ * 2;
  uint32_t* hn = (uint32_t*)(ws + off); off += (size_t)S_ * D_ * 2;
  u32x4* h1 = (u32x4*)(ws + off); off += (size_t)S_ * F_ * 2;
  u32x4* w2p = (u32x4*)(ws + off); off += (size_t)D_ * F_ * 2;

  uint32_t* cnt = (uint32_t*)(ws + off); off += 32 * 256;
  k_fwd_rows<<<256, 512, 0, stream>>>(x, T, wr, wi, (const uint32_t*)w1, (const u32x4*)w2, w2p, cnt);
  k_cols<<<328, 256, 0, stream>>>(T, wr, wi, (const u32x4*)w2, w2p);
  k_tail<<<256, 1024, 0, stream>>>(T, x, h, hn, (u32x2*)h1, (const char*)w1, b1, W2_MODE >= 1 ? (const u32x4*)w2p : (const u32x4*)w2, b2, (uint16_t*)d_out, cnt);
}
```

```cpp
#include <hip/hip_runtime.h>
#include <hip/hip_bf16.h>
#include <stdint.h>

typedef float f32x2 __attribute__((ext_vector_type(2)));
typedef float f32x4 __attribute__((ext_vector_type(4)));
typedef short bf16x8 __attribute__((ext_vector_type(8)));
typedef unsigned int u32x4 __attribute__((ext_vector_type(4)));
typedef unsigned int u32x2 __attribute__((ext_vector_type(2)));

#define DEV __device__ __forceinline__

#define STAMP(kern, i)
#define STAMP_END(kern, i)
#define STAMP_TID 0

#define W2_MODE 1
#define WT_C
#define WT_D
#define PF_CODE 1
#define MAP_D 0
#define MAP_E 0
#define PF_SELF 1
#define PF_W 1
static constexpr int S_ = 2048, D_ = 640, F_ = 1536, DF_ = 321;
static constexpr int TP = 328;

DEV float bf_lo(uint32_t p) { return __builtin_bit_cast(float, p << 16); }
DEV float bf_hi(uint32_t p) { return __builtin_bit_cast(float, p & 0xffff0000u); }
DEV f32x2 bf_unpack(uint32_t p) { f32x2 r; r.x = bf_lo(p); r.y = bf_hi(p); return r; }
typedef __bf16 bf16x2_t __attribute__((ext_vector_type(2)));
DEV uint32_t bf_pack(f32x2 v) {
  return __builtin_bit_cast(uint32_t, __builtin_convertvector(v, bf16x2_t));
}
DEV f32x2 bf_round(f32x2 v) { return bf_unpack(bf_pack(v)); }
DEV float bfr(float f) { f32x2 v; v.x = f; v.y = f; return bf_lo(bf_pack(v)); }

DEV f32x2 cadd_mi(f32x2 b, f32x2 d) {
  f32x2 r;
  asm("v_pk_add_f32 %0, %1, %2 op_sel:[0,1] op_sel_hi:[1,0] neg_hi:[0,1]" : "=v"(r) : "v"(b), "v"(d));
  return r;
}
DEV f32x2 cadd_pi(f32x2 b, f32x2 d) {
  f32x2 r;
  asm("v_pk_add_f32 %0, %1, %2 op_sel:[0,1] op_sel_hi:[1,0] neg_lo:[0,1]" : "=v"(r) : "v"(b), "v"(d));
  return r;
}
DEV f32x2 cnadd_mi(f32x2 b, f32x2 d) {
  f32x2 r;
  asm("v_pk_add_f32 %0, %1, %2 op_sel:[0,1] op_sel_hi:[1,0] neg_lo:[1,0] neg_hi:[1,1]" : "=v"(r) : "v"(b), "v"(d));
  return r;
}
DEV f32x2 cnadd_pi(f32x2 b, f32x2 d) {
  f32x2 r;
  asm("v_pk_add_f32 %0, %1, %2 op_sel:[0,1] op_sel_hi:[1,0] neg_lo:[1,1] neg_hi:[1,0]" : "=v"(r) : "v"(b), "v"(d));
  return r;
}
DEV f32x2 cfma_mi(f32x2 s, f32x2 d, f32x2 b) {
  f32x2 r;
  asm("v_pk_fma_f32 %0, %1, %2, %3 op_sel:[0,1,0] op_sel_hi:[1,0,1] neg_hi:[0,1,0]" : "=v"(r) : "v"(s), "v"(d), "v"(b));
  return r;
}
DEV f32x2 cfma_pi(f32x2 s, f32x2 d, f32x2 b) {
  f32x2 r;
  asm("v_pk_fma_f32 %0, %1, %2, %3 op_sel:[0,1,0] op_sel_hi:[1,0,1] neg_lo:[0,1,0]" : "=v"(r) : "v"(s), "v"(d), "v"(b));
  return r;
}
DEV f32x2 pfma(f32x2 a, f32x2 b, f32x2 c) { return __builtin_elementwise_fma(a, b, c); }
DEV f32x2 cmul(f32x2 a, f32x2 w) {
  f32x2 t, r;
  asm("v_pk_mul_f32 %0, %1, %2 op_sel:[0,0] op_sel_hi:[0,1]" : "=v"(t) : "v"(a), "v"(w));
  asm("v_pk_fma_f32 %0, %1, %2, %3 op_sel:[1,1,0] op_sel_hi:[1,0,1] neg_lo:[0,1,0]" : "=v"(r) : "v"(a), "v"(w), "v"(t));
  return r;
}
DEV f32x2 cmulc(f32x2 a, f32x2 w) {
  f32x2 t, r;
  asm("v_pk_mul_f32 %0, %1, %2 op_sel:[0,0] op_sel_hi:[0,1] neg_hi:[0,1]" : "=v"(t) : "v"(a), "v"(w));
  asm("v_pk_fma_f32 %0, %1, %2, %3 op_sel:[1,1,0] op_sel_hi:[1,0,1]" : "=v"(r) : "v"(a), "v"(w), "v"(t));
  return r;
}
DEV f32x2 twid(float f) {
  f32x2 r;
  r.x = __builtin_amdgcn_cosf(f);
  r.y = __builtin_amdgcn_sinf(-f);
  return r;
}
DEV void st_wt(f32x2* p, f32x2 v) {
  __hip_atomic_store((unsigned long long*)p, __builtin_bit_cast(unsigned long long, v), __ATOMIC_RELAXED,
                     __HIP_MEMORY_SCOPE_AGENT);
}
DEV void st_wt32(uint32_t* p, uint32_t v) { __hip_atomic_store(p, v, __ATOMIC_RELAXED, __HIP_MEMORY_SCOPE_AGENT); }
DEV void st_wt64(u32x2* p, u32x2 v) {
  __hip_atomic_store((unsigned long long*)p, __builtin_bit_cast(unsigned long long, v), __ATOMIC_RELAXED,
                     __HIP_MEMORY_SCOPE_AGENT);
}
DEV void st_wt128(void* p, u32x4 v) {
  asm volatile("global_store_dwordx4 %0, %1, off sc1\n\ts_nop 1" ::"v"(p), "v"(v) : "memory");
}
#define ST_H(p, v) st_wt32(p, v)
#define ST_H1(p, v) st_wt64(p, v)
template <int N> DEV void pin(f32x2 (&a)[N]) {
#pragma unroll
  for (int i = 0; i < N; ++i) asm volatile("" : "+v"(a[i]));
}
template <bool INV> DEV f32x2 add_rot(f32x2 b, f32x2 d) { return INV ? cadd_pi(b, d) : cadd_mi(b, d); }
template <bool INV> DEV f32x2 sub_rot(f32x2 b, f32x2 d) { return INV ? cadd_mi(b, d) : cadd_pi(b, d); }

template <bool INV> DEV void fft4(f32x2& a0, f32x2& a1, f32x2& a2, f32x2& a3) {
  f32x2 b0 = a0 + a2, b1 = a0 - a2, b2 = a1 + a3, d = a1 - a3;
  a0 = b0 + b2; a2 = b0 - b2;
  a1 = add_rot<INV>(b1, d); a3 = sub_rot<INV>(b1, d);
}
template <bool INV> DEV void fft8(f32x2 (&v)[8]) {
  const f32x2 r = {0.70710678118654752440f, 0.70710678118654752440f};
  const f32x2 nr = {-0.70710678118654752440f, -0.70710678118654752440f};
  f32x2 e0 = v[0] + v[4], e1 = v[1] + v[5], e2 = v[2] + v[6], e3 = v[3] + v[7];
  f32x2 o0 = v[0] - v[4], o1 = v[1] - v[5], o2 = v[2] - v[6], o3 = v[3] - v[7];
  fft4<INV>(e0, e1, e2, e3);
  f32x2 p1 = INV ? cadd_pi(o1, o1) : cadd_mi(o1, o1);
  f32x2 p3 = INV ? cnadd_pi(o3, o3) : cnadd_mi(o3, o3);
  f32x2 s = p1 + p3, d = p1 - p3;
  f32x2 b0 = add_rot<INV>(o0, o2), b1 = sub_rot<INV>(o0, o2);
  v[0] = e0; v[2] = e1; v[4] = e2; v[6] = e3;
  v[1] = pfma(r, s, b0); v[5] = pfma(nr, s, b0);
  v[3] = INV ? cfma_pi(r, d, b1) : cfma_mi(r, d, b1);
  v[7] = INV ? cfma_mi(r, d, b1) : cfma_pi(r, d, b1);
}
template <bool INV> DEV void fft5(f32x2 (&v)[5]) {
  const f32x2 c1 = {0.30901699437494742410f, 0.30901699437494742410f};
  const f32x2 c2 = {-0.80901699437494742410f, -0.80901699437494742410f};
  const f32x2 s1 = {0.95105651629515357212f, 0.95105651629515357212f};
  const f32x2 s2 = {0.58778525229247312917f, 0.58778525229247312917f};
  const f32x2 ns1 = {-0.95105651629515357212f, -0.95105651629515357212f};
  f32x2 t1 = v[1] + v[4], t2 = v[2] + v[3], t3 = v[1] - v[4], t4 = v[2] - v[3];
  f32x2 a1 = pfma(c2, t2, pfma(c1, t1, v[0]));
  f32x2 a2 = pfma(c1, t2, pfma(c2, t1, v[0]));
  f32x2 b1 = pfma(s2, t4, s1 * t3);
  f32x2 b2 = pfma(ns1, t4, s2 * t3);
  v[0] = v[0] + t1 + t2;
  v[1] = add_rot<INV>(a1, b1); v[4] = sub_rot<INV>(a1, b1);
  v[2] = add_rot<INV>(a2, b2); v[3] = sub_rot<INV>(a2, b2);
}

template <int CTRL> DEV float dppmov(float v) {
  return __builtin_bit_cast(float, __builtin_amdgcn_update_dpp(0, __builtin_bit_cast(int, v), CTRL, 0xf, 0xf, false));
}
DEV float wave_sum(float v) {
  v += dppmov<0xB1>(v);
  v += dppmov<0x4E>(v);
  v += dppmov<0x141>(v);
  v += dppmov<0x140>(v);
  int iv = __builtin_bit_cast(int, v);
  float a = __builtin_bit_cast(float, __builtin_amdgcn_readlane(iv, 0));
  float b = __builtin_bit_cast(float, __builtin_amdgcn_readlane(iv, 16));
  float c = __builtin_bit_cast(float, __builtin_amdgcn_readlane(iv, 32));
  float d = __builtin_bit_cast(float, __builtin_amdgcn_readlane(iv, 48));
  return (a + b) + (c + d);
}
DEV void wave_sync() {
  __builtin_amdgcn_fence(__ATOMIC_RELEASE, "wavefront");
  __builtin_amdgcn_wave_barrier();
  __builtin_amdgcn_fence(__ATOMIC_ACQUIRE, "wavefront");
}

DEV void row_layernorm(const f32x2 (&xv)[5], uint32_t (&zp)[5]) {
  f32x2 s2 = (xv[0] + xv[1]) + (xv[2] + xv[3]) + xv[4];
  const float mean = wave_sum(s2.x + s2.y) * (1.0f / 640.0f);
  const f32x2 nm = {-mean, -mean};
  f32x2 q2 = {0.f, 0.f};
#pragma unroll
  for (int i = 0; i < 5; ++i) { f32x2 d = xv[i] + nm; q2 = pfma(d, d, q2); }
  const float var = wave_sum(q2.x + q2.y) * (1.0f / 640.0f);
  f32x2 mv; mv.x = mean; mv.y = var;
  mv = bf_round(mv);
  const float den = bfr(sqrtf(bfr(mv.y + 1e-5f)));
  const float rden = 1.0f / den;
  const f32x2 nmb = {-mv.x, -mv.x}, rd = {rden, rden};
#pragma unroll
  for (int i = 0; i < 5; ++i) zp[i] = bf_pack(bf_round(xv[i] + nmb) * rd);
}

__global__ __launch_bounds__(512) void k_fwd_rows(const uint32_t* __restrict__ x, f32x2* __restrict__ T,
                                                  const uint16_t* __restrict__ wr, const uint16_t* __restrict__ wi,
                                                  const uint32_t* __restrict__ w1, const u32x4* __restrict__ w2,
                                                  u32x4* __restrict__ w2p, uint32_t* __restrict__ cnt) {
  __shared__ f32x2 ex[8][360];
  __shared__ f32x2 zz[8][320];
  const int tid = threadIdx.x, lane = tid & 63, j = tid >> 6;
  STAMP(0, 0);
  uint32_t pfs = 0;
  if (j == 0 && lane < 40) {
    unsigned long long pc;
    asm volatile("s_getpc_b64 %0" : "=s"(pc));
    pfs = *(const uint32_t*)((pc & ~127ull) + 128 + lane * 128);
  }
  if (blockIdx.x == 0 && tid < 32) st_wt32(cnt + tid * 64, 0u);
  const int s0 = 32 * (blockIdx.x & 7) + (blockIdx.x >> 3);
  const int row = s0 + 256 * j;
  uint32_t xd[5];
#pragma unroll
  for (int i = 0; i < 5; ++i) xd[i] = x[row * 320 + lane + 64 * i];
  uint32_t pf = 0;
  {
    const char* pa = nullptr;
    if (lane < 12) {
      const int t = (blockIdx.x & 7) + 8 * (8 * (blockIdx.x >> 3) + j);
      pa = (const char*)(((lane < 6) ? wr : wi) + (t * DF_ + min(64 * (lane % 6), DF_ - 2)));
      pa = (const char*)((uintptr_t)pa & ~(uintptr_t)3);
    } else if (lane >= 56) {
      pa = (const char*)(w1 + (blockIdx.x * 60 + min(j * 8 + (lane - 56), 59)) * 32);
    }
    else if (lane < 18) {
      unsigned long long c0, c1;
      asm volatile("s_getpc_b64 s[40:41]\n\ts_add_u32 s40, s40, _Z6k_colsPDv2_fPKtS2_PKDv4_jPS3_@rel32@lo+4\n\ts_addc_u32 s41, s41, _Z6k_colsPDv2_fPKtS2_PKDv4_jPS3_@rel32@hi+12\n\ts_mov_b64 %0, s[40:41]" : "=s"(c0) :: "s40", "s41", "scc");
      asm volatile("s_getpc_b64 s[40:41]\n\ts_add_u32 s40, s40, _Z8k_zz_endv@rel32@lo+4\n\ts_addc_u32 s41, s41, _Z8k_zz_endv@rel32@hi+12\n\ts_mov_b64 %0, s[40:41]" : "=s"(c1) :: "s40", "s41", "scc");
      const long long len = (long long)(c1 - c0);
      const int li = ((blockIdx.x >> 3) * 8 + j) * 2 + (lane - 16);
      if (lane >= 16 && len > 0 && len < 65536 && li * 128 < len) pa = (const char*)(c0 + li * 128);
    }
    if (pa) pf = *(const uint32_t*)pa;
  }
  const int wid = min((int)blockIdx.x * 512 + tid, 122879);
  const u32x4 wchunk = w2[wid];

  f32x2 tw1[4], tw2[7], twq[7];
#pragma unroll
  for (int r = 1; r < 5; ++r) tw1[r - 1] = twid((float)(lane * r) * (1.0f / 320.0f));
  const int l0 = lane & 7, rr = lane >> 3;
#pragma unroll
  for (int q = 1; q < 8; ++q) tw2[q - 1] = twid((float)(l0 * q) * (1.0f / 64.0f));
#pragma unroll
  for (int q = 1; q < 8; ++q) twq[q - 1] = twid((float)(s0 * q) * (1.0f / 2048.0f));
  f32x2 twk_[1] = {twid((float)tid * (1.0f / 640.0f))};
  pin(tw1); pin(tw2); pin(twq); pin(twk_);
  const f32x2 twk = twk_[0];
  __builtin_amdgcn_sched_barrier(0);
  STAMP(0, 1);

  f32x2 u[5];
  {
    f32x2 xv[5];
    uint32_t zp[5];
#pragma unroll
    for (int i = 0; i < 5; ++i) xv[i] = bf_unpack(xd[i]);
    row_layernorm(xv, zp);
#pragma unroll
    for (int i = 0; i < 5; ++i) u[i] = bf_unpack(zp[i]);
  }
  STAMP(0, 2);

  f32x2* E = ex[j];
  fft5<false>(u);
  E[lane] = u[0];
#pragma unroll
  for (int r = 1; r < 5; ++r) E[r * 72 + lane] = cmul(u[r], tw1[r - 1]);
  wave_sync();
  if (lane < 40) {
    f32x2 v[8];
    f32x2* Er = E + rr * 72;
#pragma unroll
    for (int l1 = 0; l1 < 8; ++l1) v[l1] = Er[l0 + 8 * l1];
    fft8<false>(v);
    wave_sync();
    Er[l0] = v[0];
#pragma unroll
    for (int q = 1; q < 8; ++q) Er[q * 9 + l0] = cmul(v[q], tw2[q - 1]);
    wave_sync();
#pragma unroll
    for (int a = 0; a < 8; ++a) v[a] = Er[l0 * 9 + a];
    fft8<false>(v);
    f32x2* Z = zz[j] + rr + 5 * l0;
#pragma unroll
    for (int p = 0; p < 8; ++p) Z[40 * p] = v[p];
  }
  if (blockIdx.x < 240) {
    const int wk_ = wid / 80, wc_ = wid % 80;
    const int rho = (wk_ & ~12) | ((wk_ & 4) << 1) | ((wk_ & 8) >> 1);
    st_wt128(w2p + ((wc_ / 10) * 1536 + rho) * 10 + (wc_ % 10), wchunk);
  }
  STAMP(0, 3);
  __syncthreads();
  STAMP(0, 4);
  if (j < 6) {
    const int k = tid;
    const int ka = (k >= 320) ? 0 : k, kb = (k == 0 || k >= 320) ? 0 : 320 - k;
    f32x2 v[8];
#pragma unroll
    for (int jj = 0; jj < 8; ++jj) {
      const f32x2 a = zz[jj][ka], b = zz[jj][kb];
      f32x2 e, d;
      e.x = a.x + b.x; e.y = a.y - b.y;
      d.x = a.x - b.x; d.y = a.y + b.y;
      v[jj] = cadd_mi(e, cmul(d, twk));
    }
    fft8<false>(v);
    if (k < DF_) {
      f32x2* dst = T + (size_t)s0 * TP;
      st_wt(dst + k, v[0]);
#pragma unroll
      for (int q = 1; q < 8; ++q) st_wt(dst + (size_t)q * 256 * TP + k, cmul(v[q], twq[q - 1]));
    }
  }
  asm volatile("" ::"v"(pf));
  asm volatile("" ::"v"(pfs));
  STAMP(0, 5);
  STAMP_END(0, 6);
}

__global__ __launch_bounds__(256) void k_cols(f32x2* __restrict__ T, const uint16_t* __restrict__ wr,
                                              const uint16_t* __restrict__ wi, const u32x4* __restrict__ w2,
                                              u32x4* __restrict__ w2p) {
  __shared__ f32x2 e1[2048];
  __shared__ f32x2 e2[4][576];
  __shared__ __attribute__((aligned(16))) uint32_t wl[2048];
  const int tid = threadIdx.x, lane = tid & 63, wv = tid >> 6;
  STAMP(1, 0);
  const int q = blockIdx.x & 7, kg = blockIdx.x >> 3;
  const int c = tid & 7, a = tid >> 3;
  const int k = kg * 8 + c;
  const bool valid = k < DF_;
  f32x2* colbase = T + (size_t)(q * 256) * TP;
  const uint32_t coff = a * TP + (valid ? k : DF_ - 1);
  f32x2 v[8];
#pragma unroll
  for (int j1 = 0; j1 < 8; ++j1) v[j1] = (colbase + (size_t)j1 * 32 * TP)[coff];
  const int kb = min(8 * kg, DF_ - 8);
  u32x4 wre, wim;
  __builtin_memcpy(&wre, wr + (size_t)(q + 8 * tid) * DF_ + kb, 16);
  __builtin_memcpy(&wim, wi + (size_t)(q + 8 * tid) * DF_ + kb, 16);

  const int b = (lane >> 3) & 3, p1lo = lane >> 5, p1 = 2 * wv + p1lo;
  f32x2 twa[7], twb[7];
#pragma unroll
  for (int p = 1; p < 8; ++p) twa[p - 1] = twid((float)(a * p) * (1.0f / 256.0f));
#pragma unroll
  for (int p = 1; p < 8; ++p) twb[p - 1] = twid((float)(b * p) * (1.0f / 32.0f));
  pin(twa); pin(twb);
  __builtin_amdgcn_sched_barrier(0);
  STAMP(1, 1);

  fft8<false>(v);
  e1[tid] = v[0];
#pragma unroll
  for (int p = 1; p < 8; ++p) e1[p * 256 + tid] = cmul(v[p], twa[p - 1]);
  {
    u32x4 o0, o1;
    o0.x = __builtin_amdgcn_perm(wim.x, wre.x, 0x05040100); o0.y = __builtin_amdgcn_perm(wim.x, wre.x, 0x07060302);
    o0.z = __builtin_amdgcn_perm(wim.y, wre.y, 0x05040100); o0.w = __builtin_amdgcn_perm(wim.y, wre.y, 0x07060302);
    o1.x = __builtin_amdgcn_perm(wim.z, wre.z, 0x05040100); o1.y = __builtin_amdgcn_perm(wim.z, wre.z, 0x07060302);
    o1.z = __builtin_amdgcn_perm(wim.w, wre.w, 0x05040100); o1.w = __builtin_amdgcn_perm(wim.w, wre.w, 0x07060302);
    *(u32x4*)(wl + tid * 8) = o0;
    *(u32x4*)(wl + tid * 8 + 4) = o1;
  }
  STAMP(1, 2);
  __syncthreads();
  STAMP(1, 3);
  uint32_t wf[8];
  {
    const uint32_t* wlp = wl + (p1 + 16 * b) * 8 + min(8 * kg + c - kb, 7);
#pragma unroll
    for (int e = 0; e < 2; ++e)
#pragma unroll
      for (int mm = 0; mm < 4; ++mm) wf[e * 4 + mm] = wlp[(8 * e + 64 * mm) * 8];
  }
  f32x2* E1 = e1 + p1 * 256 + (lane & 31);
#pragma unroll
  for (int j2 = 0; j2 < 8; ++j2) v[j2] = E1[32 * j2];
  fft8<false>(v);
  f32x2* E2w = e2[wv] + p1lo * 288 + (lane & 31);
  E2w[0] = v[0];
#pragma unroll
  for (int p = 1; p < 8; ++p) E2w[p * 36] = cmul(v[p], twb[p - 1]);
  wave_sync();
  f32x2* E2r = e2[wv] + p1lo * 288 + (2 * b) * 36 + c;
#pragma unroll
  for (int e = 0; e < 2; ++e)
#pragma unroll
    for (int bb = 0; bb < 4; ++bb) v[e * 4 + bb] = E2r[e * 36 + bb * 8];
  fft4<false>(v[0], v[1], v[2], v[3]);
  fft4<false>(v[4], v[5], v[6], v[7]);
  STAMP(1, 4);
#pragma unroll
  for (int i = 0; i < 8; ++i) v[i] = cmul(v[i], bf_unpack(wf[i]));
  fft4<true>(v[0], v[1], v[2], v[3]);
  fft4<true>(v[4], v[5], v[6], v[7]);
  STAMP(1, 5);
#pragma unroll
  for (int e = 0; e < 2; ++e)
#pragma unroll
    for (int bb = 0; bb < 4; ++bb) E2r[e * 36 + bb * 8] = v[e * 4 + bb];
  wave_sync();
  v[0] = E2w[0];
#pragma unroll
  for (int p = 1; p < 8; ++p) v[p] = cmulc(E2w[p * 36], twb[p - 1]);
  fft8<true>(v);
#pragma unroll
  for (int j2 = 0; j2 < 8; ++j2) E1[32 * j2] = v[j2];
  STAMP(1, 6);
  __syncthreads();
  STAMP(1, 7);
  v[0] = e1[tid];
#pragma unroll
  for (int p = 1; p < 8; ++p) v[p] = cmulc(e1[p * 256 + tid], twa[p - 1]);
  fft8<true>(v);
  if (valid) {
#pragma unroll
    for (int j1 = 0; j1 < 8; ++j1) st_wt(colbase + (size_t)j1 * 32 * TP + coff, v[j1]);
  }
  STAMP(1, 8);
  STAMP_END(1, 9);
}

template <int N> DEV void wait_vm() { asm volatile("s_waitcnt vmcnt(%0)" ::"n"(N) : "memory"); }
#define RAW_BAR() do { asm volatile("s_waitcnt lgkmcnt(0)" ::: "memory"); __builtin_amdgcn_s_barrier(); } while (0)

DEV void glds16(const void* src, char* lds) {
  __builtin_amdgcn_global_load_lds((const unsigned*)src, (unsigned*)lds, 16, 0, 0);
}
DEV uint32_t pk_relu_bf16(uint32_t p) {
  uint32_t r;
  asm("v_pk_max_i16 %0, %1, 0" : "=v"(r) : "v"(p));
  return r;
}

typedef short v4s_t __attribute__((ext_vector_type(4)));
typedef __attribute__((address_space(3))) v4s_t* lds_v4s_ptr;
DEV bf16x8 tr_frag(const char* p0, const char* p1) {
  const v4s_t a = __builtin_amdgcn_ds_read_tr16_b64_v4i16((lds_v4s_ptr)p0);
  const v4s_t b = __builtin_amdgcn_ds_read_tr16_b64_v4i16((lds_v4s_ptr)p1);
  return __builtin_shufflevector(a, b, 0, 1, 2, 3, 4, 5, 6, 7);
}

#define SGB(mask, n) __builtin_amdgcn_sched_group_barrier(mask, n, 0)
template <int NM, int ND> DEV void interleave_mfma_ds() {
  if constexpr (NM == 6) {
    SGB(0x008, 1); SGB(0x100, 2); SGB(0x008, 1); SGB(0x100, 2); SGB(0x008, 1); SGB(0x100, 2);
    SGB(0x008, 1); SGB(0x100, 2); SGB(0x008, 2);
  } else if constexpr (NM == 10) {
    SGB(0x008, 1); SGB(0x100, 2); SGB(0x008, 1); SGB(0x100, 2); SGB(0x008, 1); SGB(0x100, 2);
    SGB(0x008, 1); SGB(0x100, 2); SGB(0x008, 1); SGB(0x100, 2); SGB(0x008, 5);
  } else {
    SGB(0x008, 1); SGB(0x100, 2); SGB(0x008, 1); SGB(0x100, 2); SGB(0x008, 1); SGB(0x100, 2);
    SGB(0x008, 1); SGB(0x100, 2); SGB(0x008, 1); SGB(0x100, 2);
  }
}
DEV void wait_vm_rt(int n) {
  switch (n) {
    case 0: wait_vm<0>(); break;   case 1: wait_vm<1>(); break;   case 2: wait_vm<2>(); break;
    case 3: wait_vm<3>(); break;   case 4: wait_vm<4>(); break;   case 5: wait_vm<5>(); break;
    case 6: wait_vm<6>(); break;   case 7: wait_vm<7>(); break;   case 8: wait_vm<8>(); break;
    case 9: wait_vm<9>(); break;   case 10: wait_vm<10>(); break; case 11: wait_vm<11>(); break;
    case 12: wait_vm<12>(); break; case 13: wait_vm<13>(); break; case 14: wait_vm<14>(); break;
    default: wait_vm<0>(); break;
  }
}
DEV void glds16_dev(const void* src, char* lds) {
  __builtin_amdgcn_global_load_lds((const unsigned*)src, (unsigned*)lds, 16, 0, 16);
}
DEV void lds_spin(uint32_t* w, uint32_t want) {
  for (int i = 0; i < (1 << 24); ++i) {
    const uint32_t v = __hip_atomic_load(w, __ATOMIC_RELAXED, __HIP_MEMORY_SCOPE_WORKGROUP);
    if ((uint32_t)__builtin_amdgcn_readfirstlane(v) >= want) break;
    __builtin_amdgcn_s_sleep(1);
  }
  asm volatile("" ::: "memory");
}
DEV void wait_cluster(uint32_t* lcnt, uint32_t* lflag, uint32_t* cnt, int lw, int lane) {
  if (lw == 0) {
    lds_spin(lcnt, 8u);
    for (int i = 0; i < (1 << 22); ++i) {
      const uint32_t v = __hip_atomic_load(cnt, __ATOMIC_RELAXED, __HIP_MEMORY_SCOPE_AGENT);
      if ((uint32_t)__builtin_amdgcn_readfirstlane(v) >= 16u) break;
      __builtin_amdgcn_s_sleep(2);
    }
    asm volatile("" ::: "memory");
    if (lane == 0) __hip_atomic_store(lflag, 1u, __ATOMIC_RELAXED, __HIP_MEMORY_SCOPE_WORKGROUP);
  } else {
    lds_spin(lflag, 1u);
  }
}
DEV void publish(uint32_t* lcnt, uint32_t* gcnt, int lane) {
  asm volatile("s_waitcnt vmcnt(0)" ::: "memory");
  if (lane == 0) {
    const uint32_t old = __hip_atomic_fetch_add(lcnt, 1u, __ATOMIC_RELAXED, __HIP_MEMORY_SCOPE_WORKGROUP);
    if (old == 7u) __hip_atomic_fetch_add(gcnt, 1u, __ATOMIC_RELAXED, __HIP_MEMORY_SCOPE_AGENT);
  }
}

#undef STAMP_TID
#define STAMP_TID 0
__global__ __launch_bounds__(1024) void k_tail(const f32x2* __restrict__ T, const uint32_t* __restrict__ x,
                                               uint32_t* h, uint32_t* hn, u32x2* h1,
                                               const char* __restrict__ w1, const uint16_t* __restrict__ b1,
                                               const u32x4* __restrict__ w2p, const uint16_t* __restrict__ b2,
                                               uint16_t* __restrict__ out, uint32_t* cnt) {
  constexpr int BD_OFF = 0, AD_OFF = 6 * 12288;
  constexpr int BE_OFF = 0, NBE = 7, RES_OFF = NBE * 20480;
  constexpr int MISC_OFF = 163840 - 1024, B1_OFF = MISC_OFF, LC_OFF = MISC_OFF + 256, B2_OFF = MISC_OFF + 512;
  __shared__ __attribute__((aligned(16))) char lds[163840];
  const int tid = threadIdx.x, lane = tid & 63;
  const int wv = __builtin_amdgcn_readfirstlane(tid >> 6);
  const int xcd = blockIdx.x & 7, idx = blockIdx.x >> 3;
  const int cl = 2 * xcd + (idx >> 4), mi_ = idx & 15;
  const int cb = mi_, rb2 = 2 * cl + (mi_ >> 3), cb2 = mi_ & 7;
  uint32_t* cnt1 = cnt + cl * 64;
  uint32_t* cnt2 = cnt + (16 + cl) * 64;
  uint32_t* lcnt = (uint32_t*)(lds + LC_OFF);

  if (wv >= 8) {
    const int lw = wv - 8;
    const int nb = (lw < 4) ? 2 : 1, nb2 = (lw < 4) ? 3 : 2;
    const int arow = lane >> 3;
    const char* asrc = (const char*)hn + (size_t)(cl * 128 + 16 * lw + arow) * 1280 + (((lane & 7) ^ arow) << 4);
    const int id0 = lw * 64 + lane, r0 = id0 / 12, c0 = (id0 % 12) ^ (((r0 >> 3) & 1) << 1);
    const int id1 = (8 + lw) * 64 + lane, r1 = id1 / 12, c1 = (id1 % 12) ^ (((r1 >> 3) & 1) << 1);
    const char* bsrc0 = w1 + (size_t)r0 * 3072 + cb * 192 + c0 * 16;
    const char* bsrc1 = w1 + (size_t)r1 * 3072 + cb * 192 + c1 * 16;
    auto stageB = [&](int st) {
      char* dst = lds + BD_OFF + (st % 6) * 12288 + lane * 16;
      glds16(bsrc0 + (size_t)st * 64 * 3072, dst + lw * 1024);
      if (lw < 4) glds16(bsrc1 + (size_t)st * 64 * 3072, dst + (8 + lw) * 1024);
    };
    auto stageA = [&](int st) {
      char* dst = lds + AD_OFF + (st % 5) * 16384 + lane * 16;
      glds16_dev(asrc + st * 128, dst + (2 * lw) * 1024);
      glds16_dev(asrc + st * 128 + 8 * 1280, dst + (2 * lw + 1) * 1024);
    };
    if (lw == 7 && lane < 12) glds16((const u32x4*)(b1 + cb * 96) + lane, lds + B1_OFF + lane * 16);
    if (lw == 6 && lane < 10) glds16((const u32x4*)(b2 + cb2 * 80) + lane, lds + B2_OFF + lane * 16);
#pragma unroll
    for (int st = 0; st < 6; ++st) stageB(st);
    __builtin_amdgcn_s_barrier();
    wait_cluster(lcnt, lcnt + 2, cnt1, lw, lane);
#pragma unroll
    for (int st = 0; st < 5; ++st) stageA(st);
#pragma unroll
    for (int st = 0; st < 10; ++st) {
      const int ka = (st == 0) ? 8 : (st <= 6) ? 6 : (st == 7) ? 4 : (st == 8) ? 2 : 0;
      const int kb = (st <= 1) ? 0 : (st <= 5) ? st - 1 : (st == 6) ? 3 : (st == 7) ? 2 : (st == 8) ? 1 : 0;
      if (lw < 4) wait_vm_rt(ka + 2 * kb); else wait_vm_rt(ka + kb);
      __builtin_amdgcn_s_barrier();
      if (st >= 1 && st + 4 < 10) stageA(st + 4);
      if (st >= 1 && st + 5 < 10) stageB(st + 5);
    }
    __builtin_amdgcn_s_barrier();
    const u32x4* bsrc2 = w2p + (size_t)cb2 * 1536 * 10 + lw * 64 + lane;
    auto stageB2 = [&](int st) {
      char* dst = lds + BE_OFF + (st % NBE) * 20480 + lane * 16;
      glds16(bsrc2 + st * 1280, dst + lw * 1024);
      glds16(bsrc2 + st * 1280 + 512, dst + (8 + lw) * 1024);
      if (lw < 4) glds16(bsrc2 + st * 1280 + 1024, dst + (16 + lw) * 1024);
    };
#pragma unroll
    for (int st = 0; st < NBE; ++st) stageB2(st);
    {
      const int id = lw * 64 + lane, r = id / 10, ch = id % 10;
      glds16_dev((const u32x4*)((const uint16_t*)h + (size_t)(rb2 * 64 + r) * D_ + cb2 * 80) + ch, lds + RES_OFF + id * 16);
      if (lw >= 6) {
        const int id2 = (lw + 2) * 64 + lane, r2 = id2 / 10, ch2 = id2 % 10;
        glds16_dev((const u32x4*)((const uint16_t*)h + (size_t)(rb2 * 64 + r2) * D_ + cb2 * 80) + ch2, lds + RES_OFF + id2 * 16);
      }
    }
    if (lw == 0) wait_cluster(lcnt + 1, lcnt + 3, cnt2, lw, lane);
    wait_vm<0>();
#pragma unroll
    for (int st = 0; st < 12; ++st) {
      const int later = (st < NBE) ? 99 : ((st + 5 < 11 ? st + 5 : 11) - st);
      if (later != 99) { if (lw < 4) wait_vm_rt(3 * later); else wait_vm_rt(2 * later); }
      __builtin_amdgcn_s_barrier();
      if (st >= 1 && st + 6 < 12) stageB2(st + 6);
    }
    __builtin_amdgcn_s_barrier();
    (void)nb; (void)nb2;
    return;
  }

  STAMP(2, 0);
  {
    f32x2(*ex)[360] = (f32x2(*)[360])(lds + AD_OFF);
    f32x2(*xx)[384] = (f32x2(*)[384])(lds + AD_OFF + 23040);
    const int j = wv;
    const int s0 = 32 * xcd + idx;
    const int row = s0 + 256 * j;
    const int prow = s0 * 8 + j;
    f32x2 v[8];
    {
      const f32x2* src = T + (size_t)s0 * TP;
      const uint32_t kk = min(tid, DF_ - 1);
#pragma unroll
      for (int q = 0; q < 8; ++q) v[q] = (src + (size_t)q * 256 * TP)[kk];
    }
    uint32_t xd[5];
#pragma unroll
    for (int i = 0; i < 5; ++i) xd[i] = x[row * 320 + lane + 64 * i];
    if (tid < 4) lcnt[tid] = 0;

    f32x2 tw1[4], tw2[7], twq[7], twk[8];
#pragma unroll
    for (int r = 1; r < 5; ++r) tw1[r - 1] = twid((float)(lane * r) * (1.0f / 320.0f));
    const int l0 = lane & 7, rr = lane >> 3;
#pragma unroll
    for (int q = 1; q < 8; ++q) tw2[q - 1] = twid((float)(l0 * q) * (1.0f / 64.0f));
#pragma unroll
    for (int q = 1; q < 8; ++q) twq[q - 1] = twid((float)(s0 * q) * (1.0f / 2048.0f));
#pragma unroll
    for (int p = 0; p < 8; ++p) twk[p] = twid((float)(rr + 5 * l0 + 40 * p) * (1.0f / 640.0f));
    pin(tw1); pin(tw2); pin(twq); pin(twk);
    __builtin_amdgcn_sched_barrier(0);
    STAMP(2, 1);

    if (j < 6) {
#pragma unroll
      for (int q = 1; q < 8; ++q) v[q] = cmulc(v[q], twq[q - 1]);
      fft8<true>(v);
      f32x2 msk;
      msk.x = 1.0f; msk.y = (tid == 0 || tid == 320) ? 0.0f : 1.0f;
#pragma unroll
      for (int jj = 0; jj < 8; ++jj) xx[jj][tid] = v[jj] * msk;
    }
    RAW_BAR();
    asm volatile("" ::: "memory");
    f32x2* E = ex[j];
    if (lane < 40) {
      f32x2* Er = E + rr * 72;
      const f32x2* Xa = xx[j] + rr + 5 * l0;
      const f32x2* Xb = xx[j] + 320 - rr - 5 * l0;
#pragma unroll
      for (int p = 0; p < 8; ++p) {
        const f32x2 a = Xa[40 * p], b = Xb[-40 * p];
        f32x2 e, d;
        e.x = a.x + b.x; e.y = a.y - b.y;
        d.x = a.x - b.x; d.y = a.y + b.y;
        v[p] = cadd_pi(e, cmulc(d, twk[p]));
      }
      fft8<true>(v);
      Er[l0 * 9] = v[0];
#pragma unroll
      for (int a = 1; a < 8; ++a) Er[l0 * 9 + a] = cmulc(v[a], tw2[a - 1]);
      wave_sync();
#pragma unroll
      for (int q = 0; q < 8; ++q) v[q] = Er[q * 9 + l0];
      fft8<true>(v);
      wave_sync();
#pragma unroll
      for (int l1 = 0; l1 < 8; ++l1) Er[l0 + 8 * l1] = v[l1];
    }
    wave_sync();
    f32x2 u[5];
    u[0] = E[lane];
#pragma unroll
    for (int r = 1; r < 5; ++r) u[r] = cmulc(E[r * 72 + lane], tw1[r - 1]);
    fft5<true>(u);

    const f32x2 sc = {1.0f / 2621440.0f, 1.0f / 2621440.0f};
    f32x2 hv[5];
    uint32_t zp[5];
#pragma unroll
    for (int i = 0; i < 5; ++i) {
      const uint32_t hp = bf_pack(bf_unpack(xd[i]) + bf_round(u[i] * sc));
      st_wt32(h + prow * 320 + lane + 64 * i, hp);
      hv[i] = bf_unpack(hp);
    }
    row_layernorm(hv, zp);
#pragma unroll
    for (int i = 0; i < 5; ++i) st_wt32(hn + prow * 320 + lane + 64 * i, zp[i]);
    STAMP(2, 2);
    publish(lcnt, cnt1, lane);
    STAMP(2, 3);
  }
  const int g = lane >> 4, q4 = (lane & 15) >> 2, p4 = lane & 3;
  {
    const int wm = wv >> 1, wn = wv & 1;
    f32x4 acc[2][3];
#pragma unroll
    for (int mi = 0; mi < 2; ++mi)
#pragma unroll
      for (int ni = 0; ni < 3; ++ni) acc[mi][ni] = (f32x4){0.f, 0.f, 0.f, 0.f};
    const int ar = 32 * wm + (lane & 15);
    const int aoff0 = AD_OFF + ar * 128 + (((lane >> 4) ^ (ar & 7)) << 4);
    const int aoff1 = AD_OFF + ar * 128 + (((4 + (lane >> 4)) ^ (ar & 7)) << 4);
    int boff[3];
#pragma unroll
    for (int ni = 0; ni < 3; ++ni) boff[ni] = BD_OFF + (8 * g + q4) * 192 + p4 * 8 + (((3 * wn + ni) ^ (g & 1)) << 5);
    bf16x8 af0[2], bf0[3], af1[2], bf1[3];
    auto rdD = [&](bf16x8 (&af)[2], bf16x8 (&bf)[3], int st, int ks) {
      int sa = (st % 5) * 16384, sb = (st % 6) * 12288;
      asm volatile("" : "+s"(sa), "+s"(sb));
#pragma unroll
      for (int mi = 0; mi < 2; ++mi) af[mi] = *(const bf16x8*)(lds + sa + (ks ? aoff1 : aoff0) + mi * 2048);
#pragma unroll
      for (int ni = 0; ni < 3; ++ni)
        bf[ni] = tr_frag(lds + sb + boff[ni] + ks * 6144, lds + sb + boff[ni] + ks * 6144 + 768);
    };
    auto mmD = [&](const bf16x8 (&af)[2], const bf16x8 (&bf)[3]) {
#pragma unroll
      for (int mi = 0; mi < 2; ++mi)
#pragma unroll
        for (int ni = 0; ni < 3; ++ni)
          acc[mi][ni] = __builtin_amdgcn_mfma_f32_16x16x32_bf16(bf[ni], af[mi], acc[mi][ni], 0, 0, 0);
    };
    RAW_BAR();
    STAMP(2, 4);
    rdD(af0, bf0, 0, 0);
#pragma unroll
    for (int st = 0; st < 10; ++st) {
      rdD(af1, bf1, st, 1);
      mmD(af0, bf0);
      interleave_mfma_ds<6, 8>();
      __builtin_amdgcn_sched_barrier(0);
      RAW_BAR();
      __builtin_amdgcn_sched_barrier(0);
      if (st < 9) rdD(af0, bf0, st + 1, 0);
      mmD(af1, bf1);
      if (st < 9) interleave_mfma_ds<6, 8>();
      __builtin_amdgcn_sched_barrier(0);
    }
    STAMP(2, 5);
#pragma unroll
    for (int ni = 0; ni < 3; ++ni) {
      const u32x2 bb = *(const u32x2*)(lds + B1_OFF + ((3 * wn + ni) * 16 + 4 * (lane >> 4)) * 2);
      const f32x2 b01 = bf_unpack(bb.x), b23 = bf_unpack(bb.y);
      const int NT = cb * 6 + 3 * wn + ni;
      const int ks2 = NT >> 1, gg = 2 * (NT & 1) + (lane >> 5), half = (lane >> 4) & 1;
#pragma unroll
      for (int mi = 0; mi < 2; ++mi) {
        f32x2 a01 = {acc[mi][ni][0], acc[mi][ni][1]}, a23 = {acc[mi][ni][2], acc[mi][ni][3]};
        a01 = bf_unpack(pk_relu_bf16(bf_pack(bf_round(a01) + b01)));
        a23 = bf_unpack(pk_relu_bf16(bf_pack(bf_round(a23) + b23)));
        u32x2 ov;
        ov.x = bf_pack(a01 * a01); ov.y = bf_pack(a23 * a23);
        const int mt = cl * 8 + 2 * wm + mi;
        st_wt64(h1 + ((((mt >> 2) * 48 + ks2) * 4 + (mt & 3)) * 64 + (lane & 15) + 16 * gg) * 2 + half, ov);
      }
    }
    publish(lcnt + 1, cnt2, lane);
    STAMP(2, 6);
  }
  {
    const int wmp = wv & 1, wk = wv >> 1;
    f32x4 acc[2][5];
#pragma unroll
    for (int mi = 0; mi < 2; ++mi)
#pragma unroll
      for (int ni = 0; ni < 5; ++ni) acc[mi][ni] = (f32x4){0.f, 0.f, 0.f, 0.f};
    const int boff = BE_OFF + (16 * (g >> 1) + 4 * (g & 1) + q4 + 32 * wk) * 160 + p4 * 8;
    const __amdgpu_buffer_rsrc_t rs = __builtin_amdgcn_make_buffer_rsrc((void*)h1, 0, S_ * F_ * 2, 0x00020000);
    const int avoff = ((rb2 * 48 + wk) * 4 + 2 * wmp) * 1024 + lane * 16;
    bf16x8 af[12][2], bfr[2][5];
    auto ldA = [&](int st) {
      af[st][0] = __builtin_bit_cast(bf16x8, __builtin_amdgcn_raw_buffer_load_b128(rs, avoff, st * 16384, 16));
      af[st][1] = __builtin_bit_cast(bf16x8, __builtin_amdgcn_raw_buffer_load_b128(rs, avoff + 1024, st * 16384, 16));
    };
    auto rdE = [&](bf16x8 (&bf)[5], int st) {
      int sb = (st % NBE) * 20480;
      asm volatile("" : "+s"(sb));
#pragma unroll
      for (int ni = 0; ni < 5; ++ni) bf[ni] = tr_frag(lds + sb + boff + ni * 32, lds + sb + boff + ni * 32 + 1280);
    };
    constexpr int APF = 4;
    lds_spin(lcnt + 3, 1u);
#pragma unroll
    for (int i = 0; i < APF; ++i) ldA(i);
    RAW_BAR();
    STAMP(2, 7);
    rdE(bfr[0], 0);
#pragma unroll
    for (int st = 0; st < 12; ++st) {
      if (st < 11) {
        RAW_BAR();
        __builtin_amdgcn_sched_barrier(0);
        rdE(bfr[(st + 1) & 1], st + 1);
      }
#pragma unroll
      for (int ni = 0; ni < 5; ++ni)
#pragma unroll
        for (int mi = 0; mi < 2; ++mi)
          acc[mi][ni] = __builtin_amdgcn_mfma_f32_16x16x32_bf16(bfr[st & 1][ni], af[st][mi], acc[mi][ni], 0, 0, 0);
      if (st < 11) interleave_mfma_ds<10, 10>();
      __builtin_amdgcn_sched_barrier(0);
      if (st + APF < 12) ldA(st + APF);
    }
    STAMP(2, 8);
    f32x4* red = (f32x4*)(lds + BE_OFF) + (wmp * 40) * 64 + lane;
#pragma unroll
    for (int t = 0; t < 10; ++t)
      if ((t & 3) != wk) red[(t * 4 + wk) * 64] = acc[t / 5][t % 5];
    RAW_BAR();
    asm volatile("" ::: "memory");
#pragma unroll
    for (int t = 0; t < 10; ++t) {
      if ((t & 3) == wk) {
        const int mi = t / 5, ni = t % 5;
        f32x4 o = acc[mi][ni];
#pragma unroll
        for (int w2_ = 0; w2_ < 4; ++w2_)
          if (w2_ != (t & 3)) o += red[(t * 4 + w2_) * 64];
        const int lr = 32 * wmp + 16 * mi + (lane & 15);
        const int prow = rb2 * 64 + lr;
        const int mrow = (prow >> 3) + 256 * (prow & 7);
        const int nc = 16 * ni + 4 * (lane >> 4);
        const u32x2 bb = *(const u32x2*)(lds + B2_OFF + nc * 2);
        const u32x2 rsd = *(const u32x2*)(lds + RES_OFF + lr * 160 + nc * 2);
        f32x2 a01 = {o[0], o[1]}, a23 = {o[2], o[3]};
        u32x2 ov;
        ov.x = bf_pack(bf_unpack(rsd.x) + bf_round(bf_round(a01) + bf_unpack(bb.x)));
        ov.y = bf_pack(bf_unpack(rsd.y) + bf_round(bf_round(a23) + bf_unpack(bb.y)));
        *(u32x2*)(out + (size_t)mrow * D_ + cb2 * 80 + nc) = ov;
      }
    }
  }
  STAMP_END(2, 9);
}

__global__ void k_zz_end() {}
extern "C" void kernel_launch(void* const* d_in, const int* in_sizes, int n_in, void* d_out, int out_size,
                              void* d_ws, size_t ws_size, hipStream_t stream) {
  const uint32_t* x = (const uint32_t*)d_in[0];
  const uint16_t* wr = (const uint16_t*)d_in[1];
  const uint16_t* wi = (const uint16_t*)d_in[2];
  const uint16_t* w1 = (const uint16_t*)d_in[4];
  const uint16_t* b1 = (const uint16_t*)d_in[5];
  const uint16_t* w2 = (const uint16_t*)d_in[6];
  const uint16_t* b2 = (const uint16_t*)d_in[7];
  char* ws = (char*)d_ws;
  size_t off = (size_t)224 << 20;
  f32x2* T = (f32x2*)(ws + off); off += (size_t)8 * 256 * TP * 8;
  uint32_t* h = (uint32_t*)(ws + off); off += (size_t)S_ * D_ * 2;
  uint32_t* hn = (uint32_t*)(ws + off); off += (size_t)S_ * D_ * 2;
  u32x4* h1 = (u32x4*)(ws + off); off += (size_t)S_ * F_ * 2;
  u32x4* w2p = (u32x4*)(ws + off); off += (size_t)D_ * F_ * 2;

  uint32_t* cnt = (uint32_t*)(ws + off); off += 32 * 256;
  k_fwd_rows<<<256, 512, 0, stream>>>(x, T, wr, wi, (const uint32_t*)w1, (const u32x4*)w2, w2p, cnt);
  k_cols<<<328, 256, 0, stream>>>(T, wr, wi, (const u32x4*)w2, w2p);
  k_tail<<<256, 1024, 0, stream>>>(T, x, h, hn, (u32x2*)h1, (const char*)w1, b1, W2_MODE >= 1 ? (const u32x4*)w2p : (const u32x4*)w2, b2, (uint16_t*)d_out, cnt);
}
```

```cpp
#include <hip/hip_runtime.h>
#include <hip/hip_bf16.h>
#include <stdint.h>

typedef float f32x2 __attribute__((ext_vector_type(2)));
typedef float f32x4 __attribute__((ext_vector_type(4)));
typedef short bf16x8 __attribute__((ext_vector_type(8)));
typedef unsigned int u32x4 __attribute__((ext_vector_type(4)));
typedef unsigned int u32x2 __attribute__((ext_vector_type(2)));

#define DEV __device__ __forceinline__

#define STAMP(kern, i)
#define STAMP_END(kern, i)
#define STAMP_TID 0

static constexpr int S_ = 2048, D_ = 640, F_ = 1536, DF_ = 321;
static constexpr int TP = 328;
static constexpr int NEARLY = 4;

DEV float bf_lo(uint32_t p) { return __builtin_bit_cast(float, p << 16); }
DEV float bf_hi(uint32_t p) { return __builtin_bit_cast(float, p & 0xffff0000u); }
DEV f32x2 bf_unpack(uint32_t p) { f32x2 r; r.x = bf_lo(p); r.y = bf_hi(p); return r; }
typedef __bf16 bf16x2_t __attribute__((ext_vector_type(2)));
DEV uint32_t bf_pack(f32x2 v) {
  return __builtin_bit_cast(uint32_t, __builtin_convertvector(v, bf16x2_t));
}
DEV f32x2 bf_round(f32x2 v) { return bf_unpack(bf_pack(v)); }
DEV float bfr(float f) { f32x2 v; v.x = f; v.y = f; return bf_lo(bf_pack(v)); }

DEV f32x2 cadd_mi(f32x2 b, f32x2 d) {
  f32x2 r;
  asm("v_pk_add_f32 %0, %1, %2 op_sel:[0,1] op_sel_hi:[1,0] neg_hi:[0,1]" : "=v"(r) : "v"(b), "v"(d));
  return r;
}
DEV f32x2 cadd_pi(f32x2 b, f32x2 d) {
  f32x2 r;
  asm("v_pk_add_f32 %0, %1, %2 op_sel:[0,1] op_sel_hi:[1,0] neg_lo:[0,1]" : "=v"(r) : "v"(b), "v"(d));
  return r;
}
DEV f32x2 cnadd_mi(f32x2 b, f32x2 d) {
  f32x2 r;
  asm("v_pk_add_f32 %0, %1, %2 op_sel:[0,1] op_sel_hi:[1,0] neg_lo:[1,0] neg_hi:[1,1]" : "=v"(r) : "v"(b), "v"(d));
  return r;
}
DEV f32x2 cnadd_pi(f32x2 b, f32x2 d) {
  f32x2 r;
  asm("v_pk_add_f32 %0, %1, %2 op_sel:[0,1] op_sel_hi:[1,0] neg_lo:[1,1] neg_hi:[1,0]" : "=v"(r) : "v"(b), "v"(d));
  return r;
}
DEV f32x2 cfma_mi(f32x2 s, f32x2 d, f32x2 b) {
  f32x2 r;
  asm("v_pk_fma_f32 %0, %1, %2, %3 op_sel:[0,1,0] op_sel_hi:[1,0,1] neg_hi:[0,1,0]" : "=v"(r) : "v"(s), "v"(d), "v"(b));
  return r;
}
DEV f32x2 cfma_pi(f32x2 s, f32x2 d, f32x2 b) {
  f32x2 r;
  asm("v_pk_fma_f32 %0, %1, %2, %3 op_sel:[0,1,0] op_sel_hi:[1,0,1] neg_lo:[0,1,0]" : "=v"(r) : "v"(s), "v"(d), "v"(b));
  return r;
}
DEV f32x2 pfma(f32x2 a, f32x2 b, f32x2 c) { return __builtin_elementwise_fma(a, b, c); }
DEV f32x2 cmul(f32x2 a, f32x2 w) {
  f32x2 t, r;
  asm("v_pk_mul_f32 %0, %1, %2 op_sel:[0,0] op_sel_hi:[0,1]" : "=v"(t) : "v"(a), "v"(w));
  asm("v_pk_fma_f32 %0, %1, %2, %3 op_sel:[1,1,0] op_sel_hi:[1,0,1] neg_lo:[0,1,0]" : "=v"(r) : "v"(a), "v"(w), "v"(t));
  return r;
}
DEV f32x2 cmulc(f32x2 a, f32x2 w) {
  f32x2 t, r;
  asm("v_pk_mul_f32 %0, %1, %2 op_sel:[0,0] op_sel_hi:[0,1] neg_hi:[0,1]" : "=v"(t) : "v"(a), "v"(w));
  asm("v_pk_fma_f32 %0, %1, %2, %3 op_sel:[1,1,0] op_sel_hi:[1,0,1]" : "=v"(r) : "v"(a), "v"(w), "v"(t));
  return r;
}
DEV f32x2 twid(float f) {
  f32x2 r;
  r.x = __builtin_amdgcn_cosf(f);
  r.y = __builtin_amdgcn_sinf(-f);
  return r;
}
DEV void st_wt(f32x2* p, f32x2 v) {
  __hip_atomic_store((unsigned long long*)p, __builtin_bit_cast(unsigned long long, v), __ATOMIC_RELAXED,
                     __HIP_MEMORY_SCOPE_AGENT);
}
DEV void st_wt32(uint32_t* p, uint32_t v) { __hip_atomic_store(p, v, __ATOMIC_RELAXED, __HIP_MEMORY_SCOPE_AGENT); }
DEV void st_wt64(u32x2* p, u32x2 v) {
  __hip_atomic_store((unsigned long long*)p, __builtin_bit_cast(unsigned long long, v), __ATOMIC_RELAXED,
                     __HIP_MEMORY_SCOPE_AGENT);
}
DEV void st_wt128(void* p, u32x4 v) {
  asm volatile("global_store_dwordx4 %0, %1, off sc1\n\ts_nop 1" ::"v"(p), "v"(v) : "memory");
}
template <int N> DEV void pin(f32x2 (&a)[N]) {
#pragma unroll
  for (int i = 0; i < N; ++i) asm volatile("" : "+v"(a[i]));
}
template <bool INV> DEV f32x2 add_rot(f32x2 b, f32x2 d) { return INV ? cadd_pi(b, d) : cadd_mi(b, d); }
template <bool INV> DEV f32x2 sub_rot(f32x2 b, f32x2 d) { return INV ? cadd_mi(b, d) : cadd_pi(b, d); }

template <bool INV> DEV void fft4(f32x2& a0, f32x2& a1, f32x2& a2, f32x2& a3) {
  f32x2 b0 = a0 + a2, b1 = a0 - a2, b2 = a1 + a3, d = a1 - a3;
  a0 = b0 + b2; a2 = b0 - b2;
  a1 = add_rot<INV>(b1, d); a3 = sub_rot<INV>(b1, d);
}
template <bool INV> DEV void fft8(f32x2 (&v)[8]) {
  const f32x2 r = {0.70710678118654752440f, 0.70710678118654752440f};
  const f32x2 nr = {-0.70710678118654752440f, -0.70710678118654752440f};
  f32x2 e0 = v[0] + v[4], e1 = v[1] + v[5], e2 = v[2] + v[6], e3 = v[3] + v[7];
  f32x2 o0 = v[0] - v[4], o1 = v[1] - v[5], o2 = v[2] - v[6], o3 = v[3] - v[7];
  fft4<INV>(e0, e1, e2, e3);
  f32x2 p1 = INV ? cadd_pi(o1, o1) : cadd_mi(o1, o1);
  f32x2 p3 = INV ? cnadd_pi(o3, o3) : cnadd_mi(o3, o3);
  f32x2 s = p1 + p3, d = p1 - p3;
  f32x2 b0 = add_rot<INV>(o0, o2), b1 = sub_rot<INV>(o0, o2);
  v[0] = e0; v[2] = e1; v[4] = e2; v[6] = e3;
  v[1] = pfma(r, s, b0); v[5] = pfma(nr, s, b0);
  v[3] = INV ? cfma_pi(r, d, b1) : cfma_mi(r, d, b1);
  v[7] = INV ? cfma_mi(r, d, b1) : cfma_pi(r, d, b1);
}
template <bool INV> DEV void fft5(f32x2 (&v)[5]) {
  const f32x2 c1 = {0.30901699437494742410f, 0.30901699437494742410f};
  const f32x2 c2 = {-0.80901699437494742410f, -0.80901699437494742410f};
  const f32x2 s1 = {0.95105651629515357212f, 0.95105651629515357212f};
  const f32x2 s2 = {0.58778525229247312917f, 0.58778525229247312917f};
  const f32x2 ns1 = {-0.95105651629515357212f, -0.95105651629515357212f};
  f32x2 t1 = v[1] + v[4], t2 = v[2] + v[3], t3 = v[1] - v[4], t4 = v[2] - v[3];
  f32x2 a1 = pfma(c2, t2, pfma(c1, t1, v[0]));
  f32x2 a2 = pfma(c1, t2, pfma(c2, t1, v[0]));
  f32x2 b1 = pfma(s2, t4, s1 * t3);
  f32x2 b2 = pfma(ns1, t4, s2 * t3);
  v[0] = v[0] + t1 + t2;
  v[1] = add_rot<INV>(a1, b1); v[4] = sub_rot<INV>(a1, b1);
  v[2] = add_rot<INV>(a2, b2); v[3] = sub_rot<INV>(a2, b2);
}

template <int CTRL> DEV float dppmov(float v) {
  return __builtin_bit_cast(float, __builtin_amdgcn_update_dpp(0, __builtin_bit_cast(int, v), CTRL, 0xf, 0xf, false));
}
DEV float wave_sum(float v) {
  v += dppmov<0xB1>(v);
  v += dppmov<0x4E>(v);
  v += dppmov<0x141>(v);
  v += dppmov<0x140>(v);
  int iv = __builtin_bit_cast(int, v);
  float a = __builtin_bit_cast(float, __builtin_amdgcn_readlane(iv, 0));
  float b = __builtin_bit_cast(float, __builtin_amdgcn_readlane(iv, 16));
  float c = __builtin_bit_cast(float, __builtin_amdgcn_readlane(iv, 32));
  float d = __builtin_bit_cast(float, __builtin_amdgcn_readlane(iv, 48));
  return (a + b) + (c + d);
}
DEV void wave_sync() {
  __builtin_amdgcn_fence(__ATOMIC_RELEASE, "wavefront");
  __builtin_amdgcn_wave_barrier();
  __builtin_amdgcn_fence(__ATOMIC_ACQUIRE, "wavefront");
}

DEV void row_layernorm(const f32x2 (&xv)[5], uint32_t (&zp)[5]) {
  f32x2 s2 = (xv[0] + xv[1]) + (xv[2] + xv[3]) + xv[4];
  const float mean = wave_sum(s2.x + s2.y) * (1.0f / 640.0f);
  const f32x2 nm = {-mean, -mean};
  f32x2 q2 = {0.f, 0.f};
#pragma unroll
  for (int i = 0; i < 5; ++i) { f32x2 d = xv[i] + nm; q2 = pfma(d, d, q2); }
  const float var = wave_sum(q2.x + q2.y) * (1.0f / 640.0f);
  f32x2 mv; mv.x = mean; mv.y = var;
  mv = bf_round(mv);
  const float den = bfr(sqrtf(bfr(mv.y + 1e-5f)));
  const float rden = 1.0f / den;
  const f32x2 nmb = {-mv.x, -mv.x}, rd = {rden, rden};
#pragma unroll
  for (int i = 0; i < 5; ++i) zp[i] = bf_pack(bf_round(xv[i] + nmb) * rd);
}

__global__ __launch_bounds__(512) void k_fwd_rows(const uint32_t* __restrict__ x, f32x2* __restrict__ T,
                                                  const uint16_t* __restrict__ wr, const uint16_t* __restrict__ wi,
                                                  const uint32_t* __restrict__ w1, const u32x4* __restrict__ w2,
                                                  u32x4* __restrict__ w2p, uint32_t* __restrict__ cnt) {
  __shared__ f32x2 ex[8][360];
  __shared__ f32x2 zz[8][320];
  const int tid = threadIdx.x, lane = tid & 63, j = tid >> 6;
  STAMP(0, 0);
  if (blockIdx.x == 0 && tid < 64) st_wt32(cnt + tid * 32, 0u);
  const int s0 = 32 * (blockIdx.x & 7) + (blockIdx.x >> 3);
  const int row = s0 + 256 * j;
  uint32_t xd[5];
#pragma unroll
  for (int i = 0; i < 5; ++i) xd[i] = x[row * 320 + lane + 64 * i];
  f32x2 tw1[4], tw2[7], twq[7];
#pragma unroll
  for (int r = 1; r < 5; ++r) tw1[r - 1] = twid((float)(lane * r) * (1.0f / 320.0f));
  const int l0 = lane & 7, rr = lane >> 3;
#pragma unroll
  for (int q = 1; q < 8; ++q) tw2[q - 1] = twid((float)(l0 * q) * (1.0f / 64.0f));
#pragma unroll
  for (int q = 1; q < 8; ++q) twq[q - 1] = twid((float)(s0 * q) * (1.0f / 2048.0f));
  f32x2 twk_[1] = {twid((float)tid * (1.0f / 640.0f))};
  pin(tw1); pin(tw2); pin(twq); pin(twk_);
  const f32x2 twk = twk_[0];
  __builtin_amdgcn_sched_barrier(0);
  STAMP(0, 1);

  f32x2 u[5];
  {
    f32x2 xv[5];
    uint32_t zp[5];
#pragma unroll
    for (int i = 0; i < 5; ++i) xv[i] = bf_unpack(xd[i]);
    row_layernorm(xv, zp);
#pragma unroll
    for (int i = 0; i < 5; ++i) u[i] = bf_unpack(zp[i]);
  }
  STAMP(0, 2);
  __builtin_amdgcn_sched_barrier(0);
  const int wid = min((int)blockIdx.x * 512 + tid, 122879);
  const u32x4 wchunk = w2[wid];

  uint32_t pf = 0;
  {
    const char* pa = nullptr;
    if (lane < 12) {
      const int t = (blockIdx.x & 7) + 8 * (8 * (blockIdx.x >> 3) + j);
      pa = (const char*)(((lane < 6) ? wr : wi) + (t * DF_ + min(64 * (lane % 6), DF_ - 2)));
      pa = (const char*)((uintptr_t)pa & ~(uintptr_t)3);
    } else if (lane >= 56) {
      pa = (const char*)(w1 + (blockIdx.x * 60 + min(j * 8 + (lane - 56), 59)) * 32);
    }
    else if (lane < 18) {
      unsigned long long c0, c1;
      asm volatile("s_getpc_b64 s[40:41]\n\ts_add_u32 s40, s40, _Z6k_colsPDv2_fPKtS2_@rel32@lo+4\n\ts_addc_u32 s41, s41, _Z6k_colsPDv2_fPKtS2_@rel32@hi+12\n\ts_mov_b64 %0, s[40:41]" : "=s"(c0) :: "s40", "s41", "scc");
      asm volatile("s_getpc_b64 s[40:41]\n\ts_add_u32 s40, s40, _Z8k_zz_endv@rel32@lo+4\n\ts_addc_u32 s41, s41, _Z8k_zz_endv@rel32@hi+12\n\ts_mov_b64 %0, s[40:41]" : "=s"(c1) :: "s40", "s41", "scc");
      const long long len = (long long)(c1 - c0);
      const int li = ((blockIdx.x >> 3) * 8 + j) * 2 + (lane - 16);
      if (lane >= 16 && len > 0 && len < 65536 && li * 128 < len) pa = (const char*)(c0 + li * 128);
    }
    if (pa) pf = *(const uint32_t*)pa;
  }
  __builtin_amdgcn_sched_barrier(0);

  f32x2* E = ex[j];
  fft5<false>(u);
  E[lane] = u[0];
#pragma unroll
  for (int r = 1; r < 5; ++r) E[r * 72 + lane] = cmul(u[r], tw1[r - 1]);
  wave_sync();
  if (lane < 40) {
    f32x2 v[8];
    f32x2* Er = E + rr * 72;
#pragma unroll
    for (int l1 = 0; l1 < 8; ++l1) v[l1] = Er[l0 + 8 * l1];
    fft8<false>(v);
    wave_sync();
    Er[l0] = v[0];
#pragma unroll
    for (int q = 1; q < 8; ++q) Er[q * 9 + l0] = cmul(v[q], tw2[q - 1]);
    wave_sync();
#pragma unroll
    for (int a = 0; a < 8; ++a) v[a] = Er[l0 * 9 + a];
    fft8<false>(v);
    f32x2* Z = zz[j] + rr + 5 * l0;
#pragma unroll
    for (int p = 0; p < 8; ++p) Z[40 * p] = v[p];
  }
  if (blockIdx.x < 240) {
    const int wk_ = wid / 80, wc_ = wid % 80;
    const int rho = (wk_ & ~12) | ((wk_ & 4) << 1) | ((wk_ & 8) >> 1);
    st_wt128(w2p + ((wc_ / 10) * 1536 + rho) * 10 + (wc_ % 10), wchunk);
  }
  STAMP(0, 3);
  __syncthreads();
  STAMP(0, 4);
  if (j < 6) {
    const int k = tid;
    const int ka = (k >= 320) ? 0 : k, kb = (k == 0 || k >= 320) ? 0 : 320 - k;
    f32x2 v[8];
#pragma unroll
    for (int jj = 0; jj < 8; ++jj) {
      const f32x2 a = zz[jj][ka], b = zz[jj][kb];
      f32x2 e, d;
      e.x = a.x + b.x; e.y = a.y - b.y;
      d.x = a.x - b.x; d.y = a.y + b.y;
      v[jj] = cadd_mi(e, cmul(d, twk));
    }
    fft8<false>(v);
    if (k < DF_) {
      f32x2* dst = T + (size_t)s0 * TP;
      st_wt(dst + k, v[0]);
#pragma unroll
      for (int q = 1; q < 8; ++q) st_wt(dst + (size_t)q * 256 * TP + k, cmul(v[q], twq[q - 1]));
    }
  }
  asm volatile("" ::"v"(pf));
  STAMP(0, 5);
  STAMP_END(0, 6);
}

__global__ __launch_bounds__(256) void k_cols(f32x2* __restrict__ T, const uint16_t* __restrict__ wr,
                                              const uint16_t* __restrict__ wi) {
  __shared__ f32x2 e1[2048];
  __shared__ f32x2 e2[4][576];
  __shared__ __attribute__((aligned(16))) uint32_t wl[2048];
  const int tid = threadIdx.x, lane = tid & 63, wv = tid >> 6;
  STAMP(1, 0);
  const int q = blockIdx.x & 7, kg = blockIdx.x >> 3;
  const int c = tid & 7, a = tid >> 3;
  const int k = kg * 8 + c;
  const bool valid = k < DF_;
  f32x2* colbase = T + (size_t)(q * 256) * TP;
  const uint32_t coff = a * TP + (valid ? k : DF_ - 1);
  f32x2 v[8];
#pragma unroll
  for (int j1 = 0; j1 < 8; ++j1) v[j1] = (colbase + (size_t)j1 * 32 * TP)[coff];
  const int kb = min(8 * kg, DF_ - 8);
  u32x4 wre, wim;
  __builtin_memcpy(&wre, wr + (size_t)(q + 8 * tid) * DF_ + kb, 16);
  __builtin_memcpy(&wim, wi + (size_t)(q + 8 * tid) * DF_ + kb, 16);

  const int b = (lane >> 3) & 3, p1lo = lane >> 5, p1 = 2 * wv + p1lo;
  f32x2 twa[7], twb[7];
#pragma unroll
  for (int p = 1; p < 8; ++p) twa[p - 1] = twid((float)(a * p) * (1.0f / 256.0f));
#pragma unroll
  for (int p = 1; p < 8; ++p) twb[p - 1] = twid((float)(b * p) * (1.0f / 32.0f));
  pin(twa); pin(twb);
  __builtin_amdgcn_sched_barrier(0);
  STAMP(1, 1);

  fft8<false>(v);
  e1[tid] = v[0];
#pragma unroll
  for (int p = 1; p < 8; ++p) e1[p * 256 + tid] = cmul(v[p], twa[p - 1]);
  {
    u32x4 o0, o1;
    o0.x = __builtin_amdgcn_perm(wim.x, wre.x, 0x05040100); o0.y = __builtin_amdgcn_perm(wim.x, wre.x, 0x07060302);
    o0.z = __builtin_amdgcn_perm(wim.y, wre.y, 0x05040100); o0.w = __builtin_amdgcn_perm(wim.y, wre.y, 0x07060302);
    o1.x = __builtin_amdgcn_perm(wim.z, wre.z, 0x05040100); o1.y = __builtin_amdgcn_perm(wim.z, wre.z, 0x07060302);
    o1.z = __builtin_amdgcn_perm(wim.w, wre.w, 0x05040100); o1.w = __builtin_amdgcn_perm(wim.w, wre.w, 0x07060302);
    *(u32x4*)(wl + tid * 8) = o0;
    *(u32x4*)(wl + tid * 8 + 4) = o1;
  }
  STAMP(1, 2);
  __syncthreads();
  STAMP(1, 3);
  uint32_t wf[8];
  {
    const uint32_t* wlp = wl + (p1 + 16 * b) * 8 + min(8 * kg + c - kb, 7);
#pragma unroll
    for (int e = 0; e < 2; ++e)
#pragma unroll
      for (int mm = 0; mm < 4; ++mm) wf[e * 4 + mm] = wlp[(8 * e + 64 * mm) * 8];
  }
  f32x2* E1 = e1 + p1 * 256 + (lane & 31);
#pragma unroll
  for (int j2 = 0; j2 < 8; ++j2) v[j2] = E1[32 * j2];
  fft8<false>(v);
  f32x2* E2w = e2[wv] + p1lo * 288 + (lane & 31);
  E2w[0] = v[0];
#pragma unroll
  for (int p = 1; p < 8; ++p) E2w[p * 36] = cmul(v[p], twb[p - 1]);
  wave_sync();
  f32x2* E2r = e2[wv] + p1lo * 288 + (2 * b) * 36 + c;
#pragma unroll
  for (int e = 0; e < 2; ++e)
#pragma unroll
    for (int bb = 0; bb < 4; ++bb) v[e * 4 + bb] = E2r[e * 36 + bb * 8];
  fft4<false>(v[0], v[1], v[2], v[3]);
  fft4<false>(v[4], v[5], v[6], v[7]);
  STAMP(1, 4);
#pragma unroll
  for (int i = 0; i < 8; ++i) v[i] = cmul(v[i], bf_unpack(wf[i]));
  fft4<true>(v[0], v[1], v[2], v[3]);
  fft4<true>(v[4], v[5], v[6], v[7]);
  STAMP(1, 5);
#pragma unroll
  for (int e = 0; e < 2; ++e)
#pragma unroll
    for (int bb = 0; bb < 4; ++bb) E2r[e * 36 + bb * 8] = v[e * 4 + bb];
  wave_sync();
  v[0] = E2w[0];
#pragma unroll
  for (int p = 1; p < 8; ++p) v[p] = cmulc(E2w[p * 36], twb[p - 1]);
  fft8<true>(v);
#pragma unroll
  for (int j2 = 0; j2 < 8; ++j2) E1[32 * j2] = v[j2];
  STAMP(1, 6);
  __syncthreads();
  STAMP(1, 7);
  v[0] = e1[tid];
#pragma unroll
  for (int p = 1; p < 8; ++p) v[p] = cmulc(e1[p * 256 + tid], twa[p - 1]);
  fft8<true>(v);
  if (valid) {
#pragma unroll
    for (int j1 = 0; j1 < 8; ++j1) st_wt(colbase + (size_t)j1 * 32 * TP + coff, v[j1]);
  }
  STAMP(1, 8);
  STAMP_END(1, 9);
}

template <int N> DEV void wait_vm() { asm volatile("s_waitcnt vmcnt(%0)" ::"n"(N) : "memory"); }
#define RAW_BAR() do { asm volatile("s_waitcnt lgkmcnt(0)" ::: "memory"); __builtin_amdgcn_s_barrier(); } while (0)

DEV void glds16(const void* src, char* lds) {
  __builtin_amdgcn_global_load_lds((const unsigned*)src, (unsigned*)lds, 16, 0, 0);
}
DEV uint32_t pk_relu_bf16(uint32_t p) {
  uint32_t r;
  asm("v_pk_max_i16 %0, %1, 0" : "=v"(r) : "v"(p));
  return r;
}

typedef short v4s_t __attribute__((ext_vector_type(4)));
typedef __attribute__((address_space(3))) v4s_t* lds_v4s_ptr;
DEV bf16x8 tr_frag(const char* p0, const char* p1) {
  const v4s_t a = __builtin_amdgcn_ds_read_tr16_b64_v4i16((lds_v4s_ptr)p0);
  const v4s_t b = __builtin_amdgcn_ds_read_tr16_b64_v4i16((lds_v4s_ptr)p1);
  return __builtin_shufflevector(a, b, 0, 1, 2, 3, 4, 5, 6, 7);
}

#define SGB(mask, n) __builtin_amdgcn_sched_group_barrier(mask, n, 0)
template <int NM, int ND> DEV void interleave_mfma_ds() {
  if constexpr (NM == 6) {
    SGB(0x008, 1); SGB(0x100, 2); SGB(0x008, 1); SGB(0x100, 2); SGB(0x008, 1); SGB(0x100, 2);
    SGB(0x008, 1); SGB(0x100, 2); SGB(0x008, 2);
  } else if constexpr (NM == 10) {
    SGB(0x008, 1); SGB(0x100, 2); SGB(0x008, 1); SGB(0x100, 2); SGB(0x008, 1); SGB(0x100, 2);
    SGB(0x008, 1); SGB(0x100, 2); SGB(0x008, 1); SGB(0x100, 2); SGB(0x008, 5);
  } else {
    SGB(0x008, 1); SGB(0x100, 2); SGB(0x008, 1); SGB(0x100, 2); SGB(0x008, 1); SGB(0x100, 2);
    SGB(0x008, 1); SGB(0x100, 2); SGB(0x008, 1); SGB(0x100, 2);
  }
}
DEV void wait_vm_rt(int n) {
  switch (n) {
    case 0: wait_vm<0>(); break;   case 1: wait_vm<1>(); break;   case 2: wait_vm<2>(); break;
    case 3: wait_vm<3>(); break;   case 4: wait_vm<4>(); break;   case 5: wait_vm<5>(); break;
    case 6: wait_vm<6>(); break;   case 7: wait_vm<7>(); break;   case 8: wait_vm<8>(); break;
    case 9: wait_vm<9>(); break;   case 10: wait_vm<10>(); break; case 11: wait_vm<11>(); break;
    case 12: wait_vm<12>(); break; case 13: wait_vm<13>(); break; case 14: wait_vm<14>(); break;
    default: wait_vm<0>(); break;
  }
}
DEV void glds16_dev(const void* src, char* lds) {
  __builtin_amdgcn_global_load_lds((const unsigned*)src, (unsigned*)lds, 16, 0, 16);
}
DEV void lds_spin(uint32_t* w, uint32_t want) {
  for (int i = 0; i < (1 << 24); ++i) {
    const uint32_t v = __hip_atomic_load(w, __ATOMIC_RELAXED, __HIP_MEMORY_SCOPE_WORKGROUP);
    if ((uint32_t)__builtin_amdgcn_readfirstlane(v) >= want) break;
    __builtin_amdgcn_s_sleep(1);
  }
  asm volatile("" ::: "memory");
}
DEV void wait_cluster(uint32_t* lcnt, uint32_t* lflag, uint32_t* cnt, int lw, int lane) {
  if (lw == 0) {
    lds_spin(lcnt, 8u);
    for (int i = 0; i < (1 << 22); ++i) {
      const uint32_t v = __hip_atomic_load(cnt, __ATOMIC_RELAXED, __HIP_MEMORY_SCOPE_AGENT);
      if ((uint32_t)__builtin_amdgcn_readfirstlane(v) >= 16u) break;
      __builtin_amdgcn_s_sleep(2);
    }
    asm volatile("" ::: "memory");
    if (lane == 0) __hip_atomic_store(lflag, 1u, __ATOMIC_RELAXED, __HIP_MEMORY_SCOPE_WORKGROUP);
  } else {
    lds_spin(lflag, 1u);
  }
}
DEV void publish(uint32_t* lcnt, uint32_t* gcnt, int lane) {
  asm volatile("s_waitcnt vmcnt(0)" ::: "memory");
  if (lane == 0) {
    const uint32_t old = __hip_atomic_fetch_add(lcnt, 1u, __ATOMIC_RELAXED, __HIP_MEMORY_SCOPE_WORKGROUP);
    if (old == 7u) {
      __hip_atomic_fetch_add(gcnt, 1u, __ATOMIC_RELAXED, __HIP_MEMORY_SCOPE_AGENT);
    }
  }
}

#undef STAMP_TID
#define STAMP_TID 0
__global__ __launch_bounds__(1024) void k_tail(const f32x2* __restrict__ T, const uint32_t* __restrict__ x,
                                               uint32_t* h, uint32_t* hn, u32x2* h1,
                                               const char* __restrict__ w1, const uint16_t* __restrict__ b1,
                                               const u32x4* __restrict__ w2p, const uint16_t* __restrict__ b2,
                                               uint16_t* __restrict__ out, uint32_t* cnt) {
  constexpr int BD_OFF = 0, AD_OFF = 6 * 12288;
  constexpr int BE_OFF = 0, NBE = 7, RES_OFF = NBE * 20480;
  constexpr int MISC_OFF = 163840 - 1024, B1_OFF = MISC_OFF, LC_OFF = MISC_OFF + 256, B2_OFF = MISC_OFF + 512;
  __shared__ __attribute__((aligned(16))) char lds[163840];
  const int tid = threadIdx.x, lane = tid & 63;
  const int wv = __builtin_amdgcn_readfirstlane(tid >> 6);
  const int xcd = blockIdx.x & 7, idx = blockIdx.x >> 3;
  const int cl = 2 * xcd + (idx >> 4), mi_ = idx & 15;
  const int cb = mi_, rb2 = 2 * cl + (mi_ >> 3), cb2 = mi_ & 7;
  uint32_t* cnt1 = cnt + cl * 64;
  uint32_t* cnt2 = cnt + (16 + cl) * 64;
  uint32_t* lcnt = (uint32_t*)(lds + LC_OFF);

  if (wv >= 8) {
    const int lw = wv - 8;
    const int nb = (lw < 4) ? 2 : 1, nb2 = (lw < 4) ? 3 : 2;
    const int arow = lane >> 3;
    const char* asrc = (const char*)hn + (size_t)(cl * 128 + 16 * lw + arow) * 1280 + (((lane & 7) ^ arow) << 4);
    const int id0 = lw * 64 + lane, r0 = id0 / 12, c0 = (id0 % 12) ^ (((r0 >> 3) & 1) << 1);
    const int id1 = (8 + lw) * 64 + lane, r1 = id1 / 12, c1 = (id1 % 12) ^ (((r1 >> 3) & 1) << 1);
    const char* bsrc0 = w1 + (size_t)r0 * 3072 + cb * 192 + c0 * 16;
    const char* bsrc1 = w1 + (size_t)r1 * 3072 + cb * 192 + c1 * 16;
    auto stageB = [&](int st) {
      char* dst = lds + BD_OFF + (st % 6) * 12288 + lane * 16;
      glds16(bsrc0 + (size_t)st * 64 * 3072, dst + lw * 1024);
      if (lw < 4) glds16(bsrc1 + (size_t)st * 64 * 3072, dst + (8 + lw) * 1024);
    };
    auto stageA = [&](int st) {
      char* dst = lds + AD_OFF + (st % 5) * 16384 + lane * 16;
      glds16_dev(asrc + st * 128, dst + (2 * lw) * 1024);
      glds16_dev(asrc + st * 128 + 8 * 1280, dst + (2 * lw + 1) * 1024);
    };
    if (lw == 7 && lane < 12) glds16((const u32x4*)(b1 + cb * 96) + lane, lds + B1_OFF + lane * 16);
    if (lw == 6 && lane < 10) glds16((const u32x4*)(b2 + cb2 * 80) + lane, lds + B2_OFF + lane * 16);
#pragma unroll
    for (int st = 0; st < 6; ++st) stageB(st);
    __builtin_amdgcn_s_barrier();
    wait_cluster(lcnt, lcnt + 2, cnt1, lw, lane);
#pragma unroll
    for (int st = 0; st < 5; ++st) stageA(st);
#pragma unroll
    for (int st = 0; st < 10; ++st) {
      const int ka = (st == 0) ? 8 : (st <= 6) ? 6 : (st == 7) ? 4 : (st == 8) ? 2 : 0;
      const int kb = (st <= 1) ? 0 : (st <= 5) ? st - 1 : (st == 6) ? 3 : (st == 7) ? 2 : (st == 8) ? 1 : 0;
      if (lw < 4) wait_vm_rt(ka + 2 * kb); else wait_vm_rt(ka + kb);
      __builtin_amdgcn_s_barrier();
      if (st >= 1 && st + 4 < 10) stageA(st + 4);
      if (st >= 1 && st + 5 < 10) stageB(st + 5);
    }
    __builtin_amdgcn_s_barrier();
    const u32x4* bsrc2 = w2p + (size_t)cb2 * 1536 * 10 + lw * 64 + lane;
    auto stageB2 = [&](int st) {
      char* dst = lds + BE_OFF + (st % NBE) * 20480 + lane * 16;
      glds16(bsrc2 + st * 1280, dst + lw * 1024);
      glds16(bsrc2 + st * 1280 + 512, dst + (8 + lw) * 1024);
      if (lw < 4) glds16(bsrc2 + st * 1280 + 1024, dst + (16 + lw) * 1024);
    };
#pragma unroll
    for (int st = 0; st < NEARLY; ++st) stageB2(st);
    if (NEARLY < NBE) {
      lds_spin(lcnt + 1, 8u);
#pragma unroll
      for (int st = NEARLY; st < NBE; ++st) stageB2(st);
    }
    {
      const int id = lw * 64 + lane, r = id / 10, ch = id % 10;
      glds16_dev((const u32x4*)((const uint16_t*)h + (size_t)(rb2 * 64 + r) * D_ + cb2 * 80) + ch, lds + RES_OFF + id * 16);
      if (lw >= 6) {
        const int id2 = (lw + 2) * 64 + lane, r2 = id2 / 10, ch2 = id2 % 10;
        glds16_dev((const u32x4*)((const uint16_t*)h + (size_t)(rb2 * 64 + r2) * D_ + cb2 * 80) + ch2, lds + RES_OFF + id2 * 16);
      }
    }
    if (lw == 0) wait_cluster(lcnt + 1, lcnt + 3, cnt2, lw, lane);
    wait_vm<0>();
#pragma unroll
    for (int st = 0; st < 12; ++st) {
      const int later = (st < NBE) ? 99 : ((st + 5 < 11 ? st + 5 : 11) - st);
      if (later != 99) { if (lw < 4) wait_vm_rt(3 * later); else wait_vm_rt(2 * later); }
      __builtin_amdgcn_s_barrier();
      if (st >= 1 && st + 6 < 12) stageB2(st + 6);
    }
    __builtin_amdgcn_s_barrier();
    (void)nb; (void)nb2;
    return;
  }

  STAMP(2, 0);
  {
    f32x2(*ex)[360] = (f32x2(*)[360])(lds + AD_OFF);
    f32x2(*xx)[384] = (f32x2(*)[384])(lds + AD_OFF + 23040);
    const int j = wv;
    const int s0 = 32 * xcd + idx;
    const int row = s0 + 256 * j;
    const int prow = s0 * 8 + j;
    f32x2 v[8];
    {
      const f32x2* src = T + (size_t)s0 * TP;
      const uint32_t kk = min(tid, DF_ - 1);
#pragma unroll
      for (int q = 0; q < 8; ++q) v[q] = (src + (size_t)q * 256 * TP)[kk];
    }
    uint32_t xd[5];
#pragma unroll
    for (int i = 0; i < 5; ++i) xd[i] = x[row * 320 + lane + 64 * i];
    if (tid < 4) lcnt[tid] = 0;

    f32x2 tw1[4], tw2[7], twq[7], twk[8];
#pragma unroll
    for (int r = 1; r < 5; ++r) tw1[r - 1] = twid((float)(lane * r) * (1.0f / 320.0f));
    const int l0 = lane & 7, rr = lane >> 3;
#pragma unroll
    for (int q = 1; q < 8; ++q) tw2[q - 1] = twid((float)(l0 * q) * (1.0f / 64.0f));
#pragma unroll
    for (int q = 1; q < 8; ++q) twq[q - 1] = twid((float)(s0 * q) * (1.0f / 2048.0f));
#pragma unroll
    for (int p = 0; p < 8; ++p) twk[p] = twid((float)(rr + 5 * l0 + 40 * p) * (1.0f / 640.0f));
    pin(tw1); pin(tw2); pin(twq); pin(twk);
    __builtin_amdgcn_sched_barrier(0);
    STAMP(2, 1);

    if (j < 6) {
#pragma unroll
      for (int q = 1; q < 8; ++q) v[q] = cmulc(v[q], twq[q - 1]);
      fft8<true>(v);
      f32x2 msk;
      msk.x = 1.0f; msk.y = (tid == 0 || tid == 320) ? 0.0f : 1.0f;
#pragma unroll
      for (int jj = 0; jj < 8; ++jj) xx[jj][tid] = v[jj] * msk;
    }
    RAW_BAR();
    asm volatile("" ::: "memory");
    f32x2* E = ex[j];
    if (lane < 40) {
      f32x2* Er = E + rr * 72;
      const f32x2* Xa = xx[j] + rr + 5 * l0;
      const f32x2* Xb = xx[j] + 320 - rr - 5 * l0;
#pragma unroll
      for (int p = 0; p < 8; ++p) {
        const f32x2 a = Xa[40 * p], b = Xb[-40 * p];
        f32x2 e, d;
        e.x = a.x + b.x; e.y = a.y - b.y;
        d.x = a.x - b.x; d.y = a.y + b.y;
        v[p] = cadd_pi(e, cmulc(d, twk[p]));
      }
      fft8<true>(v);
      Er[l0 * 9] = v[0];
#pragma unroll
      for (int a = 1; a < 8; ++a) Er[l0 * 9 + a] = cmulc(v[a], tw2[a - 1]);
      wave_sync();
#pragma unroll
      for (int q = 0; q < 8; ++q) v[q] = Er[q * 9 + l0];
      fft8<true>(v);
      wave_sync();
#pragma unroll
      for (int l1 = 0; l1 < 8; ++l1) Er[l0 + 8 * l1] = v[l1];
    }
    wave_sync();
    f32x2 u[5];
    u[0] = E[lane];
#pragma unroll
    for (int r = 1; r < 5; ++r) u[r] = cmulc(E[r * 72 + lane], tw1[r - 1]);
    fft5<true>(u);

    const f32x2 sc = {1.0f / 2621440.0f, 1.0f / 2621440.0f};
    f32x2 hv[5];
    uint32_t zp[5];
#pragma unroll
    for (int i = 0; i < 5; ++i) {
      const uint32_t hp = bf_pack(bf_unpack(xd[i]) + bf_round(u[i] * sc));
      st_wt32(h + prow * 320 + lane + 64 * i, hp);
      hv[i] = bf_unpack(hp);
    }
    row_layernorm(hv, zp);
#pragma unroll
    for (int i = 0; i < 5; ++i) st_wt32(hn + prow * 320 + lane + 64 * i, zp[i]);
    STAMP(2, 2);
    publish(lcnt, cnt1, lane);
    STAMP(2, 3);
  }
  const int g = lane >> 4, q4 = (lane & 15) >> 2, p4 = lane & 3;
  {
    const int wm = wv >> 1, wn = wv & 1;
    f32x4 acc[2][3];
#pragma unroll
    for (int mi = 0; mi < 2; ++mi)
#pragma unroll
      for (int ni = 0; ni < 3; ++ni) acc[mi][ni] = (f32x4){0.f, 0.f, 0.f, 0.f};
    const int ar = 32 * wm + (lane & 15);
    const int aoff0 = AD_OFF + ar * 128 + (((lane >> 4) ^ (ar & 7)) << 4);
    const int aoff1 = AD_OFF + ar * 128 + (((4 + (lane >> 4)) ^ (ar & 7)) << 4);
    int boff[3];
#pragma unroll
    for (int ni = 0; ni < 3; ++ni) boff[ni] = BD_OFF + (8 * g + q4) * 192 + p4 * 8 + (((3 * wn + ni) ^ (g & 1)) << 5);
    bf16x8 af0[2], bf0[3], af1[2], bf1[3];
    auto rdD = [&](bf16x8 (&af)[2], bf16x8 (&bf)[3], int st, int ks) {
      int sa = (st % 5) * 16384, sb = (st % 6) * 12288;
      asm volatile("" : "+s"(sa), "+s"(sb));
#pragma unroll
      for (int mi = 0; mi < 2; ++mi) af[mi] = *(const bf16x8*)(lds + sa + (ks ? aoff1 : aoff0) + mi * 2048);
#pragma unroll
      for (int ni = 0; ni < 3; ++ni)
        bf[ni] = tr_frag(lds + sb + boff[ni] + ks * 6144, lds + sb + boff[ni] + ks * 6144 + 768);
    };
    auto mmD = [&](const bf16x8 (&af)[2], const bf16x8 (&bf)[3]) {
#pragma unroll
      for (int mi = 0; mi < 2; ++mi)
#pragma unroll
        for (int ni = 0; ni < 3; ++ni)
          acc[mi][ni] = __builtin_amdgcn_mfma_f32_16x16x32_bf16(bf[ni], af[mi], acc[mi][ni], 0, 0, 0);
    };
    RAW_BAR();
    STAMP(2, 4);
    rdD(af0, bf0, 0, 0);
#pragma unroll
    for (int st = 0; st < 10; ++st) {
      rdD(af1, bf1, st, 1);
      mmD(af0, bf0);
      interleave_mfma_ds<6, 8>();
      __builtin_amdgcn_sched_barrier(0);
      RAW_BAR();
      __builtin_amdgcn_sched_barrier(0);
      if (st < 9) rdD(af0, bf0, st + 1, 0);
      mmD(af1, bf1);
      if (st < 9) interleave_mfma_ds<6, 8>();
      __builtin_amdgcn_sched_barrier(0);
    }
    STAMP(2, 5);
#pragma unroll
    for (int ni = 0; ni < 3; ++ni) {
      const u32x2 bb = *(const u32x2*)(lds + B1_OFF + ((3 * wn + ni) * 16 + 4 * (lane >> 4)) * 2);
      const f32x2 b01 = bf_unpack(bb.x), b23 = bf_unpack(bb.y);
      const int NT = cb * 6 + 3 * wn + ni;
      const int ks2 = NT >> 1, gg = 2 * (NT & 1) + (lane >> 5), half = (lane >> 4) & 1;
#pragma unroll
      for (int mi = 0; mi < 2; ++mi) {
        f32x2 a01 = {acc[mi][ni][0], acc[mi][ni][1]}, a23 = {acc[mi][ni][2], acc[mi][ni][3]};
        a01 = bf_unpack(pk_relu_bf16(bf_pack(bf_round(a01) + b01)));
        a23 = bf_unpack(pk_relu_bf16(bf_pack(bf_round(a23) + b23)));
        u32x2 ov;
        ov.x = bf_pack(a01 * a01); ov.y = bf_pack(a23 * a23);
        const int mt = cl * 8 + 2 * wm + mi;
        st_wt64(h1 + ((((mt >> 2) * 48 + ks2) * 4 + (mt & 3)) * 64 + (lane & 15) + 16 * gg) * 2 + half, ov);
      }
    }
    publish(lcnt + 1, cnt2, lane);
    STAMP(2, 6);
  }
  {
    const int wmp = wv & 1, wk = wv >> 1;
    f32x4 acc[2][5];
#pragma unroll
    for (int mi = 0; mi < 2; ++mi)
#pragma unroll
      for (int ni = 0; ni < 5; ++ni) acc[mi][ni] = (f32x4){0.f, 0.f, 0.f, 0.f};
    const int boff = BE_OFF + (16 * (g >> 1) + 4 * (g & 1) + q4 + 32 * wk) * 160 + p4 * 8;
    const __amdgpu_buffer_rsrc_t rs = __builtin_amdgcn_make_buffer_rsrc((void*)h1, 0, S_ * F_ * 2, 0x00020000);
    const int avoff = ((rb2 * 48 + wk) * 4 + 2 * wmp) * 1024 + lane * 16;
    bf16x8 af[12][2], bfr[2][5];
    auto ldA = [&](int st) {
      af[st][0] = __builtin_bit_cast(bf16x8, __builtin_amdgcn_raw_buffer_load_b128(rs, avoff, st * 16384, 16));
      af[st][1] = __builtin_bit_cast(bf16x8, __builtin_amdgcn_raw_buffer_load_b128(rs, avoff + 1024, st * 16384, 16));
    };
    auto rdE = [&](bf16x8 (&bf)[5], int st) {
      int sb = (st % NBE) * 20480;
      asm volatile("" : "+s"(sb));
#pragma unroll
      for (int ni = 0; ni < 5; ++ni) bf[ni] = tr_frag(lds + sb + boff + ni * 32, lds + sb + boff + ni * 32 + 1280);
    };
    constexpr int APF = 4;
    lds_spin(lcnt + 3, 1u);
#pragma unroll
    for (int i = 0; i < APF; ++i) ldA(i);
    RAW_BAR();
    STAMP(2, 7);
    rdE(bfr[0], 0);
#pragma unroll
    for (int st = 0; st < 12; ++st) {
      if (st < 11) {
        RAW_BAR();
        __builtin_amdgcn_sched_barrier(0);
        rdE(bfr[(st + 1) & 1], st + 1);
      }
#pragma unroll
      for (int ni = 0; ni < 5; ++ni)
#pragma unroll
        for (int mi = 0; mi < 2; ++mi)
          acc[mi][ni] = __builtin_amdgcn_mfma_f32_16x16x32_bf16(bfr[st & 1][ni], af[st][mi], acc[mi][ni], 0, 0, 0);
      if (st < 11) interleave_mfma_ds<10, 10>();
      __builtin_amdgcn_sched_barrier(0);
      if (st + APF < 12) ldA(st + APF);
    }
    STAMP(2, 8);
    f32x4* red = (f32x4*)(lds + BE_OFF) + (wmp * 40) * 64 + lane;
#pragma unroll
    for (int t = 0; t < 10; ++t)
      if ((t & 3) != wk) red[(t * 4 + wk) * 64] = acc[t / 5][t % 5];
    RAW_BAR();
    asm volatile("" ::: "memory");
#pragma unroll
    for (int t = 0; t < 10; ++t) {
      if ((t & 3) == wk) {
        const int mi = t / 5, ni = t % 5;
        f32x4 o = acc[mi][ni];
#pragma unroll
        for (int w2_ = 0; w2_ < 4; ++w2_)
          if (w2_ != (t & 3)) o += red[(t * 4 + w2_) * 64];
        const int lr = 32 * wmp + 16 * mi + (lane & 15);
        const int prow = rb2 * 64 + lr;
        const int mrow = (prow >> 3) + 256 * (prow & 7);
        const int nc = 16 * ni + 4 * (lane >> 4);
        const u32x2 bb = *(const u32x2*)(lds + B2_OFF + nc * 2);
        const u32x2 rsd = *(const u32x2*)(lds + RES_OFF + lr * 160 + nc * 2);
        f32x2 a01 = {o[0], o[1]}, a23 = {o[2], o[3]};
        u32x2 ov;
        ov.x = bf_pack(bf_unpack(rsd.x) + bf_round(bf_round(a01) + bf_unpack(bb.x)));
        ov.y = bf_pack(bf_unpack(rsd.y) + bf_round(bf_round(a23) + bf_unpack(bb.y)));
        *(u32x2*)(out + (size_t)mrow * D_ + cb2 * 80 + nc) = ov;
      }
    }
  }
  STAMP_END(2, 9);
}

__global__ void k_zz_end() {}
extern "C" void kernel_launch(void* const* d_in, const int* in_sizes, int n_in, void* d_out, int out_size,
                              void* d_ws, size_t ws_size, hipStream_t stream) {
  const uint32_t* x = (const uint32_t*)d_in[0];
  const uint16_t* wr = (const uint16_t*)d_in[1];
  const uint16_t* wi = (const uint16_t*)d_in[2];
  const uint16_t* w1 = (const uint16_t*)d_in[4];
  const uint16_t* b1 = (const uint16_t*)d_in[5];
  const uint16_t* w2 = (const uint16_t*)d_in[6];
  const uint16_t* b2 = (const uint16_t*)d_in[7];
  char* ws = (char*)d_ws;
  size_t off = 0;
  f32x2* T = (f32x2*)(ws + off); off += (size_t)8 * 256 * TP * 8;
  uint32_t* h = (uint32_t*)(ws + off); off += (size_t)S_ * D_ * 2;
  uint32_t* hn = (uint32_t*)(ws + off); off += (size_t)S_ * D_ * 2;
  u32x4* h1 = (u32x4*)(ws + off); off += (size_t)S_ * F_ * 2;
  u32x4* w2p = (u32x4*)(ws + off); off += (size_t)D_ * F_ * 2;

  uint32_t* cnt = (uint32_t*)(ws + off); off += 32 * 256;
  k_fwd_rows<<<256, 512, 0, stream>>>(x, T, wr, wi, (const uint32_t*)w1, (const u32x4*)w2, w2p, cnt);
  k_cols<<<328, 256, 0, stream>>>(T, wr, wi);
  k_tail<<<256, 1024, 0, stream>>>(T, x, h, hn, (u32x2*)h1, (const char*)w1, b1, w2p, b2, (uint16_t*)d_out, cnt);
}
```

```cpp
#define HELPER_DELAY 0
#include <hip/hip_runtime.h>
#include <hip/hip_bf16.h>
#include <stdint.h>

typedef float f32x2 __attribute__((ext_vector_type(2)));
typedef float f32x4 __attribute__((ext_vector_type(4)));
typedef short bf16x8 __attribute__((ext_vector_type(8)));
typedef unsigned int u32x4 __attribute__((ext_vector_type(4)));
typedef unsigned int u32x2 __attribute__((ext_vector_type(2)));

#define DEV __device__ __forceinline__

#define STAMP(kern, i)
#define STAMP_END(kern, i)
#define STAMP_TID 0

static constexpr int S_ = 2048, D_ = 640, F_ = 1536, DF_ = 321;
static constexpr int TP = 328;
static constexpr int NEARLY = 4;

DEV float bf_lo(uint32_t p) { return __builtin_bit_cast(float, p << 16); }
DEV float bf_hi(uint32_t p) { return __builtin_bit_cast(float, p & 0xffff0000u); }
DEV f32x2 bf_unpack(uint32_t p) { f32x2 r; r.x = bf_lo(p); r.y = bf_hi(p); return r; }
typedef __bf16 bf16x2_t __attribute__((ext_vector_type(2)));
DEV uint32_t bf_pack(f32x2 v) {
  return __builtin_bit_cast(uint32_t, __builtin_convertvector(v, bf16x2_t));
}
DEV f32x2 bf_round(f32x2 v) { return bf_unpack(bf_pack(v)); }
DEV float bfr(float f) { f32x2 v; v.x = f; v.y = f; return bf_lo(bf_pack(v)); }

DEV f32x2 cadd_mi(f32x2 b, f32x2 d) {
  f32x2 r;
  asm("v_pk_add_f32 %0, %1, %2 op_sel:[0,1] op_sel_hi:[1,0] neg_hi:[0,1]" : "=v"(r) : "v"(b), "v"(d));
  return r;
}
DEV f32x2 cadd_pi(f32x2 b, f32x2 d) {
  f32x2 r;
  asm("v_pk_add_f32 %0, %1, %2 op_sel:[0,1] op_sel_hi:[1,0] neg_lo:[0,1]" : "=v"(r) : "v"(b), "v"(d));
  return r;
}
DEV f32x2 cnadd_mi(f32x2 b, f32x2 d) {
  f32x2 r;
  asm("v_pk_add_f32 %0, %1, %2 op_sel:[0,1] op_sel_hi:[1,0] neg_lo:[1,0] neg_hi:[1,1]" : "=v"(r) : "v"(b), "v"(d));
  return r;
}
DEV f32x2 cnadd_pi(f32x2 b, f32x2 d) {
  f32x2 r;
  asm("v_pk_add_f32 %0, %1, %2 op_sel:[0,1] op_sel_hi:[1,0] neg_lo:[1,1] neg_hi:[1,0]" : "=v"(r) : "v"(b), "v"(d));
  return r;
}
DEV f32x2 cfma_mi(f32x2 s, f32x2 d, f32x2 b) {
  f32x2 r;
  asm("v_pk_fma_f32 %0, %1, %2, %3 op_sel:[0,1,0] op_sel_hi:[1,0,1] neg_hi:[0,1,0]" : "=v"(r) : "v"(s), "v"(d), "v"(b));
  return r;
}
DEV f32x2 cfma_pi(f32x2 s, f32x2 d, f32x2 b) {
  f32x2 r;
  asm("v_pk_fma_f32 %0, %1, %2, %3 op_sel:[0,1,0] op_sel_hi:[1,0,1] neg_lo:[0,1,0]" : "=v"(r) : "v"(s), "v"(d), "v"(b));
  return r;
}
DEV f32x2 pfma(f32x2 a, f32x2 b, f32x2 c) { return __builtin_elementwise_fma(a, b, c); }
DEV f32x2 cmul(f32x2 a, f32x2 w) {
  f32x2 t, r;
  asm("v_pk_mul_f32 %0, %1, %2 op_sel:[0,0] op_sel_hi:[0,1]" : "=v"(t) : "v"(a), "v"(w));
  asm("v_pk_fma_f32 %0, %1, %2, %3 op_sel:[1,1,0] op_sel_hi:[1,0,1] neg_lo:[0,1,0]" : "=v"(r) : "v"(a), "v"(w), "v"(t));
  return r;
}
DEV f32x2 cmulc(f32x2 a, f32x2 w) {
  f32x2 t, r;
  asm("v_pk_mul_f32 %0, %1, %2 op_sel:[0,0] op_sel_hi:[0,1] neg_hi:[0,1]" : "=v"(t) : "v"(a), "v"(w));
  asm("v_pk_fma_f32 %0, %1, %2, %3 op_sel:[1,1,0] op_sel_hi:[1,0,1]" : "=v"(r) : "v"(a), "v"(w), "v"(t));
  return r;
}
DEV f32x2 twid(float f) {
  f32x2 r;
  r.x = __builtin_amdgcn_cosf(f);
  r.y = __builtin_amdgcn_sinf(-f);
  return r;
}
DEV void st_wt(f32x2* p, f32x2 v) {
  __hip_atomic_store((unsigned long long*)p, __builtin_bit_cast(unsigned long long, v), __ATOMIC_RELAXED,
                     __HIP_MEMORY_SCOPE_AGENT);
}
DEV void st_wt32(uint32_t* p, uint32_t v) { __hip_atomic_store(p, v, __ATOMIC_RELAXED, __HIP_MEMORY_SCOPE_AGENT); }
DEV void st_wt64(u32x2* p, u32x2 v) {
  __hip_atomic_store((unsigned long long*)p, __builtin_bit_cast(unsigned long long, v), __ATOMIC_RELAXED,
                     __HIP_MEMORY_SCOPE_AGENT);
}
DEV void st_wt128(void* p, u32x4 v) {
  asm volatile("global_store_dwordx4 %0, %1, off sc1\n\ts_nop 1" ::"v"(p), "v"(v) : "memory");
}
template <int N> DEV void pin(f32x2 (&a)[N]) {
#pragma unroll
  for (int i = 0; i < N; ++i) asm volatile("" : "+v"(a[i]));
}
template <bool INV> DEV f32x2 add_rot(f32x2 b, f32x2 d) { return INV ? cadd_pi(b, d) : cadd_mi(b, d); }
template <bool INV> DEV f32x2 sub_rot(f32x2 b, f32x2 d) { return INV ? cadd_mi(b, d) : cadd_pi(b, d); }

template <bool INV> DEV void fft4(f32x2& a0, f32x2& a1, f32x2& a2, f32x2& a3) {
  f32x2 b0 = a0 + a2, b1 = a0 - a2, b2 = a1 + a3, d = a1 - a3;
  a0 = b0 + b2; a2 = b0 - b2;
  a1 = add_rot<INV>(b1, d); a3 = sub_rot<INV>(b1, d);
}
template <bool INV> DEV void fft8(f32x2 (&v)[8]) {
  const f32x2 r = {0.70710678118654752440f, 0.70710678118654752440f};
  const f32x2 nr = {-0.70710678118654752440f, -0.70710678118654752440f};
  f32x2 e0 = v[0] + v[4], e1 = v[1] + v[5], e2 = v[2] + v[6], e3 = v[3] + v[7];
  f32x2 o0 = v[0] - v[4], o1 = v[1] - v[5], o2 = v[2] - v[6], o3 = v[3] - v[7];
  fft4<INV>(e0, e1, e2, e3);
  f32x2 p1 = INV ? cadd_pi(o1, o1) : cadd_mi(o1, o1);
  f32x2 p3 = INV ? cnadd_pi(o3, o3) : cnadd_mi(o3, o3);
  f32x2 s = p1 + p3, d = p1 - p3;
  f32x2 b0 = add_rot<INV>(o0, o2), b1 = sub_rot<INV>(o0, o2);
  v[0] = e0; v[2] = e1; v[4] = e2; v[6] = e3;
  v[1] = pfma(r, s, b0); v[5] = pfma(nr, s, b0);
  v[3] = INV ? cfma_pi(r, d, b1) : cfma_mi(r, d, b1);
  v[7] = INV ? cfma_mi(r, d, b1) : cfma_pi(r, d, b1);
}
template <bool INV> DEV void fft5(f32x2 (&v)[5]) {
  const f32x2 c1 = {0.30901699437494742410f, 0.30901699437494742410f};
  const f32x2 c2 = {-0.80901699437494742410f, -0.80901699437494742410f};
  const f32x2 s1 = {0.95105651629515357212f, 0.95105651629515357212f};
  const f32x2 s2 = {0.58778525229247312917f, 0.58778525229247312917f};
  const f32x2 ns1 = {-0.95105651629515357212f, -0.95105651629515357212f};
  f32x2 t1 = v[1] + v[4], t2 = v[2] + v[3], t3 = v[1] - v[4], t4 = v[2] - v[3];
  f32x2 a1 = pfma(c2, t2, pfma(c1, t1, v[0]));
  f32x2 a2 = pfma(c1, t2, pfma(c2, t1, v[0]));
  f32x2 b1 = pfma(s2, t4, s1 * t3);
  f32x2 b2 = pfma(ns1, t4, s2 * t3);
  v[0] = v[0] + t1 + t2;
  v[1] = add_rot<INV>(a1, b1); v[4] = sub_rot<INV>(a1, b1);
  v[2] = add_rot<INV>(a2, b2); v[3] = sub_rot<INV>(a2, b2);
}

template <int CTRL> DEV float dppmov(float v) {
  return __builtin_bit_cast(float, __builtin_amdgcn_update_dpp(0, __builtin_bit_cast(int, v), CTRL, 0xf, 0xf, false));
}
DEV float wave_sum(float v) {
  v += dppmov<0xB1>(v);
  v += dppmov<0x4E>(v);
  v += dppmov<0x141>(v);
  v += dppmov<0x140>(v);
  int iv = __builtin_bit_cast(int, v);
  float a = __builtin_bit_cast(float, __builtin_amdgcn_readlane(iv, 0));
  float b = __builtin_bit_cast(float, __builtin_amdgcn_readlane(iv, 16));
  float c = __builtin_bit_cast(float, __builtin_amdgcn_readlane(iv, 32));
  float d = __builtin_bit_cast(float, __builtin_amdgcn_readlane(iv, 48));
  return (a + b) + (c + d);
}
DEV void wave_sync() {
  __builtin_amdgcn_fence(__ATOMIC_RELEASE, "wavefront");
  __builtin_amdgcn_wave_barrier();
  __builtin_amdgcn_fence(__ATOMIC_ACQUIRE, "wavefront");
}

DEV void row_layernorm(const f32x2 (&xv)[5], uint32_t (&zp)[5]) {
  f32x2 s2 = (xv[0] + xv[1]) + (xv[2] + xv[3]) + xv[4];
  const float mean = wave_sum(s2.x + s2.y) * (1.0f / 640.0f);
  const f32x2 nm = {-mean, -mean};
  f32x2 q2 = {0.f, 0.f};
#pragma unroll
  for (int i = 0; i < 5; ++i) { f32x2 d = xv[i] + nm; q2 = pfma(d, d, q2); }
  const float var = wave_sum(q2.x + q2.y) * (1.0f / 640.0f);
  f32x2 mv; mv.x = mean; mv.y = var;
  mv = bf_round(mv);
  const float den = bfr(sqrtf(bfr(mv.y + 1e-5f)));
  const float rden = 1.0f / den;
  const f32x2 nmb = {-mv.x, -mv.x}, rd = {rden, rden};
#pragma unroll
  for (int i = 0; i < 5; ++i) zp[i] = bf_pack(bf_round(xv[i] + nmb) * rd);
}

__global__ __launch_bounds__(1024) void k_fwd_rows(const uint32_t* __restrict__ x, f32x2* __restrict__ T,
                                                  const uint16_t* __restrict__ wr, const uint16_t* __restrict__ wi,
                                                  const uint32_t* __restrict__ w1, const u32x4* __restrict__ w2,
                                                  u32x4* __restrict__ w2p, uint32_t* __restrict__ cnt) {
  __shared__ f32x2 ex[8][360];
  __shared__ f32x2 zz[8][320];
  if (threadIdx.x >= 512) {
    const int tid = threadIdx.x - 512, lane = tid & 63, j = tid >> 6;
    if (HELPER_DELAY) __builtin_amdgcn_s_sleep(HELPER_DELAY);
  const int wid = min((int)blockIdx.x * 512 + tid, 122879);
  const u32x4 wchunk = w2[wid];

  uint32_t pf = 0;
  {
    const char* pa = nullptr;
    if (lane < 12) {
      const int t = (blockIdx.x & 7) + 8 * (8 * (blockIdx.x >> 3) + j);
      pa = (const char*)(((lane < 6) ? wr : wi) + (t * DF_ + min(64 * (lane % 6), DF_ - 2)));
      pa = (const char*)((uintptr_t)pa & ~(uintptr_t)3);
    } else if (lane >= 56) {
      pa = (const char*)(w1 + (blockIdx.x * 60 + min(j * 8 + (lane - 56), 59)) * 32);
    }
    else if (lane < 18) {
      unsigned long long c0, c1;
      asm volatile("s_getpc_b64 s[40:41]\n\ts_add_u32 s40, s40, _Z6k_colsPDv2_fPKtS2_@rel32@lo+4\n\ts_addc_u32 s41, s41, _Z6k_colsPDv2_fPKtS2_@rel32@hi+12\n\ts_mov_b64 %0, s[40:41]" : "=s"(c0) :: "s40", "s41", "scc");
      asm volatile("s_getpc_b64 s[40:41]\n\ts_add_u32 s40, s40, _Z8k_zz_endv@rel32@lo+4\n\ts_addc_u32 s41, s41, _Z8k_zz_endv@rel32@hi+12\n\ts_mov_b64 %0, s[40:41]" : "=s"(c1) :: "s40", "s41", "scc");
      const long long len = (long long)(c1 - c0);
      const int li = ((blockIdx.x >> 3) * 8 + j) * 2 + (lane - 16);
      if (lane >= 16 && len > 0 && len < 65536 && li * 128 < len) pa = (const char*)(c0 + li * 128);
    }
    if (pa) pf = *(const uint32_t*)pa;
  }

  if (blockIdx.x < 240) {
    const int wk_ = wid / 80, wc_ = wid % 80;
    const int rho = (wk_ & ~12) | ((wk_ & 4) << 1) | ((wk_ & 8) >> 1);
    st_wt128(w2p + ((wc_ / 10) * 1536 + rho) * 10 + (wc_ % 10), wchunk);
  }
  asm volatile("" ::"v"(pf));
    return;
  }
  const int tid = threadIdx.x, lane = tid & 63, j = tid >> 6;
  STAMP(0, 0);
  if (blockIdx.x == 0 && tid < 64) st_wt32(cnt + tid * 32, 0u);
  const int s0 = 32 * (blockIdx.x & 7) + (blockIdx.x >> 3);
  const int row = s0 + 256 * j;
  uint32_t xd[5];
#pragma unroll
  for (int i = 0; i < 5; ++i) xd[i] = x[row * 320 + lane + 64 * i];
  f32x2 tw1[4], tw2[7], twq[7];
#pragma unroll
  for (int r = 1; r < 5; ++r) tw1[r - 1] = twid((float)(lane * r) * (1.0f / 320.0f));
  const int l0 = lane & 7, rr = lane >> 3;
#pragma unroll
  for (int q = 1; q < 8; ++q) tw2[q - 1] = twid((float)(l0 * q) * (1.0f / 64.0f));
#pragma unroll
  for (int q = 1; q < 8; ++q) twq[q - 1] = twid((float)(s0 * q) * (1.0f / 2048.0f));
  f32x2 twk_[1] = {twid((float)tid * (1.0f / 640.0f))};
  pin(tw1); pin(tw2); pin(twq); pin(twk_);
  const f32x2 twk = twk_[0];
  __builtin_amdgcn_sched_barrier(0);
  STAMP(0, 1);

  f32x2 u[5];
  {
    f32x2 xv[5];
    uint32_t zp[5];
#pragma unroll
    for (int i = 0; i < 5; ++i) xv[i] = bf_unpack(xd[i]);
    row_layernorm(xv, zp);
#pragma unroll
    for (int i = 0; i < 5; ++i) u[i] = bf_unpack(zp[i]);
  }
  STAMP(0, 2);
  f32x2* E = ex[j];
  fft5<false>(u);
  E[lane] = u[0];
#pragma unroll
  for (int r = 1; r < 5; ++r) E[r * 72 + lane] = cmul(u[r], tw1[r - 1]);
  wave_sync();
  if (lane < 40) {
    f32x2 v[8];
    f32x2* Er = E + rr * 72;
#pragma unroll
    for (int l1 = 0; l1 < 8; ++l1) v[l1] = Er[l0 + 8 * l1];
    fft8<false>(v);
    wave_sync();
    Er[l0] = v[0];
#pragma unroll
    for (int q = 1; q < 8; ++q) Er[q * 9 + l0] = cmul(v[q], tw2[q - 1]);
    wave_sync();
#pragma unroll
    for (int a = 0; a < 8; ++a) v[a] = Er[l0 * 9 + a];
    fft8<false>(v);
    f32x2* Z = zz[j] + rr + 5 * l0;
#pragma unroll
    for (int p = 0; p < 8; ++p) Z[40 * p] = v[p];
  }
  STAMP(0, 3);
  __syncthreads();
  STAMP(0, 4);
  if (j < 6) {
    const int k = tid;
    const int ka = (k >= 320) ? 0 : k, kb = (k == 0 || k >= 320) ? 0 : 320 - k;
    f32x2 v[8];
#pragma unroll
    for (int jj = 0; jj < 8; ++jj) {
      const f32x2 a = zz[jj][ka], b = zz[jj][kb];
      f32x2 e, d;
      e.x = a.x + b.x; e.y = a.y - b.y;
      d.x = a.x - b.x; d.y = a.y + b.y;
      v[jj] = cadd_mi(e, cmul(d, twk));
    }
    fft8<false>(v);
    if (k < DF_) {
      f32x2* dst = T + (size_t)s0 * TP;
      st_wt(dst + k, v[0]);
#pragma unroll
      for (int q = 1; q < 8; ++q) st_wt(dst + (size_t)q * 256 * TP + k, cmul(v[q], twq[q - 1]));
    }
  }
  STAMP(0, 5);
  STAMP_END(0, 6);
}

__global__ __launch_bounds__(256) void k_cols(f32x2* __restrict__ T, const uint16_t* __restrict__ wr,
                                              const uint16_t* __restrict__ wi) {
  __shared__ f32x2 e1[2048];
  __shared__ f32x2 e2[4][576];
  __shared__ __attribute__((aligned(16))) uint32_t wl[2048];
  const int tid = threadIdx.x, lane = tid & 63, wv = tid >> 6;
  STAMP(1, 0);
  const int q = blockIdx.x & 7, kg = blockIdx.x >> 3;
  const int c = tid & 7, a = tid >> 3;
  const int k = kg * 8 + c;
  const bool valid = k < DF_;
  f32x2* colbase = T + (size_t)(q * 256) * TP;
  const uint32_t coff = a * TP + (valid ? k : DF_ - 1);
  f32x2 v[8];
#pragma unroll
  for (int j1 = 0; j1 < 8; ++j1) v[j1] = (colbase + (size_t)j1 * 32 * TP)[coff];
  const int kb = min(8 * kg, DF_ - 8);
  u32x4 wre, wim;
  __builtin_memcpy(&wre, wr + (size_t)(q + 8 * tid) * DF_ + kb, 16);
  __builtin_memcpy(&wim, wi + (size_t)(q + 8 * tid) * DF_ + kb, 16);

  const int b = (lane >> 3) & 3, p1lo = lane >> 5, p1 = 2 * wv + p1lo;
  f32x2 twa[7], twb[7];
#pragma unroll
  for (int p = 1; p < 8; ++p) twa[p - 1] = twid((float)(a * p) * (1.0f / 256.0f));
#pragma unroll
  for (int p = 1; p < 8; ++p) twb[p - 1] = twid((float)(b * p) * (1.0f / 32.0f));
  pin(twa); pin(twb);
  __builtin_amdgcn_sched_barrier(0);
  STAMP(1, 1);

  fft8<false>(v);
  e1[tid] = v[0];
#pragma unroll
  for (int p = 1; p < 8; ++p) e1[p * 256 + tid] = cmul(v[p], twa[p - 1]);
  {
    u32x4 o0, o1;
    o0.x = __builtin_amdgcn_perm(wim.x, wre.x, 0x05040100); o0.y = __builtin_amdgcn_perm(wim.x, wre.x, 0x07060302);
    o0.z = __builtin_amdgcn_perm(wim.y, wre.y, 0x05040100); o0.w = __builtin_amdgcn_perm(wim.y, wre.y, 0x07060302);
    o1.x = __builtin_amdgcn_perm(wim.z, wre.z, 0x05040100); o1.y = __builtin_amdgcn_perm(wim.z, wre.z, 0x07060302);
    o1.z = __builtin_amdgcn_perm(wim.w, wre.w, 0x05040100); o1.w = __builtin_amdgcn_perm(wim.w, wre.w, 0x07060302);
    *(u32x4*)(wl + tid * 8) = o0;
    *(u32x4*)(wl + tid * 8 + 4) = o1;
  }
  STAMP(1, 2);
  __syncthreads();
  STAMP(1, 3);
  uint32_t wf[8];
  {
    const uint32_t* wlp = wl + (p1 + 16 * b) * 8 + min(8 * kg + c - kb, 7);
#pragma unroll
    for (int e = 0; e < 2; ++e)
#pragma unroll
      for (int mm = 0; mm < 4; ++mm) wf[e * 4 + mm] = wlp[(8 * e + 64 * mm) * 8];
  }
  f32x2* E1 = e1 + p1 * 256 + (lane & 31);
#pragma unroll
  for (int j2 = 0; j2 < 8; ++j2) v[j2] = E1[32 * j2];
  fft8<false>(v);
  f32x2* E2w = e2[wv] + p1lo * 288 + (lane & 31);
  E2w[0] = v[0];
#pragma unroll
  for (int p = 1; p < 8; ++p) E2w[p * 36] = cmul(v[p], twb[p - 1]);
  wave_sync();
  f32x2* E2r = e2[wv] + p1lo * 288 + (2 * b) * 36 + c;
#pragma unroll
  for (int e = 0; e < 2; ++e)
#pragma unroll
    for (int bb = 0; bb < 4; ++bb) v[e * 4 + bb] = E2r[e * 36 + bb * 8];
  fft4<false>(v[0], v[1], v[2], v[3]);
  fft4<false>(v[4], v[5], v[6], v[7]);
  STAMP(1, 4);
#pragma unroll
  for (int i = 0; i < 8; ++i) v[i] = cmul(v[i], bf_unpack(wf[i]));
  fft4<true>(v[0], v[1], v[2], v[3]);
  fft4<true>(v[4], v[5], v[6], v[7]);
  STAMP(1, 5);
#pragma unroll
  for (int e = 0; e < 2; ++e)
#pragma unroll
    for (int bb = 0; bb < 4; ++bb) E2r[e * 36 + bb * 8] = v[e * 4 + bb];
  wave_sync();
  v[0] = E2w[0];
#pragma unroll
  for (int p = 1; p < 8; ++p) v[p] = cmulc(E2w[p * 36], twb[p - 1]);
  fft8<true>(v);
#pragma unroll
  for (int j2 = 0; j2 < 8; ++j2) E1[32 * j2] = v[j2];
  STAMP(1, 6);
  __syncthreads();
  STAMP(1, 7);
  v[0] = e1[tid];
#pragma unroll
  for (int p = 1; p < 8; ++p) v[p] = cmulc(e1[p * 256 + tid], twa[p - 1]);
  fft8<true>(v);
  if (valid) {
#pragma unroll
    for (int j1 = 0; j1 < 8; ++j1) st_wt(colbase + (size_t)j1 * 32 * TP + coff, v[j1]);
  }
  STAMP(1, 8);
  STAMP_END(1, 9);
}

template <int N> DEV void wait_vm() { asm volatile("s_waitcnt vmcnt(%0)" ::"n"(N) : "memory"); }
#define RAW_BAR() do { asm volatile("s_waitcnt lgkmcnt(0)" ::: "memory"); __builtin_amdgcn_s_barrier(); } while (0)

DEV void glds16(const void* src, char* lds) {
  __builtin_amdgcn_global_load_lds((const unsigned*)src, (unsigned*)lds, 16, 0, 0);
}
DEV uint32_t pk_relu_bf16(uint32_t p) {
  uint32_t r;
  asm("v_pk_max_i16 %0, %1, 0" : "=v"(r) : "v"(p));
  return r;
}

typedef short v4s_t __attribute__((ext_vector_type(4)));
typedef __attribute__((address_space(3))) v4s_t* lds_v4s_ptr;
DEV bf16x8 tr_frag(const char* p0, const char* p1) {
  const v4s_t a = __builtin_amdgcn_ds_read_tr16_b64_v4i16((lds_v4s_ptr)p0);
  const v4s_t b = __builtin_amdgcn_ds_read_tr16_b64_v4i16((lds_v4s_ptr)p1);
  return __builtin_shufflevector(a, b, 0, 1, 2, 3, 4, 5, 6, 7);
}

#define SGB(mask, n) __builtin_amdgcn_sched_group_barrier(mask, n, 0)
template <int NM, int ND> DEV void interleave_mfma_ds() {
  if constexpr (NM == 6) {
    SGB(0x008, 1); SGB(0x100, 2); SGB(0x008, 1); SGB(0x100, 2); SGB(0x008, 1); SGB(0x100, 2);
    SGB(0x008, 1); SGB(0x100, 2); SGB(0x008, 2);
  } else if constexpr (NM == 10) {
    SGB(0x008, 1); SGB(0x100, 2); SGB(0x008, 1); SGB(0x100, 2); SGB(0x008, 1); SGB(0x100, 2);
    SGB(0x008, 1); SGB(0x100, 2); SGB(0x008, 1); SGB(0x100, 2); SGB(0x008, 5);
  } else {
    SGB(0x008, 1); SGB(0x100, 2); SGB(0x008, 1); SGB(0x100, 2); SGB(0x008, 1); SGB(0x100, 2);
    SGB(0x008, 1); SGB(0x100, 2); SGB(0x008, 1); SGB(0x100, 2);
  }
}
DEV void wait_vm_rt(int n) {
  switch (n) {
    case 0: wait_vm<0>(); break;   case 1: wait_vm<1>(); break;   case 2: wait_vm<2>(); break;
    case 3: wait_vm<3>(); break;   case 4: wait_vm<4>(); break;   case 5: wait_vm<5>(); break;
    case 6: wait_vm<6>(); break;   case 7: wait_vm<7>(); break;   case 8: wait_vm<8>(); break;
    case 9: wait_vm<9>(); break;   case 10: wait_vm<10>(); break; case 11: wait_vm<11>(); break;
    case 12: wait_vm<12>(); break; case 13: wait_vm<13>(); break; case 14: wait_vm<14>(); break;
    default: wait_vm<0>(); break;
  }
}
DEV void glds16_dev(const void* src, char* lds) {
  __builtin_amdgcn_global_load_lds((const unsigned*)src, (unsigned*)lds, 16, 0, 16);
}
DEV void lds_spin(uint32_t* w, uint32_t want) {
  for (int i = 0; i < (1 << 24); ++i) {
    const uint32_t v = __hip_atomic_load(w, __ATOMIC_RELAXED, __HIP_MEMORY_SCOPE_WORKGROUP);
    if ((uint32_t)__builtin_amdgcn_readfirstlane(v) >= want) break;
    __builtin_amdgcn_s_sleep(1);
  }
  asm volatile("" ::: "memory");
}
DEV void wait_cluster(uint32_t* lcnt, uint32_t* lflag, uint32_t* cnt, int lw, int lane) {
  if (lw == 0) {
    lds_spin(lcnt, 8u);
    for (int i = 0; i < (1 << 22); ++i) {
      const uint32_t v = __hip_atomic_load(cnt, __ATOMIC_RELAXED, __HIP_MEMORY_SCOPE_AGENT);
      if ((uint32_t)__builtin_amdgcn_readfirstlane(v) >= 16u) break;
      __builtin_amdgcn_s_sleep(2);
    }
    asm volatile("" ::: "memory");
    if (lane == 0) __hip_atomic_store(lflag, 1u, __ATOMIC_RELAXED, __HIP_MEMORY_SCOPE_WORKGROUP);
  } else {
    lds_spin(lflag, 1u);
  }
}
DEV void publish(uint32_t* lcnt, uint32_t* gcnt, int lane) {
  asm volatile("s_waitcnt vmcnt(0)" ::: "memory");
  if (lane == 0) {
    const uint32_t old = __hip_atomic_fetch_add(lcnt, 1u, __ATOMIC_RELAXED, __HIP_MEMORY_SCOPE_WORKGROUP);
    if (old == 7u) {
      __hip_atomic_fetch_add(gcnt, 1u, __ATOMIC_RELAXED, __HIP_MEMORY_SCOPE_AGENT);
    }
  }
}

#undef STAMP_TID
#define STAMP_TID 0
__global__ __launch_bounds__(1024) void k_tail(const f32x2* __restrict__ T, const uint32_t* __restrict__ x,
                                               uint32_t* h, uint32_t* hn, u32x2* h1,
                                               const char* __restrict__ w1, const uint16_t* __restrict__ b1,
                                               const u32x4* __restrict__ w2p, const uint16_t* __restrict__ b2,
                                               uint16_t* __restrict__ out, uint32_t* cnt) {
  constexpr int BD_OFF = 0, AD_OFF = 6 * 12288;
  constexpr int BE_OFF = 0, NBE = 7, RES_OFF = NBE * 20480;
  constexpr int MISC_OFF = 163840 - 1024, B1_OFF = MISC_OFF, LC_OFF = MISC_OFF + 256, B2_OFF = MISC_OFF + 512;
  __shared__ __attribute__((aligned(16))) char lds[163840];
  const int tid = threadIdx.x, lane = tid & 63;
  const int wv = __builtin_amdgcn_readfirstlane(tid >> 6);
  const int xcd = blockIdx.x & 7, idx = blockIdx.x >> 3;
  const int cl = 2 * xcd + (idx >> 4), mi_ = idx & 15;
  const int cb = mi_, rb2 = 2 * cl + (mi_ >> 3), cb2 = mi_ & 7;
  uint32_t* cnt1 = cnt + cl * 64;
  uint32_t* cnt2 = cnt + (16 + cl) * 64;
  uint32_t* lcnt = (uint32_t*)(lds + LC_OFF);

  if (wv >= 8) {
    const int lw = wv - 8;
    const int nb = (lw < 4) ? 2 : 1, nb2 = (lw < 4) ? 3 : 2;
    const int arow = lane >> 3;
    const char* asrc = (const char*)hn + (size_t)(cl * 128 + 16 * lw + arow) * 1280 + (((lane & 7) ^ arow) << 4);
    const int id0 = lw * 64 + lane, r0 = id0 / 12, c0 = (id0 % 12) ^ (((r0 >> 3) & 1) << 1);
    const int id1 = (8 + lw) * 64 + lane, r1 = id1 / 12, c1 = (id1 % 12) ^ (((r1 >> 3) & 1) << 1);
    const char* bsrc0 = w1 + (size_t)r0 * 3072 + cb * 192 + c0 * 16;
    const char* bsrc1 = w1 + (size_t)r1 * 3072 + cb * 192 + c1 * 16;
    auto stageB = [&](int st) {
      char* dst = lds + BD_OFF + (st % 6) * 12288 + lane * 16;
      glds16(bsrc0 + (size_t)st * 64 * 3072, dst + lw * 1024);
      if (lw < 4) glds16(bsrc1 + (size_t)st * 64 * 3072, dst + (8 + lw) * 1024);
    };
    auto stageA = [&](int st) {
      char* dst = lds + AD_OFF + (st % 5) * 16384 + lane * 16;
      glds16_dev(asrc + st * 128, dst + (2 * lw) * 1024);
      glds16_dev(asrc + st * 128 + 8 * 1280, dst + (2 * lw + 1) * 1024);
    };
    if (lw == 7 && lane < 12) glds16((const u32x4*)(b1 + cb * 96) + lane, lds + B1_OFF + lane * 16);
    if (lw == 6 && lane < 10) glds16((const u32x4*)(b2 + cb2 * 80) + lane, lds + B2_OFF + lane * 16);
#pragma unroll
    for (int st = 0; st < 6; ++st) stageB(st);
    __builtin_amdgcn_s_barrier();
    wait_cluster(lcnt, lcnt + 2, cnt1, lw, lane);
#pragma unroll
    for (int st = 0; st < 5; ++st) stageA(st);
#pragma unroll
    for (int st = 0; st < 10; ++st) {
      const int ka = (st == 0) ? 8 : (st <= 6) ? 6 : (st == 7) ? 4 : (st == 8) ? 2 : 0;
      const int kb = (st <= 1) ? 0 : (st <= 5) ? st - 1 : (st == 6) ? 3 : (st == 7) ? 2 : (st == 8) ? 1 : 0;
      if (lw < 4) wait_vm_rt(ka + 2 * kb); else wait_vm_rt(ka + kb);
      __builtin_amdgcn_s_barrier();
      if (st >= 1 && st + 4 < 10) stageA(st + 4);
      if (st >= 1 && st + 5 < 10) stageB(st + 5);
    }
    __builtin_amdgcn_s_barrier();
    const u32x4* bsrc2 = w2p + (size_t)cb2 * 1536 * 10 + lw * 64 + lane;
    auto stageB2 = [&](int st) {
      char* dst = lds + BE_OFF + (st % NBE) * 20480 + lane * 16;
      glds16(bsrc2 + st * 1280, dst + lw * 1024);
      glds16(bsrc2 + st * 1280 + 512, dst + (8 + lw) * 1024);
      if (lw < 4) glds16(bsrc2 + st * 1280 + 1024, dst + (16 + lw) * 1024);
    };
#pragma unroll
    for (int st = 0; st < NEARLY; ++st) stageB2(st);
    if (NEARLY < NBE) {
      lds_spin(lcnt + 1, 8u);
#pragma unroll
      for (int st = NEARLY; st < NBE; ++st) stageB2(st);
    }
    {
      const int id = lw * 64 + lane, r = id / 10, ch = id % 10;
      glds16_dev((const u32x4*)((const uint16_t*)h + (size_t)(rb2 * 64 + r) * D_ + cb2 * 80) + ch, lds + RES_OFF + id * 16);
      if (lw >= 6) {
        const int id2 = (lw + 2) * 64 + lane, r2 = id2 / 10, ch2 = id2 % 10;
        glds16_dev((const u32x4*)((const uint16_t*)h + (size_t)(rb2 * 64 + r2) * D_ + cb2 * 80) + ch2, lds + RES_OFF + id2 * 16);
      }
    }
    if (lw == 0) wait_cluster(lcnt + 1, lcnt + 3, cnt2, lw, lane);
    wait_vm<0>();
#pragma unroll
    for (int st = 0; st < 12; ++st) {
      const int later = (st < NBE) ? 99 : ((st + 5 < 11 ? st + 5 : 11) - st);
      if (later != 99) { if (lw < 4) wait_vm_rt(3 * later); else wait_vm_rt(2 * later); }
      __builtin_amdgcn_s_barrier();
      if (st >= 1 && st + 6 < 12) stageB2(st + 6);
    }
    __builtin_amdgcn_s_barrier();
    (void)nb; (void)nb2;
    return;
  }

  STAMP(2, 0);
  {
    f32x2(*ex)[360] = (f32x2(*)[360])(lds + AD_OFF);
    f32x2(*xx)[384] = (f32x2(*)[384])(lds + AD_OFF + 23040);
    const int j = wv;
    const int s0 = 32 * xcd + idx;
    const int row = s0 + 256 * j;
    const int prow = s0 * 8 + j;
    f32x2 v[8];
    {
      const f32x2* src = T + (size_t)s0 * TP;
      const uint32_t kk = min(tid, DF_ - 1);
#pragma unroll
      for (int q = 0; q < 8; ++q) v[q] = (src + (size_t)q * 256 * TP)[kk];
    }
    uint32_t xd[5];
#pragma unroll
    for (int i = 0; i < 5; ++i) xd[i] = x[row * 320 + lane + 64 * i];
    if (tid < 4) lcnt[tid] = 0;

    f32x2 tw1[4], tw2[7], twq[7], twk[8];
#pragma unroll
    for (int r = 1; r < 5; ++r) tw1[r - 1] = twid((float)(lane * r) * (1.0f / 320.0f));
    const int l0 = lane & 7, rr = lane >> 3;
#pragma unroll
    for (int q = 1; q < 8; ++q) tw2[q - 1] = twid((float)(l0 * q) * (1.0f / 64.0f));
#pragma unroll
    for (int q = 1; q < 8; ++q) twq[q - 1] = twid((float)(s0 * q) * (1.0f / 2048.0f));
#pragma unroll
    for (int p = 0; p < 8; ++p) twk[p] = twid((float)(rr + 5 * l0 + 40 * p) * (1.0f / 640.0f));
    pin(tw1); pin(tw2); pin(twq); pin(twk);
    __builtin_amdgcn_sched_barrier(0);
    STAMP(2, 1);

    if (j < 6) {
#pragma unroll
      for (int q = 1; q < 8; ++q) v[q] = cmulc(v[q], twq[q - 1]);
      fft8<true>(v);
      f32x2 msk;
      msk.x = 1.0f; msk.y = (tid == 0 || tid == 320) ? 0.0f : 1.0f;
#pragma unroll
      for (int jj = 0; jj < 8; ++jj) xx[jj][tid] = v[jj] * msk;
    }
    RAW_BAR();
    asm volatile("" ::: "memory");
    f32x2* E = ex[j];
    if (lane < 40) {
      f32x2* Er = E + rr * 72;
      const f32x2* Xa = xx[j] + rr + 5 * l0;
      const f32x2* Xb = xx[j] + 320 - rr - 5 * l0;
#pragma unroll
      for (int p = 0; p < 8; ++p) {
        const f32x2 a = Xa[40 * p], b = Xb[-40 * p];
        f32x2 e, d;
        e.x = a.x + b.x; e.y = a.y - b.y;
        d.x = a.x - b.x; d.y = a.y + b.y;
        v[p] = cadd_pi(e, cmulc(d, twk[p]));
      }
      fft8<true>(v);
      Er[l0 * 9] = v[0];
#pragma unroll
      for (int a = 1; a < 8; ++a) Er[l0 * 9 + a] = cmulc(v[a], tw2[a - 1]);
      wave_sync();
#pragma unroll
      for (int q = 0; q < 8; ++q) v[q] = Er[q * 9 + l0];
      fft8<true>(v);
      wave_sync();
#pragma unroll
      for (int l1 = 0; l1 < 8; ++l1) Er[l0 + 8 * l1] = v[l1];
    }
    wave_sync();
    f32x2 u[5];
    u[0] = E[lane];
#pragma unroll
    for (int r = 1; r < 5; ++r) u[r] = cmulc(E[r * 72 + lane], tw1[r - 1]);
    fft5<true>(u);

    const f32x2 sc = {1.0f / 2621440.0f, 1.0f / 2621440.0f};
    f32x2 hv[5];
    uint32_t zp[5];
#pragma unroll
    for (int i = 0; i < 5; ++i) {
      const uint32_t hp = bf_pack(bf_unpack(xd[i]) + bf_round(u[i] * sc));
      st_wt32(h + prow * 320 + lane + 64 * i, hp);
      hv[i] = bf_unpack(hp);
    }
    row_layernorm(hv, zp);
#pragma unroll
    for (int i = 0; i < 5; ++i) st_wt32(hn + prow * 320 + lane + 64 * i, zp[i]);
    STAMP(2, 2);
    publish(lcnt, cnt1, lane);
    STAMP(2, 3);
  }
  const int g = lane >> 4, q4 = (lane & 15) >> 2, p4 = lane & 3;
  {
    const int wm = wv >> 1, wn = wv & 1;
    f32x4 acc[2][3];
#pragma unroll
    for (int mi = 0; mi < 2; ++mi)
#pragma unroll
      for (int ni = 0; ni < 3; ++ni) acc[mi][ni] = (f32x4){0.f, 0.f, 0.f, 0.f};
    const int ar = 32 * wm + (lane & 15);
    const int aoff0 = AD_OFF + ar * 128 + (((lane >> 4) ^ (ar & 7)) << 4);
    const int aoff1 = AD_OFF + ar * 128 + (((4 + (lane >> 4)) ^ (ar & 7)) << 4);
    int boff[3];
#pragma unroll
    for (int ni = 0; ni < 3; ++ni) boff[ni] = BD_OFF + (8 * g + q4) * 192 + p4 * 8 + (((3 * wn + ni) ^ (g & 1)) << 5);
    bf16x8 af0[2], bf0[3], af1[2], bf1[3];
    auto rdD = [&](bf16x8 (&af)[2], bf16x8 (&bf)[3], int st, int ks) {
      int sa = (st % 5) * 16384, sb = (st % 6) * 12288;
      asm volatile("" : "+s"(sa), "+s"(sb));
#pragma unroll
      for (int mi = 0; mi < 2; ++mi) af[mi] = *(const bf16x8*)(lds + sa + (ks ? aoff1 : aoff0) + mi * 2048);
#pragma unroll
      for (int ni = 0; ni < 3; ++ni)
        bf[ni] = tr_frag(lds + sb + boff[ni] + ks * 6144, lds + sb + boff[ni] + ks * 6144 + 768);
    };
    auto mmD = [&](const bf16x8 (&af)[2], const bf16x8 (&bf)[3]) {
#pragma unroll
      for (int mi = 0; mi < 2; ++mi)
#pragma unroll
        for (int ni = 0; ni < 3; ++ni)
          acc[mi][ni] = __builtin_amdgcn_mfma_f32_16x16x32_bf16(bf[ni], af[mi], acc[mi][ni], 0, 0, 0);
    };
    RAW_BAR();
    STAMP(2, 4);
    rdD(af0, bf0, 0, 0);
#pragma unroll
    for (int st = 0; st < 10; ++st) {
      rdD(af1, bf1, st, 1);
      mmD(af0, bf0);
      interleave_mfma_ds<6, 8>();
      __builtin_amdgcn_sched_barrier(0);
      RAW_BAR();
      __builtin_amdgcn_sched_barrier(0);
      if (st < 9) rdD(af0, bf0, st + 1, 0);
      mmD(af1, bf1);
      if (st < 9) interleave_mfma_ds<6, 8>();
      __builtin_amdgcn_sched_barrier(0);
    }
    STAMP(2, 5);
#pragma unroll
    for (int ni = 0; ni < 3; ++ni) {
      const u32x2 bb = *(const u32x2*)(lds + B1_OFF + ((3 * wn + ni) * 16 + 4 * (lane >> 4)) * 2);
      const f32x2 b01 = bf_unpack(bb.x), b23 = bf_unpack(bb.y);
      const int NT = cb * 6 + 3 * wn + ni;
      const int ks2 = NT >> 1, gg = 2 * (NT & 1) + (lane >> 5), half = (lane >> 4) & 1;
#pragma unroll
      for (int mi = 0; mi < 2; ++mi) {
        f32x2 a01 = {acc[mi][ni][0], acc[mi][ni][1]}, a23 = {acc[mi][ni][2], acc[mi][ni][3]};
        a01 = bf_unpack(pk_relu_bf16(bf_pack(bf_round(a01) + b01)));
        a23 = bf_unpack(pk_relu_bf16(bf_pack(bf_round(a23) + b23)));
        u32x2 ov;
        ov.x = bf_pack(a01 * a01); ov.y = bf_pack(a23 * a23);
        const int mt = cl * 8 + 2 * wm + mi;
        st_wt64(h1 + ((((mt >> 2) * 48 + ks2) * 4 + (mt & 3)) * 64 + (lane & 15) + 16 * gg) * 2 + half, ov);
      }
    }
    publish(lcnt + 1, cnt2, lane);
    STAMP(2, 6);
  }
  {
    const int wmp = wv & 1, wk = wv >> 1;
    f32x4 acc[2][5];
#pragma unroll
    for (int mi = 0; mi < 2; ++mi)
#pragma unroll
      for (int ni = 0; ni < 5; ++ni) acc[mi][ni] = (f32x4){0.f, 0.f, 0.f, 0.f};
    const int boff = BE_OFF + (16 * (g >> 1) + 4 * (g & 1) + q4 + 32 * wk) * 160 + p4 * 8;
    const __amdgpu_buffer_rsrc_t rs = __builtin_amdgcn_make_buffer_rsrc((void*)h1, 0, S_ * F_ * 2, 0x00020000);
    const int avoff = ((rb2 * 48 + wk) * 4 + 2 * wmp) * 1024 + lane * 16;
    bf16x8 af[12][2], bfr[2][5];
    auto ldA = [&](int st) {
      af[st][0] = __builtin_bit_cast(bf16x8, __builtin_amdgcn_raw_buffer_load_b128(rs, avoff, st * 16384, 16));
      af[st][1] = __builtin_bit_cast(bf16x8, __builtin_amdgcn_raw_buffer_load_b128(rs, avoff + 1024, st * 16384, 16));
    };
    auto rdE = [&](bf16x8 (&bf)[5], int st) {
      int sb = (st % NBE) * 20480;
      asm volatile("" : "+s"(sb));
#pragma unroll
      for (int ni = 0; ni < 5; ++ni) bf[ni] = tr_frag(lds + sb + boff + ni * 32, lds + sb + boff + ni * 32 + 1280);
    };
    constexpr int APF = 4;
    lds_spin(lcnt + 3, 1u);
#pragma unroll
    for (int i = 0; i < APF; ++i) ldA(i);
    RAW_BAR();
    STAMP(2, 7);
    rdE(bfr[0], 0);
#pragma unroll
    for (int st = 0; st < 12; ++st) {
      if (st < 11) {
        RAW_BAR();
        __builtin_amdgcn_sched_barrier(0);
        rdE(bfr[(st + 1) & 1], st + 1);
      }
#pragma unroll
      for (int ni = 0; ni < 5; ++ni)
#pragma unroll
        for (int mi = 0; mi < 2; ++mi)
          acc[mi][ni] = __builtin_amdgcn_mfma_f32_16x16x32_bf16(bfr[st & 1][ni], af[st][mi], acc[mi][ni], 0, 0, 0);
      if (st < 11) interleave_mfma_ds<10, 10>();
      __builtin_amdgcn_sched_barrier(0);
      if (st + APF < 12) ldA(st + APF);
    }
    STAMP(2, 8);
    f32x4* red = (f32x4*)(lds + BE_OFF) + (wmp * 40) * 64 + lane;
#pragma unroll
    for (int t = 0; t < 10; ++t)
      if ((t & 3) != wk) red[(t * 4 + wk) * 64] = acc[t / 5][t % 5];
    RAW_BAR();
    asm volatile("" ::: "memory");
#pragma unroll
    for (int t = 0; t < 10; ++t) {
      if ((t & 3) == wk) {
        const int mi = t / 5, ni = t % 5;
        f32x4 o = acc[mi][ni];
#pragma unroll
        for (int w2_ = 0; w2_ < 4; ++w2_)
          if (w2_ != (t & 3)) o += red[(t * 4 + w2_) * 64];
        const int lr = 32 * wmp + 16 * mi + (lane & 15);
        const int prow = rb2 * 64 + lr;
        const int mrow = (prow >> 3) + 256 * (prow & 7);
        const int nc = 16 * ni + 4 * (lane >> 4);
        const u32x2 bb = *(const u32x2*)(lds + B2_OFF + nc * 2);
        const u32x2 rsd = *(const u32x2*)(lds + RES_OFF + lr * 160 + nc * 2);
        f32x2 a01 = {o[0], o[1]}, a23 = {o[2], o[3]};
        u32x2 ov;
        ov.x = bf_pack(bf_unpack(rsd.x) + bf_round(bf_round(a01) + bf_unpack(bb.x)));
        ov.y = bf_pack(bf_unpack(rsd.y) + bf_round(bf_round(a23) + bf_unpack(bb.y)));
        *(u32x2*)(out + (size_t)mrow * D_ + cb2 * 80 + nc) = ov;
      }
    }
  }
  STAMP_END(2, 9);
}

__global__ void k_zz_end() {}
extern "C" void kernel_launch(void* const* d_in, const int* in_sizes, int n_in, void* d_out, int out_size,
                              void* d_ws, size_t ws_size, hipStream_t stream) {
  const uint32_t* x = (const uint32_t*)d_in[0];
  const uint16_t* wr = (const uint16_t*)d_in[1];
  const uint16_t* wi = (const uint16_t*)d_in[2];
  const uint16_t* w1 = (const uint16_t*)d_in[4];
  const uint16_t* b1 = (const uint16_t*)d_in[5];
  const uint16_t* w2 = (const uint16_t*)d_in[6];
  const uint16_t* b2 = (const uint16_t*)d_in[7];
  char* ws = (char*)d_ws;
  size_t off = 0;
  f32x2* T = (f32x2*)(ws + off); off += (size_t)8 * 256 * TP * 8;
  uint32_t* h = (uint32_t*)(ws + off); off += (size_t)S_ * D_ * 2;
  uint32_t* hn = (uint32_t*)(ws + off); off += (size_t)S_ * D_ * 2;
  u32x4* h1 = (u32x4*)(ws + off); off += (size_t)S_ * F_ * 2;
  u32x4* w2p = (u32x4*)(ws + off); off += (size_t)D_ * F_ * 2;

  uint32_t* cnt = (uint32_t*)(ws + off); off += 32 * 256;
  k_fwd_rows<<<256, 1024, 0, stream>>>(x, T, wr, wi, (const uint32_t*)w1, (const u32x4*)w2, w2p, cnt);
  k_cols<<<328, 256, 0, stream>>>(T, wr, wi);
  k_tail<<<256, 1024, 0, stream>>>(T, x, h, hn, (u32x2*)h1, (const char*)w1, b1, w2p, b2, (uint16_t*)d_out, cnt);
}
```

```cpp
#include <hip/hip_runtime.h>
#include <hip/hip_bf16.h>
#include <stdint.h>

typedef float f32x2 __attribute__((ext_vector_type(2)));
typedef float f32x4 __attribute__((ext_vector_type(4)));
typedef short bf16x8 __attribute__((ext_vector_type(8)));
typedef unsigned int u32x4 __attribute__((ext_vector_type(4)));
typedef unsigned int u32x2 __attribute__((ext_vector_type(2)));

#define DEV __device__ __forceinline__

#define STAMP(kern, i)
#define STAMP_END(kern, i)
#define STAMP_TID 0

static constexpr int S_ = 2048, D_ = 640, F_ = 1536, DF_ = 321;
static constexpr int TP = 328;
static constexpr int NEARLY = 4;

DEV float bf_lo(uint32_t p) { return __builtin_bit_cast(float, p << 16); }
DEV float bf_hi(uint32_t p) { return __builtin_bit_cast(float, p & 0xffff0000u); }
DEV f32x2 bf_unpack(uint32_t p) { f32x2 r; r.x = bf_lo(p); r.y = bf_hi(p); return r; }
typedef __bf16 bf16x2_t __attribute__((ext_vector_type(2)));
DEV uint32_t bf_pack(f32x2 v) {
  return __builtin_bit_cast(uint32_t, __builtin_convertvector(v, bf16x2_t));
}
DEV f32x2 bf_round(f32x2 v) { return bf_unpack(bf_pack(v)); }
DEV float bfr(float f) { f32x2 v; v.x = f; v.y = f; return bf_lo(bf_pack(v)); }

DEV f32x2 cadd_mi(f32x2 b, f32x2 d) {
  f32x2 r;
  asm("v_pk_add_f32 %0, %1, %2 op_sel:[0,1] op_sel_hi:[1,0] neg_hi:[0,1]" : "=v"(r) : "v"(b), "v"(d));
  return r;
}
DEV f32x2 cadd_pi(f32x2 b, f32x2 d) {
  f32x2 r;
  asm("v_pk_add_f32 %0, %1, %2 op_sel:[0,1] op_sel_hi:[1,0] neg_lo:[0,1]" : "=v"(r) : "v"(b), "v"(d));
  return r;
}
DEV f32x2 cnadd_mi(f32x2 b, f32x2 d) {
  f32x2 r;
  asm("v_pk_add_f32 %0, %1, %2 op_sel:[0,1] op_sel_hi:[1,0] neg_lo:[1,0] neg_hi:[1,1]" : "=v"(r) : "v"(b), "v"(d));
  return r;
}
DEV f32x2 cnadd_pi(f32x2 b, f32x2 d) {
  f32x2 r;
  asm("v_pk_add_f32 %0, %1, %2 op_sel:[0,1] op_sel_hi:[1,0] neg_lo:[1,1] neg_hi:[1,0]" : "=v"(r) : "v"(b), "v"(d));
  return r;
}
DEV f32x2 cfma_mi(f32x2 s, f32x2 d, f32x2 b) {
  f32x2 r;
  asm("v_pk_fma_f32 %0, %1, %2, %3 op_sel:[0,1,0] op_sel_hi:[1,0,1] neg_hi:[0,1,0]" : "=v"(r) : "v"(s), "v"(d), "v"(b));
  return r;
}
DEV f32x2 cfma_pi(f32x2 s, f32x2 d, f32x2 b) {
  f32x2 r;
  asm("v_pk_fma_f32 %0, %1, %2, %3 op_sel:[0,1,0] op_sel_hi:[1,0,1] neg_lo:[0,1,0]" : "=v"(r) : "v"(s), "v"(d), "v"(b));
  return r;
}
DEV f32x2 pfma(f32x2 a, f32x2 b, f32x2 c) { return __builtin_elementwise_fma(a, b, c); }
DEV f32x2 cmul(f32x2 a, f32x2 w) {
  f32x2 t, r;
  asm("v_pk_mul_f32 %0, %1, %2 op_sel:[0,0] op_sel_hi:[0,1]" : "=v"(t) : "v"(a), "v"(w));
  asm("v_pk_fma_f32 %0, %1, %2, %3 op_sel:[1,1,0] op_sel_hi:[1,0,1] neg_lo:[0,1,0]" : "=v"(r) : "v"(a), "v"(w), "v"(t));
  return r;
}
DEV f32x2 cmulc(f32x2 a, f32x2 w) {
  f32x2 t, r;
  asm("v_pk_mul_f32 %0, %1, %2 op_sel:[0,0] op_sel_hi:[0,1] neg_hi:[0,1]" : "=v"(t) : "v"(a), "v"(w));
  asm("v_pk_fma_f32 %0, %1, %2, %3 op_sel:[1,1,0] op_sel_hi:[1,0,1]" : "=v"(r) : "v"(a), "v"(w), "v"(t));
  return r;
}
DEV f32x2 twid(float f) {
  f32x2 r;
  r.x = __builtin_amdgcn_cosf(f);
  r.y = __builtin_amdgcn_sinf(-f);
  return r;
}
DEV void st_wt(f32x2* p, f32x2 v) {
  __hip_atomic_store((unsigned long long*)p, __builtin_bit_cast(unsigned long long, v), __ATOMIC_RELAXED,
                     __HIP_MEMORY_SCOPE_AGENT);
}
DEV void st_wt32(uint32_t* p, uint32_t v) { __hip_atomic_store(p, v, __ATOMIC_RELAXED, __HIP_MEMORY_SCOPE_AGENT); }
DEV void st_wt64(u32x2* p, u32x2 v) {
  __hip_atomic_store((unsigned long long*)p, __builtin_bit_cast(unsigned long long, v), __ATOMIC_RELAXED,
                     __HIP_MEMORY_SCOPE_AGENT);
}
DEV void st_wt128(void* p, u32x4 v) {
  asm volatile("global_store_dwordx4 %0, %1, off sc1\n\ts_nop 1" ::"v"(p), "v"(v) : "memory");
}
template <int N> DEV void pin(f32x2 (&a)[N]) {
#pragma unroll
  for (int i = 0; i < N; ++i) asm volatile("" : "+v"(a[i]));
}
template <bool INV> DEV f32x2 add_rot(f32x2 b, f32x2 d) { return INV ? cadd_pi(b, d) : cadd_mi(b, d); }
template <bool INV> DEV f32x2 sub_rot(f32x2 b, f32x2 d) { return INV ? cadd_mi(b, d) : cadd_pi(b, d); }

template <bool INV> DEV void fft4(f32x2& a0, f32x2& a1, f32x2& a2, f32x2& a3) {
  f32x2 b0 = a0 + a2, b1 = a0 - a2, b2 = a1 + a3, d = a1 - a3;
  a0 = b0 + b2; a2 = b0 - b2;
  a1 = add_rot<INV>(b1, d); a3 = sub_rot<INV>(b1, d);
}
template <bool INV> DEV void fft8(f32x2 (&v)[8]) {
  const f32x2 r = {0.70710678118654752440f, 0.70710678118654752440f};
  const f32x2 nr = {-0.70710678118654752440f, -0.70710678118654752440f};
  f32x2 e0 = v[0] + v[4], e1 = v[1] + v[5], e2 = v[2] + v[6], e3 = v[3] + v[7];
  f32x2 o0 = v[0] - v[4], o1 = v[1] - v[5], o2 = v[2] - v[6], o3 = v[3] - v[7];
  fft4<INV>(e0, e1, e2, e3);
  f32x2 p1 = INV ? cadd_pi(o1, o1) : cadd_mi(o1, o1);
  f32x2 p3 = INV ? cnadd_pi(o3, o3) : cnadd_mi(o3, o3);
  f32x2 s = p1 + p3, d = p1 - p3;
  f32x2 b0 = add_rot<INV>(o0, o2), b1 = sub_rot<INV>(o0, o2);
  v[0] = e0; v[2] = e1; v[4] = e2; v[6] = e3;
  v[1] = pfma(r, s, b0); v[5] = pfma(nr, s, b0);
  v[3] = INV ? cfma_pi(r, d, b1) : cfma_mi(r, d, b1);
  v[7] = INV ? cfma_mi(r, d, b1) : cfma_pi(r, d, b1);
}
template <bool INV> DEV void fft5(f32x2 (&v)[5]) {
  const f32x2 c1 = {0.30901699437494742410f, 0.30901699437494742410f};
  const f32x2 c2 = {-0.80901699437494742410f, -0.80901699437494742410f};
  const f32x2 s1 = {0.95105651629515357212f, 0.95105651629515357212f};
  const f32x2 s2 = {0.58778525229247312917f, 0.58778525229247312917f};
  const f32x2 ns1 = {-0.95105651629515357212f, -0.95105651629515357212f};
  f32x2 t1 = v[1] + v[4], t2 = v[2] + v[3], t3 = v[1] - v[4], t4 = v[2] - v[3];
  f32x2 a1 = pfma(c2, t2, pfma(c1, t1, v[0]));
  f32x2 a2 = pfma(c1, t2, pfma(c2, t1, v[0]));
  f32x2 b1 = pfma(s2, t4, s1 * t3);
  f32x2 b2 = pfma(ns1, t4, s2 * t3);
  v[0] = v[0] + t1 + t2;
  v[1] = add_rot<INV>(a1, b1); v[4] = sub_rot<INV>(a1, b1);
  v[2] = add_rot<INV>(a2, b2); v[3] = sub_rot<INV>(a2, b2);
}

template <int CTRL> DEV float dppmov(float v) {
  return __builtin_bit_cast(float, __builtin_amdgcn_update_dpp(0, __builtin_bit_cast(int, v), CTRL, 0xf, 0xf, false));
}
DEV float wave_sum(float v) {
  v += dppmov<0xB1>(v);
  v += dppmov<0x4E>(v);
  v += dppmov<0x141>(v);
  v += dppmov<0x140>(v);
  int iv = __builtin_bit_cast(int, v);
  float a = __builtin_bit_cast(float, __builtin_amdgcn_readlane(iv, 0));
  float b = __builtin_bit_cast(float, __builtin_amdgcn_readlane(iv, 16));
  float c = __builtin_bit_cast(float, __builtin_amdgcn_readlane(iv, 32));
  float d = __builtin_bit_cast(float, __builtin_amdgcn_readlane(iv, 48));
  return (a + b) + (c + d);
}
DEV void wave_sync() {
  __builtin_amdgcn_fence(__ATOMIC_RELEASE, "wavefront");
  __builtin_amdgcn_wave_barrier();
  __builtin_amdgcn_fence(__ATOMIC_ACQUIRE, "wavefront");
}

DEV void row_layernorm(const f32x2 (&xv)[5], uint32_t (&zp)[5]) {
  f32x2 s2 = (xv[0] + xv[1]) + (xv[2] + xv[3]) + xv[4];
  const float mean = wave_sum(s2.x + s2.y) * (1.0f / 640.0f);
  const f32x2 nm = {-mean, -mean};
  f32x2 q2 = {0.f, 0.f};
#pragma unroll
  for (int i = 0; i < 5; ++i) { f32x2 d = xv[i] + nm; q2 = pfma(d, d, q2); }
  const float var = wave_sum(q2.x + q2.y) * (1.0f / 640.0f);
  f32x2 mv; mv.x = mean; mv.y = var;
  mv = bf_round(mv);
  const float den = bfr(sqrtf(bfr(mv.y + 1e-5f)));
  const float rden = 1.0f / den;
  const f32x2 nmb = {-mv.x, -mv.x}, rd = {rden, rden};
#pragma unroll
  for (int i = 0; i < 5; ++i) zp[i] = bf_pack(bf_round(xv[i] + nmb) * rd);
}

__global__ __launch_bounds__(1024) void k_fwd_rows(const uint32_t* __restrict__ x, f32x2* __restrict__ T,
                                                  const uint16_t* __restrict__ wr, const uint16_t* __restrict__ wi,
                                                  const uint32_t* __restrict__ w1, const u32x4* __restrict__ w2,
                                                  u32x4* __restrict__ w2p, uint32_t* __restrict__ cnt) {
  __shared__ f32x2 ex[8][360];
  __shared__ f32x2 zz[8][320];
  if (threadIdx.x >= 512) {
    const int tid = threadIdx.x - 512, lane = tid & 63, j = tid >> 6;
  uint32_t pf = 0;
  {
    const char* pa = nullptr;
    if (lane < 12) {
      const int t = (blockIdx.x & 7) + 8 * (8 * (blockIdx.x >> 3) + j);
      pa = (const char*)(((lane < 6) ? wr : wi) + (t * DF_ + min(64 * (lane % 6), DF_ - 2)));
      pa = (const char*)((uintptr_t)pa & ~(uintptr_t)3);
    } else if (lane >= 56) {
      pa = (const char*)(w1 + (blockIdx.x * 60 + min(j * 8 + (lane - 56), 59)) * 32);
    }
    else if (lane >= 48) {
      pa = (const char*)((const uint32_t*)w2 + (blockIdx.x * 60 + min(j * 8 + (lane - 48), 59)) * 32);
    }
    else if (lane < 18) {
      unsigned long long c0, c1;
      asm volatile("s_getpc_b64 s[40:41]\n\ts_add_u32 s40, s40, _Z6k_colsPDv2_fPKtS2_PKDv4_jPS3_@rel32@lo+4\n\ts_addc_u32 s41, s41, _Z6k_colsPDv2_fPKtS2_PKDv4_jPS3_@rel32@hi+12\n\ts_mov_b64 %0, s[40:41]" : "=s"(c0) :: "s40", "s41", "scc");
      asm volatile("s_getpc_b64 s[40:41]\n\ts_add_u32 s40, s40, _Z8k_zz_endv@rel32@lo+4\n\ts_addc_u32 s41, s41, _Z8k_zz_endv@rel32@hi+12\n\ts_mov_b64 %0, s[40:41]" : "=s"(c1) :: "s40", "s41", "scc");
      const long long len = (long long)(c1 - c0);
      const int li = ((blockIdx.x >> 3) * 8 + j) * 2 + (lane - 16);
      if (lane >= 16 && len > 0 && len < 65536 && li * 128 < len) pa = (const char*)(c0 + li * 128);
    }
    if (pa) pf = *(const uint32_t*)pa;
  }

  asm volatile("" ::"v"(pf));
    return;
  }
  const int tid = threadIdx.x, lane = tid & 63, j = tid >> 6;
  STAMP(0, 0);
  if (blockIdx.x == 0 && tid < 64) st_wt32(cnt + tid * 32, 0u);
  const int s0 = 32 * (blockIdx.x & 7) + (blockIdx.x >> 3);
  const int row = s0 + 256 * j;
  uint32_t xd[5];
#pragma unroll
  for (int i = 0; i < 5; ++i) xd[i] = x[row * 320 + lane + 64 * i];
  f32x2 tw1[4], tw2[7], twq[7];
#pragma unroll
  for (int r = 1; r < 5; ++r) tw1[r - 1] = twid((float)(lane * r) * (1.0f / 320.0f));
  const int l0 = lane & 7, rr = lane >> 3;
#pragma unroll
  for (int q = 1; q < 8; ++q) tw2[q - 1] = twid((float)(l0 * q) * (1.0f / 64.0f));
#pragma unroll
  for (int q = 1; q < 8; ++q) twq[q - 1] = twid((float)(s0 * q) * (1.0f / 2048.0f));
  f32x2 twk_[1] = {twid((float)tid * (1.0f / 640.0f))};
  pin(tw1); pin(tw2); pin(twq); pin(twk_);
  const f32x2 twk = twk_[0];
  __builtin_amdgcn_sched_barrier(0);
  STAMP(0, 1);

  f32x2 u[5];
  {
    f32x2 xv[5];
    uint32_t zp[5];
#pragma unroll
    for (int i = 0; i < 5; ++i) xv[i] = bf_unpack(xd[i]);
    row_layernorm(xv, zp);
#pragma unroll
    for (int i = 0; i < 5; ++i) u[i] = bf_unpack(zp[i]);
  }
  STAMP(0, 2);
  f32x2* E = ex[j];
  fft5<false>(u);
  E[lane] = u[0];
#pragma unroll
  for (int r = 1; r < 5; ++r) E[r * 72 + lane] = cmul(u[r], tw1[r - 1]);
  wave_sync();
  if (lane < 40) {
    f32x2 v[8];
    f32x2* Er = E + rr * 72;
#pragma unroll
    for (int l1 = 0; l1 < 8; ++l1) v[l1] = Er[l0 + 8 * l1];
    fft8<false>(v);
    wave_sync();
    Er[l0] = v[0];
#pragma unroll
    for (int q = 1; q < 8; ++q) Er[q * 9 + l0] = cmul(v[q], tw2[q - 1]);
    wave_sync();
#pragma unroll
    for (int a = 0; a < 8; ++a) v[a] = Er[l0 * 9 + a];
    fft8<false>(v);
    f32x2* Z = zz[j] + rr + 5 * l0;
#pragma unroll
    for (int p = 0; p < 8; ++p) Z[40 * p] = v[p];
  }
  STAMP(0, 3);
  __syncthreads();
  STAMP(0, 4);
  if (j < 6) {
    const int k = tid;
    const int ka = (k >= 320) ? 0 : k, kb = (k == 0 || k >= 320) ? 0 : 320 - k;
    f32x2 v[8];
#pragma unroll
    for (int jj = 0; jj < 8; ++jj) {
      const f32x2 a = zz[jj][ka], b = zz[jj][kb];
      f32x2 e, d;
      e.x = a.x + b.x; e.y = a.y - b.y;
      d.x = a.x - b.x; d.y = a.y + b.y;
      v[jj] = cadd_mi(e, cmul(d, twk));
    }
    fft8<false>(v);
    if (k < DF_) {
      f32x2* dst = T + (size_t)s0 * TP;
      st_wt(dst + k, v[0]);
#pragma unroll
      for (int q = 1; q < 8; ++q) st_wt(dst + (size_t)q * 256 * TP + k, cmul(v[q], twq[q - 1]));
    }
  }
  STAMP(0, 5);
  STAMP_END(0, 6);
}

__global__ __launch_bounds__(256) void k_cols(f32x2* __restrict__ T, const uint16_t* __restrict__ wr,
                                              const uint16_t* __restrict__ wi, const u32x4* __restrict__ w2,
                                              u32x4* __restrict__ w2p) {
  __shared__ f32x2 e1[2048];
  __shared__ f32x2 e2[4][576];
  __shared__ __attribute__((aligned(16))) uint32_t wl[2048];
  const int tid = threadIdx.x, lane = tid & 63, wv = tid >> 6;
  if (blockIdx.x >= 328) {
    const int w0 = (blockIdx.x - 328) * 512 + tid;
    const u32x4 c0 = w2[w0], c1 = w2[w0 + 256];
#pragma unroll
    for (int i = 0; i < 2; ++i) {
      const int wid = w0 + 256 * i;
      const int wk_ = wid / 80, wc_ = wid % 80;
      const int rho = (wk_ & ~12) | ((wk_ & 4) << 1) | ((wk_ & 8) >> 1);
      st_wt128(w2p + ((wc_ / 10) * 1536 + rho) * 10 + (wc_ % 10), i ? c1 : c0);
    }
    return;
  }
  STAMP(1, 0);
  const int q = blockIdx.x & 7, kg = blockIdx.x >> 3;
  const int c = tid & 7, a = tid >> 3;
  const int k = kg * 8 + c;
  const bool valid = k < DF_;
  f32x2* colbase = T + (size_t)(q * 256) * TP;
  const uint32_t coff = a * TP + (valid ? k : DF_ - 1);
  f32x2 v[8];
#pragma unroll
  for (int j1 = 0; j1 < 8; ++j1) v[j1] = (colbase + (size_t)j1 * 32 * TP)[coff];
  const int kb = min(8 * kg, DF_ - 8);
  u32x4 wre, wim;
  __builtin_memcpy(&wre, wr + (size_t)(q + 8 * tid) * DF_ + kb, 16);
  __builtin_memcpy(&wim, wi + (size_t)(q + 8 * tid) * DF_ + kb, 16);

  const int b = (lane >> 3) & 3, p1lo = lane >> 5, p1 = 2 * wv + p1lo;
  f32x2 twa[7], twb[7];
#pragma unroll
  for (int p = 1; p < 8; ++p) twa[p - 1] = twid((float)(a * p) * (1.0f / 256.0f));
#pragma unroll
  for (int p = 1; p < 8; ++p) twb[p - 1] = twid((float)(b * p) * (1.0f / 32.0f));
  pin(twa); pin(twb);
  __builtin_amdgcn_sched_barrier(0);
  STAMP(1, 1);

  fft8<false>(v);
  e1[tid] = v[0];
#pragma unroll
  for (int p = 1; p < 8; ++p) e1[p * 256 + tid] = cmul(v[p], twa[p - 1]);
  {
    u32x4 o0, o1;
    o0.x = __builtin_amdgcn_perm(wim.x, wre.x, 0x05040100); o0.y = __builtin_amdgcn_perm(wim.x, wre.x, 0x07060302);
    o0.z = __builtin_amdgcn_perm(wim.y, wre.y, 0x05040100); o0.w = __builtin_amdgcn_perm(wim.y, wre.y, 0x07060302);
    o1.x = __builtin_amdgcn_perm(wim.z, wre.z, 0x05040100); o1.y = __builtin_amdgcn_perm(wim.z, wre.z, 0x07060302);
    o1.z = __builtin_amdgcn_perm(wim.w, wre.w, 0x05040100); o1.w = __builtin_amdgcn_perm(wim.w, wre.w, 0x07060302);
    *(u32x4*)(wl + tid * 8) = o0;
    *(u32x4*)(wl + tid * 8 + 4) = o1;
  }
  STAMP(1, 2);
  __syncthreads();
  STAMP(1, 3);
  uint32_t wf[8];
  {
    const uint32_t* wlp = wl + (p1 + 16 * b) * 8 + min(8 * kg + c - kb, 7);
#pragma unroll
    for (int e = 0; e < 2; ++e)
#pragma unroll
      for (int mm = 0; mm < 4; ++mm) wf[e * 4 + mm] = wlp[(8 * e + 64 * mm) * 8];
  }
  f32x2* E1 = e1 + p1 * 256 + (lane & 31);
#pragma unroll
  for (int j2 = 0; j2 < 8; ++j2) v[j2] = E1[32 * j2];
  fft8<false>(v);
  f32x2* E2w = e2[wv] + p1lo * 288 + (lane & 31);
  E2w[0] = v[0];
#pragma unroll
  for (int p = 1; p < 8; ++p) E2w[p * 36] = cmul(v[p], twb[p - 1]);
  wave_sync();
  f32x2* E2r = e2[wv] + p1lo * 288 + (2 * b) * 36 + c;
#pragma unroll
  for (int e = 0; e < 2; ++e)
#pragma unroll
    for (int bb = 0; bb < 4; ++bb) v[e * 4 + bb] = E2r[e * 36 + bb * 8];
  fft4<false>(v[0], v[1], v[2], v[3]);
  fft4<false>(v[4], v[5], v[6], v[7]);
  STAMP(1, 4);
#pragma unroll
  for (int i = 0; i < 8; ++i) v[i] = cmul(v[i], bf_unpack(wf[i]));
  fft4<true>(v[0], v[1], v[2], v[3]);
  fft4<true>(v[4], v[5], v[6], v[7]);
  STAMP(1, 5);
#pragma unroll
  for (int e = 0; e < 2; ++e)
#pragma unroll
    for (int bb = 0; bb < 4; ++bb) E2r[e * 36 + bb * 8] = v[e * 4 + bb];
  wave_sync();
  v[0] = E2w[0];
#pragma unroll
  for (int p = 1; p < 8; ++p) v[p] = cmulc(E2w[p * 36], twb[p - 1]);
  fft8<true>(v);
#pragma unroll
  for (int j2 = 0; j2 < 8; ++j2) E1[32 * j2] = v[j2];
  STAMP(1, 6);
  __syncthreads();
  STAMP(1, 7);
  v[0] = e1[tid];
#pragma unroll
  for (int p = 1; p < 8; ++p) v[p] = cmulc(e1[p * 256 + tid], twa[p - 1]);
  fft8<true>(v);
  if (valid) {
#pragma unroll
    for (int j1 = 0; j1 < 8; ++j1) st_wt(colbase + (size_t)j1 * 32 * TP + coff, v[j1]);
  }
  STAMP(1, 8);
  STAMP_END(1, 9);
}

template <int N> DEV void wait_vm() { asm volatile("s_waitcnt vmcnt(%0)" ::"n"(N) : "memory"); }
#define RAW_BAR() do { asm volatile("s_waitcnt lgkmcnt(0)" ::: "memory"); __builtin_amdgcn_s_barrier(); } while (0)

DEV void glds16(const void* src, char* lds) {
  __builtin_amdgcn_global_load_lds((const unsigned*)src, (unsigned*)lds, 16, 0, 0);
}
DEV uint32_t pk_relu_bf16(uint32_t p) {
  uint32_t r;
  asm("v_pk_max_i16 %0, %1, 0" : "=v"(r) : "v"(p));
  return r;
}

typedef short v4s_t __attribute__((ext_vector_type(4)));
typedef __attribute__((address_space(3))) v4s_t* lds_v4s_ptr;
DEV bf16x8 tr_frag(const char* p0, const char* p1) {
  const v4s_t a = __builtin_amdgcn_ds_read_tr16_b64_v4i16((lds_v4s_ptr)p0);
  const v4s_t b = __builtin_amdgcn_ds_read_tr16_b64_v4i16((lds_v4s_ptr)p1);
  return __builtin_shufflevector(a, b, 0, 1, 2, 3, 4, 5, 6, 7);
}

#define SGB(mask, n) __builtin_amdgcn_sched_group_barrier(mask, n, 0)
template <int NM, int ND> DEV void interleave_mfma_ds() {
  if constexpr (NM == 6) {
    SGB(0x008, 1); SGB(0x100, 2); SGB(0x008, 1); SGB(0x100, 2); SGB(0x008, 1); SGB(0x100, 2);
    SGB(0x008, 1); SGB(0x100, 2); SGB(0x008, 2);
  } else if constexpr (NM == 10) {
    SGB(0x008, 1); SGB(0x100, 2); SGB(0x008, 1); SGB(0x100, 2); SGB(0x008, 1); SGB(0x100, 2);
    SGB(0x008, 1); SGB(0x100, 2); SGB(0x008, 1); SGB(0x100, 2); SGB(0x008, 5);
  } else {
    SGB(0x008, 1); SGB(0x100, 2); SGB(0x008, 1); SGB(0x100, 2); SGB(0x008, 1); SGB(0x100, 2);
    SGB(0x008, 1); SGB(0x100, 2); SGB(0x008, 1); SGB(0x100, 2);
  }
}
DEV void wait_vm_rt(int n) {
  switch (n) {
    case 0: wait_vm<0>(); break;   case 1: wait_vm<1>(); break;   case 2: wait_vm<2>(); break;
    case 3: wait_vm<3>(); break;   case 4: wait_vm<4>(); break;   case 5: wait_vm<5>(); break;
    case 6: wait_vm<6>(); break;   case 7: wait_vm<7>(); break;   case 8: wait_vm<8>(); break;
    case 9: wait_vm<9>(); break;   case 10: wait_vm<10>(); break; case 11: wait_vm<11>(); break;
    case 12: wait_vm<12>(); break; case 13: wait_vm<13>(); break; case 14: wait_vm<14>(); break;
    default: wait_vm<0>(); break;
  }
}
DEV void glds16_dev(const void* src, char* lds) {
  __builtin_amdgcn_global_load_lds((const unsigned*)src, (unsigned*)lds, 16, 0, 16);
}
DEV void lds_spin(uint32_t* w, uint32_t want) {
  for (int i = 0; i < (1 << 24); ++i) {
    const uint32_t v = __hip_atomic_load(w, __ATOMIC_RELAXED, __HIP_MEMORY_SCOPE_WORKGROUP);
    if ((uint32_t)__builtin_amdgcn_readfirstlane(v) >= want) break;
    __builtin_amdgcn_s_sleep(1);
  }
  asm volatile("" ::: "memory");
}
DEV void wait_cluster(uint32_t* lcnt, uint32_t* lflag, uint32_t* cnt, int lw, int lane) {
  if (lw == 0) {
    lds_spin(lcnt, 8u);
    for (int i = 0; i < (1 << 22); ++i) {
      const uint32_t v = __hip_atomic_load(cnt, __ATOMIC_RELAXED, __HIP_MEMORY_SCOPE_AGENT);
      if ((uint32_t)__builtin_amdgcn_readfirstlane(v) >= 16u) break;
      __builtin_amdgcn_s_sleep(2);
    }
    asm volatile("" ::: "memory");
    if (lane == 0) __hip_atomic_store(lflag, 1u, __ATOMIC_RELAXED, __HIP_MEMORY_SCOPE_WORKGROUP);
  } else {
    lds_spin(lflag, 1u);
  }
}
DEV void publish(uint32_t* lcnt, uint32_t* gcnt, int lane) {
  asm volatile("s_waitcnt vmcnt(0)" ::: "memory");
  if (lane == 0) {
    const uint32_t old = __hip_atomic_fetch_add(lcnt, 1u, __ATOMIC_RELAXED, __HIP_MEMORY_SCOPE_WORKGROUP);
    if (old == 7u) {
      __hip_atomic_fetch_add(gcnt, 1u, __ATOMIC_RELAXED, __HIP_MEMORY_SCOPE_AGENT);
    }
  }
}

#undef STAMP_TID
#define STAMP_TID 0
__global__ __launch_bounds__(1024) void k_tail(const f32x2* __restrict__ T, const uint32_t* __restrict__ x,
                                               uint32_t* h, uint32_t* hn, u32x2* h1,
                                               const char* __restrict__ w1, const uint16_t* __restrict__ b1,
                                               const u32x4* __restrict__ w2p, const uint16_t* __restrict__ b2,
                                               uint16_t* __restrict__ out, uint32_t* cnt) {
  constexpr int BD_OFF = 0, AD_OFF = 6 * 12288;
  constexpr int BE_OFF = 0, NBE = 7, RES_OFF = NBE * 20480;
  constexpr int MISC_OFF = 163840 - 1024, B1_OFF = MISC_OFF, LC_OFF = MISC_OFF + 256, B2_OFF = MISC_OFF + 512;
  __shared__ __attribute__((aligned(16))) char lds[163840];
  const int tid = threadIdx.x, lane = tid & 63;
  const int wv = __builtin_amdgcn_readfirstlane(tid >> 6);
  const int xcd = blockIdx.x & 7, idx = blockIdx.x >> 3;
  const int cl = 2 * xcd + (idx >> 4), mi_ = idx & 15;
  const int cb = mi_, rb2 = 2 * cl + (mi_ >> 3), cb2 = mi_ & 7;
  uint32_t* cnt1 = cnt + cl * 64;
  uint32_t* cnt2 = cnt + (16 + cl) * 64;
  uint32_t* lcnt = (uint32_t*)(lds + LC_OFF);

  if (wv >= 8) {
    const int lw = wv - 8;
    const int nb = (lw < 4) ? 2 : 1, nb2 = (lw < 4) ? 3 : 2;
    const int arow = lane >> 3;
    const char* asrc = (const char*)hn + (size_t)(cl * 128 + 16 * lw + arow) * 1280 + (((lane & 7) ^ arow) << 4);
    const int id0 = lw * 64 + lane, r0 = id0 / 12, c0 = (id0 % 12) ^ (((r0 >> 3) & 1) << 1);
    const int id1 = (8 + lw) * 64 + lane, r1 = id1 / 12, c1 = (id1 % 12) ^ (((r1 >> 3) & 1) << 1);
    const char* bsrc0 = w1 + (size_t)r0 * 3072 + cb * 192 + c0 * 16;
    const char* bsrc1 = w1 + (size_t)r1 * 3072 + cb * 192 + c1 * 16;
    auto stageB = [&](int st) {
      char* dst = lds + BD_OFF + (st % 6) * 12288 + lane * 16;
      glds16(bsrc0 + (size_t)st * 64 * 3072, dst + lw * 1024);
      if (lw < 4) glds16(bsrc1 + (size_t)st * 64 * 3072, dst + (8 + lw) * 1024);
    };
    auto stageA = [&](int st) {
      char* dst = lds + AD_OFF + (st % 5) * 16384 + lane * 16;
      glds16_dev(asrc + st * 128, dst + (2 * lw) * 1024);
      glds16_dev(asrc + st * 128 + 8 * 1280, dst + (2 * lw + 1) * 1024);
    };
    if (lw == 7 && lane < 12) glds16((const u32x4*)(b1 + cb * 96) + lane, lds + B1_OFF + lane * 16);
    if (lw == 6 && lane < 10) glds16((const u32x4*)(b2 + cb2 * 80) + lane, lds + B2_OFF + lane * 16);
#pragma unroll
    for (int st = 0; st < 6; ++st) stageB(st);
    __builtin_amdgcn_s_barrier();
    wait_cluster(lcnt, lcnt + 2, cnt1, lw, lane);
#pragma unroll
    for (int st = 0; st < 5; ++st) stageA(st);
#pragma unroll
    for (int st = 0; st < 10; ++st) {
      const int ka = (st == 0) ? 8 : (st <= 6) ? 6 : (st == 7) ? 4 : (st == 8) ? 2 : 0;
      const int kb = (st <= 1) ? 0 : (st <= 5) ? st - 1 : (st == 6) ? 3 : (st == 7) ? 2 : (st == 8) ? 1 : 0;
      if (lw < 4) wait_vm_rt(ka + 2 * kb); else wait_vm_rt(ka + kb);
      __builtin_amdgcn_s_barrier();
      if (st >= 1 && st + 4 < 10) stageA(st + 4);
      if (st >= 1 && st + 5 < 10) stageB(st + 5);
    }
    __builtin_amdgcn_s_barrier();
    const u32x4* bsrc2 = w2p + (size_t)cb2 * 1536 * 10 + lw * 64 + lane;
    auto stageB2 = [&](int st) {
      char* dst = lds + BE_OFF + (st % NBE) * 20480 + lane * 16;
      glds16(bsrc2 + st * 1280, dst + lw * 1024);
      glds16(bsrc2 + st * 1280 + 512, dst + (8 + lw) * 1024);
      if (lw < 4) glds16(bsrc2 + st * 1280 + 1024, dst + (16 + lw) * 1024);
    };
#pragma unroll
    for (int st = 0; st < NEARLY; ++st) stageB2(st);
    if (NEARLY < NBE) {
      lds_spin(lcnt + 1, 8u);
#pragma unroll
      for (int st = NEARLY; st < NBE; ++st) stageB2(st);
    }
    {
      const int id = lw * 64 + lane, r = id / 10, ch = id % 10;
      glds16_dev((const u32x4*)((const uint16_t*)h + (size_t)(rb2 * 64 + r) * D_ + cb2 * 80) + ch, lds + RES_OFF + id * 16);
      if (lw >= 6) {
        const int id2 = (lw + 2) * 64 + lane, r2 = id2 / 10, ch2 = id2 % 10;
        glds16_dev((const u32x4*)((const uint16_t*)h + (size_t)(rb2 * 64 + r2) * D_ + cb2 * 80) + ch2, lds + RES_OFF + id2 * 16);
      }
    }
    if (lw == 0) wait_cluster(lcnt + 1, lcnt + 3, cnt2, lw, lane);
    wait_vm<0>();
#pragma unroll
    for (int st = 0; st < 12; ++st) {
      const int later = (st < NBE) ? 99 : ((st + 5 < 11 ? st + 5 : 11) - st);
      if (later != 99) { if (lw < 4) wait_vm_rt(3 * later); else wait_vm_rt(2 * later); }
      __builtin_amdgcn_s_barrier();
      if (st >= 1 && st + 6 < 12) stageB2(st + 6);
    }
    __builtin_amdgcn_s_barrier();
    (void)nb; (void)nb2;
    return;
  }

  STAMP(2, 0);
  {
    f32x2(*ex)[360] = (f32x2(*)[360])(lds + AD_OFF);
    f32x2(*xx)[384] = (f32x2(*)[384])(lds + AD_OFF + 23040);
    const int j = wv;
    const int s0 = 32 * xcd + idx;
    const int row = s0 + 256 * j;
    const int prow = s0 * 8 + j;
    f32x2 v[8];
    {
      const f32x2* src = T + (size_t)s0 * TP;
      const uint32_t kk = min(tid, DF_ - 1);
#pragma unroll
      for (int q = 0; q < 8; ++q) v[q] = (src + (size_t)q * 256 * TP)[kk];
    }
    uint32_t xd[5];
#pragma unroll
    for (int i = 0; i < 5; ++i) xd[i] = x[row * 320 + lane + 64 * i];
    if (tid < 4) lcnt[tid] = 0;

    f32x2 tw1[4], tw2[7], twq[7], twk[8];
#pragma unroll
    for (int r = 1; r < 5; ++r) tw1[r - 1] = twid((float)(lane * r) * (1.0f / 320.0f));
    const int l0 = lane & 7, rr = lane >> 3;
#pragma unroll
    for (int q = 1; q < 8; ++q) tw2[q - 1] = twid((float)(l0 * q) * (1.0f / 64.0f));
#pragma unroll
    for (int q = 1; q < 8; ++q) twq[q - 1] = twid((float)(s0 * q) * (1.0f / 2048.0f));
#pragma unroll
    for (int p = 0; p < 8; ++p) twk[p] = twid((float)(rr + 5 * l0 + 40 * p) * (1.0f / 640.0f));
    pin(tw1); pin(tw2); pin(twq); pin(twk);
    __builtin_amdgcn_sched_barrier(0);
    STAMP(2, 1);

    if (j < 6) {
#pragma unroll
      for (int q = 1; q < 8; ++q) v[q] = cmulc(v[q], twq[q - 1]);
      fft8<true>(v);
      f32x2 msk;
      msk.x = 1.0f; msk.y = (tid == 0 || tid == 320) ? 0.0f : 1.0f;
#pragma unroll
      for (int jj = 0; jj < 8; ++jj) xx[jj][tid] = v[jj] * msk;
    }
    RAW_BAR();
    asm volatile("" ::: "memory");
    f32x2* E = ex[j];
    if (lane < 40) {
      f32x2* Er = E + rr * 72;
      const f32x2* Xa = xx[j] + rr + 5 * l0;
      const f32x2* Xb = xx[j] + 320 - rr - 5 * l0;
#pragma unroll
      for (int p = 0; p < 8; ++p) {
        const f32x2 a = Xa[40 * p], b = Xb[-40 * p];
        f32x2 e, d;
        e.x = a.x + b.x; e.y = a.y - b.y;
        d.x = a.x - b.x; d.y = a.y + b.y;
        v[p] = cadd_pi(e, cmulc(d, twk[p]));
      }
      fft8<true>(v);
      Er[l0 * 9] = v[0];
#pragma unroll
      for (int a = 1; a < 8; ++a) Er[l0 * 9 + a] = cmulc(v[a], tw2[a - 1]);
      wave_sync();
#pragma unroll
      for (int q = 0; q < 8; ++q) v[q] = Er[q * 9 + l0];
      fft8<true>(v);
      wave_sync();
#pragma unroll
      for (int l1 = 0; l1 < 8; ++l1) Er[l0 + 8 * l1] = v[l1];
    }
    wave_sync();
    f32x2 u[5];
    u[0] = E[lane];
#pragma unroll
    for (int r = 1; r < 5; ++r) u[r] = cmulc(E[r * 72 + lane], tw1[r - 1]);
    fft5<true>(u);

    const f32x2 sc = {1.0f / 2621440.0f, 1.0f / 2621440.0f};
    f32x2 hv[5];
    uint32_t zp[5];
#pragma unroll
    for (int i = 0; i < 5; ++i) {
      const uint32_t hp = bf_pack(bf_unpack(xd[i]) + bf_round(u[i] * sc));
      st_wt32(h + prow * 320 + lane + 64 * i, hp);
      hv[i] = bf_unpack(hp);
    }
    row_layernorm(hv, zp);
#pragma unroll
    for (int i = 0; i < 5; ++i) st_wt32(hn + prow * 320 + lane + 64 * i, zp[i]);
    STAMP(2, 2);
    publish(lcnt, cnt1, lane);
    STAMP(2, 3);
  }
  const int g = lane >> 4, q4 = (lane & 15) >> 2, p4 = lane & 3;
  {
    const int wm = wv >> 1, wn = wv & 1;
    f32x4 acc[2][3];
#pragma unroll
    for (int mi = 0; mi < 2; ++mi)
#pragma unroll
      for (int ni = 0; ni < 3; ++ni) acc[mi][ni] = (f32x4){0.f, 0.f, 0.f, 0.f};
    const int ar = 32 * wm + (lane & 15);
    const int aoff0 = AD_OFF + ar * 128 + (((lane >> 4) ^ (ar & 7)) << 4);
    const int aoff1 = AD_OFF + ar * 128 + (((4 + (lane >> 4)) ^ (ar & 7)) << 4);
    int boff[3];
#pragma unroll
    for (int ni = 0; ni < 3; ++ni) boff[ni] = BD_OFF + (8 * g + q4) * 192 + p4 * 8 + (((3 * wn + ni) ^ (g & 1)) << 5);
    bf16x8 af0[2], bf0[3], af1[2], bf1[3];
    auto rdD = [&](bf16x8 (&af)[2], bf16x8 (&bf)[3], int st, int ks) {
      int sa = (st % 5) * 16384, sb = (st % 6) * 12288;
      asm volatile("" : "+s"(sa), "+s"(sb));
#pragma unroll
      for (int mi = 0; mi < 2; ++mi) af[mi] = *(const bf16x8*)(lds + sa + (ks ? aoff1 : aoff0) + mi * 2048);
#pragma unroll
      for (int ni = 0; ni < 3; ++ni)
        bf[ni] = tr_frag(lds + sb + boff[ni] + ks * 6144, lds + sb + boff[ni] + ks * 6144 + 768);
    };
    auto mmD = [&](const bf16x8 (&af)[2], const bf16x8 (&bf)[3]) {
#pragma unroll
      for (int mi = 0; mi < 2; ++mi)
#pragma unroll
        for (int ni = 0; ni < 3; ++ni)
          acc[mi][ni] = __builtin_amdgcn_mfma_f32_16x16x32_bf16(bf[ni], af[mi], acc[mi][ni], 0, 0, 0);
    };
    RAW_BAR();
    STAMP(2, 4);
    rdD(af0, bf0, 0, 0);
#pragma unroll
    for (int st = 0; st < 10; ++st) {
      rdD(af1, bf1, st, 1);
      mmD(af0, bf0);
      interleave_mfma_ds<6, 8>();
      __builtin_amdgcn_sched_barrier(0);
      RAW_BAR();
      __builtin_amdgcn_sched_barrier(0);
      if (st < 9) rdD(af0, bf0, st + 1, 0);
      mmD(af1, bf1);
      if (st < 9) interleave_mfma_ds<6, 8>();
      __builtin_amdgcn_sched_barrier(0);
    }
    STAMP(2, 5);
#pragma unroll
    for (int ni = 0; ni < 3; ++ni) {
      const u32x2 bb = *(const u32x2*)(lds + B1_OFF + ((3 * wn + ni) * 16 + 4 * (lane >> 4)) * 2);
      const f32x2 b01 = bf_unpack(bb.x), b23 = bf_unpack(bb.y);
      const int NT = cb * 6 + 3 * wn + ni;
      const int ks2 = NT >> 1, gg = 2 * (NT & 1) + (lane >> 5), half = (lane >> 4) & 1;
#pragma unroll
      for (int mi = 0; mi < 2; ++mi) {
        f32x2 a01 = {acc[mi][ni][0], acc[mi][ni][1]}, a23 = {acc[mi][ni][2], acc[mi][ni][3]};
        a01 = bf_unpack(pk_relu_bf16(bf_pack(bf_round(a01) + b01)));
        a23 = bf_unpack(pk_relu_bf16(bf_pack(bf_round(a23) + b23)));
        u32x2 ov;
        ov.x = bf_pack(a01 * a01); ov.y = bf_pack(a23 * a23);
        const int mt = cl * 8 + 2 * wm + mi;
        st_wt64(h1 + ((((mt >> 2) * 48 + ks2) * 4 + (mt & 3)) * 64 + (lane & 15) + 16 * gg) * 2 + half, ov);
      }
    }
    publish(lcnt + 1, cnt2, lane);
    STAMP(2, 6);
  }
  {
    const int wmp = wv & 1, wk = wv >> 1;
    f32x4 acc[2][5];
#pragma unroll
    for (int mi = 0; mi < 2; ++mi)
#pragma unroll
      for (int ni = 0; ni < 5; ++ni) acc[mi][ni] = (f32x4){0.f, 0.f, 0.f, 0.f};
    const int boff = BE_OFF + (16 * (g >> 1) + 4 * (g & 1) + q4 + 32 * wk) * 160 + p4 * 8;
    const __amdgpu_buffer_rsrc_t rs = __builtin_amdgcn_make_buffer_rsrc((void*)h1, 0, S_ * F_ * 2, 0x00020000);
    const int avoff = ((rb2 * 48 + wk) * 4 + 2 * wmp) * 1024 + lane * 16;
    bf16x8 af[12][2], bfr[2][5];
    auto ldA = [&](int st) {
      af[st][0] = __builtin_bit_cast(bf16x8, __builtin_amdgcn_raw_buffer_load_b128(rs, avoff, st * 16384, 16));
      af[st][1] = __builtin_bit_cast(bf16x8, __builtin_amdgcn_raw_buffer_load_b128(rs, avoff + 1024, st * 16384, 16));
    };
    auto rdE = [&](bf16x8 (&bf)[5], int st) {
      int sb = (st % NBE) * 20480;
      asm volatile("" : "+s"(sb));
#pragma unroll
      for (int ni = 0; ni < 5; ++ni) bf[ni] = tr_frag(lds + sb + boff + ni * 32, lds + sb + boff + ni * 32 + 1280);
    };
    constexpr int APF = 4;
    lds_spin(lcnt + 3, 1u);
#pragma unroll
    for (int i = 0; i < APF; ++i) ldA(i);
    RAW_BAR();
    STAMP(2, 7);
    rdE(bfr[0], 0);
#pragma unroll
    for (int st = 0; st < 12; ++st) {
      if (st < 11) {
        RAW_BAR();
        __builtin_amdgcn_sched_barrier(0);
        rdE(bfr[(st + 1) & 1], st + 1);
      }
#pragma unroll
      for (int ni = 0; ni < 5; ++ni)
#pragma unroll
        for (int mi = 0; mi < 2; ++mi)
          acc[mi][ni] = __builtin_amdgcn_mfma_f32_16x16x32_bf16(bfr[st & 1][ni], af[st][mi], acc[mi][ni], 0, 0, 0);
      if (st < 11) interleave_mfma_ds<10, 10>();
      __builtin_amdgcn_sched_barrier(0);
      if (st + APF < 12) ldA(st + APF);
    }
    STAMP(2, 8);
    f32x4* red = (f32x4*)(lds + BE_OFF) + (wmp * 40) * 64 + lane;
#pragma unroll
    for (int t = 0; t < 10; ++t)
      if ((t & 3) != wk) red[(t * 4 + wk) * 64] = acc[t / 5][t % 5];
    RAW_BAR();
    asm volatile("" ::: "memory");
#pragma unroll
    for (int t = 0; t < 10; ++t) {
      if ((t & 3) == wk) {
        const int mi = t / 5, ni = t % 5;
        f32x4 o = acc[mi][ni];
#pragma unroll
        for (int w2_ = 0; w2_ < 4; ++w2_)
          if (w2_ != (t & 3)) o += red[(t * 4 + w2_) * 64];
        const int lr = 32 * wmp + 16 * mi + (lane & 15);
        const int prow = rb2 * 64 + lr;
        const int mrow = (prow >> 3) + 256 * (prow & 7);
        const int nc = 16 * ni + 4 * (lane >> 4);
        const u32x2 bb = *(const u32x2*)(lds + B2_OFF + nc * 2);
        const u32x2 rsd = *(const u32x2*)(lds + RES_OFF + lr * 160 + nc * 2);
        f32x2 a01 = {o[0], o[1]}, a23 = {o[2], o[3]};
        u32x2 ov;
        ov.x = bf_pack(bf_unpack(rsd.x) + bf_round(bf_round(a01) + bf_unpack(bb.x)));
        ov.y = bf_pack(bf_unpack(rsd.y) + bf_round(bf_round(a23) + bf_unpack(bb.y)));
        *(u32x2*)(out + (size_t)mrow * D_ + cb2 * 80 + nc) = ov;
      }
    }
  }
  STAMP_END(2, 9);
}

__global__ void k_zz_end() {}
extern "C" void kernel_launch(void* const* d_in, const int* in_sizes, int n_in, void* d_out, int out_size,
                              void* d_ws, size_t ws_size, hipStream_t stream) {
  const uint32_t* x = (const uint32_t*)d_in[0];
  const uint16_t* wr = (const uint16_t*)d_in[1];
  const uint16_t* wi = (const uint16_t*)d_in[2];
  const uint16_t* w1 = (const uint16_t*)d_in[4];
  const uint16_t* b1 = (const uint16_t*)d_in[5];
  const uint16_t* w2 = (const uint16_t*)d_in[6];
  const uint16_t* b2 = (const uint16_t*)d_in[7];
  char* ws = (char*)d_ws;
  size_t off = 0;
  f32x2* T = (f32x2*)(ws + off); off += (size_t)8 * 256 * TP * 8;
  uint32_t* h = (uint32_t*)(ws + off); off += (size_t)S_ * D_ * 2;
  uint32_t* hn = (uint32_t*)(ws + off); off += (size_t)S_ * D_ * 2;
  u32x4* h1 = (u32x4*)(ws + off); off += (size_t)S_ * F_ * 2;
  u32x4* w2p = (u32x4*)(ws + off); off += (size_t)D_ * F_ * 2;

  uint32_t* cnt = (uint32_t*)(ws + off); off += 32 * 256;
  k_fwd_rows<<<256, 1024, 0, stream>>>(x, T, wr, wi, (const uint32_t*)w1, (const u32x4*)w2, w2p, cnt);
  k_cols<<<328 + 240, 256, 0, stream>>>(T, wr, wi, (const u32x4*)w2, w2p);
  k_tail<<<256, 1024, 0, stream>>>(T, x, h, hn, (u32x2*)h1, (const char*)w1, b1, w2p, b2, (uint16_t*)d_out, cnt);
}
```

```cpp
#include <hip/hip_runtime.h>
#include <hip/hip_bf16.h>
#include <stdint.h>

typedef float f32x2 __attribute__((ext_vector_type(2)));
typedef float f32x4 __attribute__((ext_vector_type(4)));
typedef short bf16x8 __attribute__((ext_vector_type(8)));
typedef unsigned int u32x4 __attribute__((ext_vector_type(4)));
typedef unsigned int u32x2 __attribute__((ext_vector_type(2)));

#define DEV __device__ __forceinline__

#define STAMP(kern, i)
#define STAMP_END(kern, i)
#define STAMP_TID 0

static constexpr int S_ = 2048, D_ = 640, F_ = 1536, DF_ = 321;
static constexpr int TP = 328;
static constexpr int NEARLY = 4;

DEV float bf_lo(uint32_t p) { return __builtin_bit_cast(float, p << 16); }
DEV float bf_hi(uint32_t p) { return __builtin_bit_cast(float, p & 0xffff0000u); }
DEV f32x2 bf_unpack(uint32_t p) { f32x2 r; r.x = bf_lo(p); r.y = bf_hi(p); return r; }
typedef __bf16 bf16x2_t __attribute__((ext_vector_type(2)));
DEV uint32_t bf_pack(f32x2 v) {
  return __builtin_bit_cast(uint32_t, __builtin_convertvector(v, bf16x2_t));
}
DEV f32x2 bf_round(f32x2 v) { return bf_unpack(bf_pack(v)); }
DEV float bfr(float f) { f32x2 v; v.x = f; v.y = f; return bf_lo(bf_pack(v)); }

DEV f32x2 cadd_mi(f32x2 b, f32x2 d) {
  f32x2 r;
  asm("v_pk_add_f32 %0, %1, %2 op_sel:[0,1] op_sel_hi:[1,0] neg_hi:[0,1]" : "=v"(r) : "v"(b), "v"(d));
  return r;
}
DEV f32x2 cadd_pi(f32x2 b, f32x2 d) {
  f32x2 r;
  asm("v_pk_add_f32 %0, %1, %2 op_sel:[0,1] op_sel_hi:[1,0] neg_lo:[0,1]" : "=v"(r) : "v"(b), "v"(d));
  return r;
}
DEV f32x2 cnadd_mi(f32x2 b, f32x2 d) {
  f32x2 r;
  asm("v_pk_add_f32 %0, %1, %2 op_sel:[0,1] op_sel_hi:[1,0] neg_lo:[1,0] neg_hi:[1,1]" : "=v"(r) : "v"(b), "v"(d));
  return r;
}
DEV f32x2 cnadd_pi(f32x2 b, f32x2 d) {
  f32x2 r;
  asm("v_pk_add_f32 %0, %1, %2 op_sel:[0,1] op_sel_hi:[1,0] neg_lo:[1,1] neg_hi:[1,0]" : "=v"(r) : "v"(b), "v"(d));
  return r;
}
DEV f32x2 cfma_mi(f32x2 s, f32x2 d, f32x2 b) {
  f32x2 r;
  asm("v_pk_fma_f32 %0, %1, %2, %3 op_sel:[0,1,0] op_sel_hi:[1,0,1] neg_hi:[0,1,0]" : "=v"(r) : "v"(s), "v"(d), "v"(b));
  return r;
}
DEV f32x2 cfma_pi(f32x2 s, f32x2 d, f32x2 b) {
  f32x2 r;
  asm("v_pk_fma_f32 %0, %1, %2, %3 op_sel:[0,1,0] op_sel_hi:[1,0,1] neg_lo:[0,1,0]" : "=v"(r) : "v"(s), "v"(d), "v"(b));
  return r;
}
DEV f32x2 pfma(f32x2 a, f32x2 b, f32x2 c) { return __builtin_elementwise_fma(a, b, c); }
DEV f32x2 cmul(f32x2 a, f32x2 w) {
  f32x2 t, r;
  asm("v_pk_mul_f32 %0, %1, %2 op_sel:[0,0] op_sel_hi:[0,1]" : "=v"(t) : "v"(a), "v"(w));
  asm("v_pk_fma_f32 %0, %1, %2, %3 op_sel:[1,1,0] op_sel_hi:[1,0,1] neg_lo:[0,1,0]" : "=v"(r) : "v"(a), "v"(w), "v"(t));
  return r;
}
DEV f32x2 cmulc(f32x2 a, f32x2 w) {
  f32x2 t, r;
  asm("v_pk_mul_f32 %0, %1, %2 op_sel:[0,0] op_sel_hi:[0,1] neg_hi:[0,1]" : "=v"(t) : "v"(a), "v"(w));
  asm("v_pk_fma_f32 %0, %1, %2, %3 op_sel:[1,1,0] op_sel_hi:[1,0,1]" : "=v"(r) : "v"(a), "v"(w), "v"(t));
  return r;
}
DEV f32x2 twid(float f) {
  f32x2 r;
  r.x = __builtin_amdgcn_cosf(f);
  r.y = __builtin_amdgcn_sinf(-f);
  return r;
}
DEV void st_wt(f32x2* p, f32x2 v) {
  __hip_atomic_store((unsigned long long*)p, __builtin_bit_cast(unsigned long long, v), __ATOMIC_RELAXED,
                     __HIP_MEMORY_SCOPE_AGENT);
}
DEV void st_wt32(uint32_t* p, uint32_t v) { __hip_atomic_store(p, v, __ATOMIC_RELAXED, __HIP_MEMORY_SCOPE_AGENT); }
DEV void st_wt64(u32x2* p, u32x2 v) {
  __hip_atomic_store((unsigned long long*)p, __builtin_bit_cast(unsigned long long, v), __ATOMIC_RELAXED,
                     __HIP_MEMORY_SCOPE_AGENT);
}
DEV void st_wt128(void* p, u32x4 v) {
  asm volatile("global_store_dwordx4 %0, %1, off sc1\n\ts_nop 1" ::"v"(p), "v"(v) : "memory");
}
template <int N> DEV void pin(f32x2 (&a)[N]) {
#pragma unroll
  for (int i = 0; i < N; ++i) asm volatile("" : "+v"(a[i]));
}
template <bool INV> DEV f32x2 add_rot(f32x2 b, f32x2 d) { return INV ? cadd_pi(b, d) : cadd_mi(b, d); }
template <bool INV> DEV f32x2 sub_rot(f32x2 b, f32x2 d) { return INV ? cadd_mi(b, d) : cadd_pi(b, d); }

template <bool INV> DEV void fft4(f32x2& a0, f32x2& a1, f32x2& a2, f32x2& a3) {
  f32x2 b0 = a0 + a2, b1 = a0 - a2, b2 = a1 + a3, d = a1 - a3;
  a0 = b0 + b2; a2 = b0 - b2;
  a1 = add_rot<INV>(b1, d); a3 = sub_rot<INV>(b1, d);
}
template <bool INV> DEV void fft8(f32x2 (&v)[8]) {
  const f32x2 r = {0.70710678118654752440f, 0.70710678118654752440f};
  const f32x2 nr = {-0.70710678118654752440f, -0.70710678118654752440f};
  f32x2 e0 = v[0] + v[4], e1 = v[1] + v[5], e2 = v[2] + v[6], e3 = v[3] + v[7];
  f32x2 o0 = v[0] - v[4], o1 = v[1] - v[5], o2 = v[2] - v[6], o3 = v[3] - v[7];
  fft4<INV>(e0, e1, e2, e3);
  f32x2 p1 = INV ? cadd_pi(o1, o1) : cadd_mi(o1, o1);
  f32x2 p3 = INV ? cnadd_pi(o3, o3) : cnadd_mi(o3, o3);
  f32x2 s = p1 + p3, d = p1 - p3;
  f32x2 b0 = add_rot<INV>(o0, o2), b1 = sub_rot<INV>(o0, o2);
  v[0] = e0; v[2] = e1; v[4] = e2; v[6] = e3;
  v[1] = pfma(r, s, b0); v[5] = pfma(nr, s, b0);
  v[3] = INV ? cfma_pi(r, d, b1) : cfma_mi(r, d, b1);
  v[7] = INV ? cfma_mi(r, d, b1) : cfma_pi(r, d, b1);
}
template <bool INV> DEV void fft5(f32x2 (&v)[5]) {
  const f32x2 c1 = {0.30901699437494742410f, 0.30901699437494742410f};
  const f32x2 c2 = {-0.80901699437494742410f, -0.80901699437494742410f};
  const f32x2 s1 = {0.95105651629515357212f, 0.95105651629515357212f};
  const f32x2 s2 = {0.58778525229247312917f, 0.58778525229247312917f};
  const f32x2 ns1 = {-0.95105651629515357212f, -0.95105651629515357212f};
  f32x2 t1 = v[1] + v[4], t2 = v[2] + v[3], t3 = v[1] - v[4], t4 = v[2] - v[3];
  f32x2 a1 = pfma(c2, t2, pfma(c1, t1, v[0]));
  f32x2 a2 = pfma(c1, t2, pfma(c2, t1, v[0]));
  f32x2 b1 = pfma(s2, t4, s1 * t3);
  f32x2 b2 = pfma(ns1, t4, s2 * t3);
  v[0] = v[0] + t1 + t2;
  v[1] = add_rot<INV>(a1, b1); v[4] = sub_rot<INV>(a1, b1);
  v[2] = add_rot<INV>(a2, b2); v[3] = sub_rot<INV>(a2, b2);
}

template <int CTRL> DEV float dppmov(float v) {
  return __builtin_bit_cast(float, __builtin_amdgcn_update_dpp(0, __builtin_bit_cast(int, v), CTRL, 0xf, 0xf, false));
}
DEV float wave_sum(float v) {
  v += dppmov<0xB1>(v);
  v += dppmov<0x4E>(v);
  v += dppmov<0x141>(v);
  v += dppmov<0x140>(v);
  int iv = __builtin_bit_cast(int, v);
  float a = __builtin_bit_cast(float, __builtin_amdgcn_readlane(iv, 0));
  float b = __builtin_bit_cast(float, __builtin_amdgcn_readlane(iv, 16));
  float c = __builtin_bit_cast(float, __builtin_amdgcn_readlane(iv, 32));
  float d = __builtin_bit_cast(float, __builtin_amdgcn_readlane(iv, 48));
  return (a + b) + (c + d);
}
DEV void wave_sync() {
  __builtin_amdgcn_fence(__ATOMIC_RELEASE, "wavefront");
  __builtin_amdgcn_wave_barrier();
  __builtin_amdgcn_fence(__ATOMIC_ACQUIRE, "wavefront");
}

DEV void row_layernorm(const f32x2 (&xv)[5], uint32_t (&zp)[5]) {
  f32x2 s2 = (xv[0] + xv[1]) + (xv[2] + xv[3]) + xv[4];
  const float mean = wave_sum(s2.x + s2.y) * (1.0f / 640.0f);
  const f32x2 nm = {-mean, -mean};
  f32x2 q2 = {0.f, 0.f};
#pragma unroll
  for (int i = 0; i < 5; ++i) { f32x2 d = xv[i] + nm; q2 = pfma(d, d, q2); }
  const float var = wave_sum(q2.x + q2.y) * (1.0f / 640.0f);
  f32x2 mv; mv.x = mean; mv.y = var;
  mv = bf_round(mv);
  const float den = bfr(sqrtf(bfr(mv.y + 1e-5f)));
  const float rden = 1.0f / den;
  const f32x2 nmb = {-mv.x, -mv.x}, rd = {rden, rden};
#pragma unroll
  for (int i = 0; i < 5; ++i) zp[i] = bf_pack(bf_round(xv[i] + nmb) * rd);
}

__global__ __launch_bounds__(1024) void k_fwd_rows(const uint32_t* __restrict__ x, f32x2* __restrict__ T,
                                                  const uint16_t* __restrict__ wr, const uint16_t* __restrict__ wi,
                                                  const uint32_t* __restrict__ w1, const u32x4* __restrict__ w2,
                                                  u32x4* __restrict__ w2p, uint32_t* __restrict__ cnt) {
  __shared__ f32x2 ex[8][360];
  __shared__ f32x2 zz[8][320];
  if (threadIdx.x >= 512) {
    const int tid = threadIdx.x - 512, lane = tid & 63, j = tid >> 6;
  const int wid = min((int)blockIdx.x * 512 + tid, 122879);
  const u32x4 wchunk = w2[wid];

  uint32_t pf = 0;
  {
    const char* pa = nullptr;
    if (lane < 12) {
      const int t = (blockIdx.x & 7) + 8 * (8 * (blockIdx.x >> 3) + j);
      pa = (const char*)(((lane < 6) ? wr : wi) + (t * DF_ + min(64 * (lane % 6), DF_ - 2)));
      pa = (const char*)((uintptr_t)pa & ~(uintptr_t)3);
    } else if (lane >= 56) {
      pa = (const char*)(w1 + (blockIdx.x * 60 + min(j * 8 + (lane - 56), 59)) * 32);
    }
    else if (lane < 18) {
      unsigned long long c0, c1;
      asm volatile("s_getpc_b64 s[40:41]\n\ts_add_u32 s40, s40, _Z6k_colsPDv2_fPKtS2_@rel32@lo+4\n\ts_addc_u32 s41, s41, _Z6k_colsPDv2_fPKtS2_@rel32@hi+12\n\ts_mov_b64 %0, s[40:41]" : "=s"(c0) :: "s40", "s41", "scc");
      asm volatile("s_getpc_b64 s[40:41]\n\ts_add_u32 s40, s40, _Z8k_zz_endv@rel32@lo+4\n\ts_addc_u32 s41, s41, _Z8k_zz_endv@rel32@hi+12\n\ts_mov_b64 %0, s[40:41]" : "=s"(c1) :: "s40", "s41", "scc");
      const long long len = (long long)(c1 - c0);
      const int li = ((blockIdx.x >> 3) * 8 + j) * 2 + (lane - 16);
      if (lane >= 16 && len > 0 && len < 65536 && li * 128 < len) pa = (const char*)(c0 + li * 128);
    }
    if (pa) pf = *(const uint32_t*)pa;
  }

  if (blockIdx.x < 240) {
    const int wk_ = wid / 80, wc_ = wid % 80;
    const int rho = (wk_ & ~12) | ((wk_ & 4) << 1) | ((wk_ & 8) >> 1);
    st_wt128(w2p + ((wc_ / 10) * 1536 + rho) * 10 + (wc_ % 10), wchunk);
  }
  asm volatile("" ::"v"(pf));
    return;
  }
  const int tid = threadIdx.x, lane = tid & 63, j = tid >> 6;
  STAMP(0, 0);
  if (blockIdx.x == 0 && tid < 64) st_wt32(cnt + tid * 32, 0u);
  const int s0 = 32 * (blockIdx.x & 7) + (blockIdx.x >> 3);
  const int row = s0 + 256 * j;
  uint32_t xd[5];
#pragma unroll
  for (int i = 0; i < 5; ++i) xd[i] = x[row * 320 + lane + 64 * i];
  f32x2 tw1[4], tw2[7], twq[7];
#pragma unroll
  for (int r = 1; r < 5; ++r) tw1[r - 1] = twid((float)(lane * r) * (1.0f / 320.0f));
  const int l0 = lane & 7, rr = lane >> 3;
#pragma unroll
  for (int q = 1; q < 8; ++q) tw2[q - 1] = twid((float)(l0 * q) * (1.0f / 64.0f));
#pragma unroll
  for (int q = 1; q < 8; ++q) twq[q - 1] = twid((float)(s0 * q) * (1.0f / 2048.0f));
  f32x2 twk_[1] = {twid((float)tid * (1.0f / 640.0f))};
  pin(tw1); pin(tw2); pin(twq); pin(twk_);
  const f32x2 twk = twk_[0];
  __builtin_amdgcn_sched_barrier(0);
  STAMP(0, 1);

  f32x2 u[5];
  {
    f32x2 xv[5];
    uint32_t zp[5];
#pragma unroll
    for (int i = 0; i < 5; ++i) xv[i] = bf_unpack(xd[i]);
    row_layernorm(xv, zp);
#pragma unroll
    for (int i = 0; i < 5; ++i) u[i] = bf_unpack(zp[i]);
  }
  STAMP(0, 2);
  f32x2* E = ex[j];
  fft5<false>(u);
  E[lane] = u[0];
#pragma unroll
  for (int r = 1; r < 5; ++r) E[r * 72 + lane] = cmul(u[r], tw1[r - 1]);
  wave_sync();
  if (lane < 40) {
    f32x2 v[8];
    f32x2* Er = E + rr * 72;
#pragma unroll
    for (int l1 = 0; l1 < 8; ++l1) v[l1] = Er[l0 + 8 * l1];
    fft8<false>(v);
    wave_sync();
    Er[l0] = v[0];
#pragma unroll
    for (int q = 1; q < 8; ++q) Er[q * 9 + l0] = cmul(v[q], tw2[q - 1]);
    wave_sync();
#pragma unroll
    for (int a = 0; a < 8; ++a) v[a] = Er[l0 * 9 + a];
    fft8<false>(v);
    f32x2* Z = zz[j] + rr + 5 * l0;
#pragma unroll
    for (int p = 0; p < 8; ++p) Z[40 * p] = v[p];
  }
  STAMP(0, 3);
  __syncthreads();
  STAMP(0, 4);
  if (j < 6) {
    const int k = tid;
    const int ka = (k >= 320) ? 0 : k, kb = (k == 0 || k >= 320) ? 0 : 320 - k;
    f32x2 v[8];
#pragma unroll
    for (int jj = 0; jj < 8; ++jj) {
      const f32x2 a = zz[jj][ka], b = zz[jj][kb];
      f32x2 e, d;
      e.x = a.x + b.x; e.y = a.y - b.y;
      d.x = a.x - b.x; d.y = a.y + b.y;
      v[jj] = cadd_mi(e, cmul(d, twk));
    }
    fft8<false>(v);
    if (k < DF_) {
      f32x2* dst = T + (size_t)s0 * TP;
      st_wt(dst + k, v[0]);
#pragma unroll
      for (int q = 1; q < 8; ++q) st_wt(dst + (size_t)q * 256 * TP + k, cmul(v[q], twq[q - 1]));
    }
  }
  STAMP(0, 5);
  STAMP_END(0, 6);
}

__global__ __launch_bounds__(256) void k_cols(f32x2* __restrict__ T, const uint16_t* __restrict__ wr,
                                              const uint16_t* __restrict__ wi) {
  __shared__ f32x2 e1[2048];
  __shared__ f32x2 e2[4][576];
  __shared__ __attribute__((aligned(16))) uint32_t wl[2048];
  const int tid = threadIdx.x, lane = tid & 63, wv = tid >> 6;
  STAMP(1, 0);
  const int q = blockIdx.x & 7, kg = blockIdx.x >> 3;
  const int c = tid & 7, a = tid >> 3;
  const int k = kg * 8 + c;
  const bool valid = k < DF_;
  f32x2* colbase = T + (size_t)(q * 256) * TP;
  const uint32_t coff = a * TP + (valid ? k : DF_ - 1);
  f32x2 v[8];
#pragma unroll
  for (int j1 = 0; j1 < 8; ++j1) v[j1] = (colbase + (size_t)j1 * 32 * TP)[coff];
  const int kb = min(8 * kg, DF_ - 8);
  u32x4 wre, wim;
  __builtin_memcpy(&wre, wr + (size_t)(q + 8 * tid) * DF_ + kb, 16);
  __builtin_memcpy(&wim, wi + (size_t)(q + 8 * tid) * DF_ + kb, 16);

  const int b = (lane >> 3) & 3, p1lo = lane >> 5, p1 = 2 * wv + p1lo;
  f32x2 twa[7], twb[7];
#pragma unroll
  for (int p = 1; p < 8; ++p) twa[p - 1] = twid((float)(a * p) * (1.0f / 256.0f));
#pragma unroll
  for (int p = 1; p < 8; ++p) twb[p - 1] = twid((float)(b * p) * (1.0f / 32.0f));
  pin(twa); pin(twb);
  __builtin_amdgcn_sched_barrier(0);
  STAMP(1, 1);

  fft8<false>(v);
  e1[tid] = v[0];
#pragma unroll
  for (int p = 1; p < 8; ++p) e1[p * 256 + tid] = cmul(v[p], twa[p - 1]);
  {
    u32x4 o0, o1;
    o0.x = __builtin_amdgcn_perm(wim.x, wre.x, 0x05040100); o0.y = __builtin_amdgcn_perm(wim.x, wre.x, 0x07060302);
    o0.z = __builtin_amdgcn_perm(wim.y, wre.y, 0x05040100); o0.w = __builtin_amdgcn_perm(wim.y, wre.y, 0x07060302);
    o1.x = __builtin_amdgcn_perm(wim.z, wre.z, 0x05040100); o1.y = __builtin_amdgcn_perm(wim.z, wre.z, 0x07060302);
    o1.z = __builtin_amdgcn_perm(wim.w, wre.w, 0x05040100); o1.w = __builtin_amdgcn_perm(wim.w, wre.w, 0x07060302);
    *(u32x4*)(wl + tid * 8) = o0;
    *(u32x4*)(wl + tid * 8 + 4) = o1;
  }
  STAMP(1, 2);
  __syncthreads();
  STAMP(1, 3);
  uint32_t wf[8];
  {
    const uint32_t* wlp = wl + (p1 + 16 * b) * 8 + min(8 * kg + c - kb, 7);
#pragma unroll
    for (int e = 0; e < 2; ++e)
#pragma unroll
      for (int mm = 0; mm < 4; ++mm) wf[e * 4 + mm] = wlp[(8 * e + 64 * mm) * 8];
  }
  f32x2* E1 = e1 + p1 * 256 + (lane & 31);
#pragma unroll
  for (int j2 = 0; j2 < 8; ++j2) v[j2] = E1[32 * j2];
  fft8<false>(v);
  f32x2* E2w = e2[wv] + p1lo * 288 + (lane & 31);
  E2w[0] = v[0];
#pragma unroll
  for (int p = 1; p < 8; ++p) E2w[p * 36] = cmul(v[p], twb[p - 1]);
  wave_sync();
  f32x2* E2r = e2[wv] + p1lo * 288 + (2 * b) * 36 + c;
#pragma unroll
  for (int e = 0; e < 2; ++e)
#pragma unroll
    for (int bb = 0; bb < 4; ++bb) v[e * 4 + bb] = E2r[e * 36 + bb * 8];
  fft4<false>(v[0], v[1], v[2], v[3]);
  fft4<false>(v[4], v[5], v[6], v[7]);
  STAMP(1, 4);
#pragma unroll
  for (int i = 0; i < 8; ++i) v[i] = cmul(v[i], bf_unpack(wf[i]));
  fft4<true>(v[0], v[1], v[2], v[3]);
  fft4<true>(v[4], v[5], v[6], v[7]);
  STAMP(1, 5);
#pragma unroll
  for (int e = 0; e < 2; ++e)
#pragma unroll
    for (int bb = 0; bb < 4; ++bb) E2r[e * 36 + bb * 8] = v[e * 4 + bb];
  wave_sync();
  v[0] = E2w[0];
#pragma unroll
  for (int p = 1; p < 8; ++p) v[p] = cmulc(E2w[p * 36], twb[p - 1]);
  fft8<true>(v);
#pragma unroll
  for (int j2 = 0; j2 < 8; ++j2) E1[32 * j2] = v[j2];
  STAMP(1, 6);
  __syncthreads();
  STAMP(1, 7);
  v[0] = e1[tid];
#pragma unroll
  for (int p = 1; p < 8; ++p) v[p] = cmulc(e1[p * 256 + tid], twa[p - 1]);
  fft8<true>(v);
  if (valid) {
#pragma unroll
    for (int j1 = 0; j1 < 8; ++j1) st_wt(colbase + (size_t)j1 * 32 * TP + coff, v[j1]);
  }
  STAMP(1, 8);
  STAMP_END(1, 9);
}

template <int N> DEV void wait_vm() { asm volatile("s_waitcnt vmcnt(%0)" ::"n"(N) : "memory"); }
#define RAW_BAR() do { asm volatile("s_waitcnt lgkmcnt(0)" ::: "memory"); __builtin_amdgcn_s_barrier(); } while (0)

DEV void glds16(const void* src, char* lds) {
  __builtin_amdgcn_global_load_lds((const unsigned*)src, (unsigned*)lds, 16, 0, 0);
}
DEV uint32_t pk_relu_bf16(uint32_t p) {
  uint32_t r;
  asm("v_pk_max_i16 %0, %1, 0" : "=v"(r) : "v"(p));
  return r;
}

typedef short v4s_t __attribute__((ext_vector_type(4)));
typedef __attribute__((address_space(3))) v4s_t* lds_v4s_ptr;
DEV bf16x8 tr_frag(const char* p0, const char* p1) {
  const v4s_t a = __builtin_amdgcn_ds_read_tr16_b64_v4i16((lds_v4s_ptr)p0);
  const v4s_t b = __builtin_amdgcn_ds_read_tr16_b64_v4i16((lds_v4s_ptr)p1);
  return __builtin_shufflevector(a, b, 0, 1, 2, 3, 4, 5, 6, 7);
}

#define SGB(mask, n) __builtin_amdgcn_sched_group_barrier(mask, n, 0)
template <int NM, int ND> DEV void interleave_mfma_ds() {
  if constexpr (NM == 6) {
    SGB(0x008, 1); SGB(0x100, 2); SGB(0x008, 1); SGB(0x100, 2); SGB(0x008, 1); SGB(0x100, 2);
    SGB(0x008, 1); SGB(0x100, 2); SGB(0x008, 2);
  } else if constexpr (NM == 10) {
    SGB(0x008, 1); SGB(0x100, 2); SGB(0x008, 1); SGB(0x100, 2); SGB(0x008, 1); SGB(0x100, 2);
    SGB(0x008, 1); SGB(0x100, 2); SGB(0x008, 1); SGB(0x100, 2); SGB(0x008, 5);
  } else {
    SGB(0x008, 1); SGB(0x100, 2); SGB(0x008, 1); SGB(0x100, 2); SGB(0x008, 1); SGB(0x100, 2);
    SGB(0x008, 1); SGB(0x100, 2); SGB(0x008, 1); SGB(0x100, 2);
  }
}
DEV void wait_vm_rt(int n) {
  switch (n) {
    case 0: wait_vm<0>(); break;   case 1: wait_vm<1>(); break;   case 2: wait_vm<2>(); break;
    case 3: wait_vm<3>(); break;   case 4: wait_vm<4>(); break;   case 5: wait_vm<5>(); break;
    case 6: wait_vm<6>(); break;   case 7: wait_vm<7>(); break;   case 8: wait_vm<8>(); break;
    case 9: wait_vm<9>(); break;   case 10: wait_vm<10>(); break; case 11: wait_vm<11>(); break;
    case 12: wait_vm<12>(); break; case 13: wait_vm<13>(); break; case 14: wait_vm<14>(); break;
    default: wait_vm<0>(); break;
  }
}
DEV void glds16_dev(const void* src, char* lds) {
  __builtin_amdgcn_global_load_lds((const unsigned*)src, (unsigned*)lds, 16, 0, 16);
}
DEV void lds_spin(uint32_t* w, uint32_t want) {
  for (int i = 0; i < (1 << 24); ++i) {
    const uint32_t v = __hip_atomic_load(w, __ATOMIC_RELAXED, __HIP_MEMORY_SCOPE_WORKGROUP);
    if ((uint32_t)__builtin_amdgcn_readfirstlane(v) >= want) break;
    __builtin_amdgcn_s_sleep(1);
  }
  asm volatile("" ::: "memory");
}
DEV void wait_cluster(uint32_t* lcnt, uint32_t* lflag, uint32_t* cnt, int lw, int lane) {
  if (lw == 0) {
    lds_spin(lcnt, 8u);
    __builtin_amdgcn_s_sleep(12);
    for (int i = 0; i < (1 << 22); ++i) {
      const uint32_t v = __hip_atomic_load(cnt, __ATOMIC_RELAXED, __HIP_MEMORY_SCOPE_AGENT);
      if ((uint32_t)__builtin_amdgcn_readfirstlane(v) >= 16u) break;
      __builtin_amdgcn_s_sleep(2);
    }
    asm volatile("" ::: "memory");
    if (lane == 0) __hip_atomic_store(lflag, 1u, __ATOMIC_RELAXED, __HIP_MEMORY_SCOPE_WORKGROUP);
  } else {
    lds_spin(lflag, 1u);
  }
}
DEV void publish(uint32_t* lcnt, uint32_t* gcnt, int lane) {
  asm volatile("s_waitcnt vmcnt(0)" ::: "memory");
  if (lane == 0) {
    const uint32_t old = __hip_atomic_fetch_add(lcnt, 1u, __ATOMIC_RELAXED, __HIP_MEMORY_SCOPE_WORKGROUP);
    if (old == 7u) {
      __hip_atomic_fetch_add(gcnt, 1u, __ATOMIC_RELAXED, __HIP_MEMORY_SCOPE_AGENT);
    }
  }
}

#undef STAMP_TID
#define STAMP_TID 0
__global__ __launch_bounds__(1024) void k_tail(const f32x2* __restrict__ T, const uint32_t* __restrict__ x,
                                               uint32_t* h, uint32_t* hn, u32x2* h1,
                                               const char* __restrict__ w1, const uint16_t* __restrict__ b1,
                                               const u32x4* __restrict__ w2p, const uint16_t* __restrict__ b2,
                                               uint16_t* __restrict__ out, uint32_t* cnt) {
  constexpr int BD_OFF = 0, AD_OFF = 6 * 12288;
  constexpr int BE_OFF = 0, NBE = 7, RES_OFF = NBE * 20480;
  constexpr int MISC_OFF = 163840 - 1024, B1_OFF = MISC_OFF, LC_OFF = MISC_OFF + 256, B2_OFF = MISC_OFF + 512;
  __shared__ __attribute__((aligned(16))) char lds[163840];
  const int tid = threadIdx.x, lane = tid & 63;
  const int wv = __builtin_amdgcn_readfirstlane(tid >> 6);
  const int xcd = blockIdx.x & 7, idx = blockIdx.x >> 3;
  const int cl = 2 * xcd + (idx >> 4), mi_ = idx & 15;
  const int cb = mi_, rb2 = 2 * cl + (mi_ >> 3), cb2 = mi_ & 7;
  uint32_t* cnt1 = cnt + cl * 64;
  uint32_t* cnt2 = cnt + (16 + cl) * 64;
  uint32_t* lcnt = (uint32_t*)(lds + LC_OFF);

  if (wv >= 8) {
    const int lw = wv - 8;
    const int nb = (lw < 4) ? 2 : 1, nb2 = (lw < 4) ? 3 : 2;
    const int arow = lane >> 3;
    const char* asrc = (const char*)hn + (size_t)(cl * 128 + 16 * lw + arow) * 1280 + (((lane & 7) ^ arow) << 4);
    const int id0 = lw * 64 + lane, r0 = id0 / 12, c0 = (id0 % 12) ^ (((r0 >> 3) & 1) << 1);
    const int id1 = (8 + lw) * 64 + lane, r1 = id1 / 12, c1 = (id1 % 12) ^ (((r1 >> 3) & 1) << 1);
    const char* bsrc0 = w1 + (size_t)r0 * 3072 + cb * 192 + c0 * 16;
    const char* bsrc1 = w1 + (size_t)r1 * 3072 + cb * 192 + c1 * 16;
    auto stageB = [&](int st) {
      char* dst = lds + BD_OFF + (st % 6) * 12288 + lane * 16;
      glds16(bsrc0 + (size_t)st * 64 * 3072, dst + lw * 1024);
      if (lw < 4) glds16(bsrc1 + (size_t)st * 64 * 3072, dst + (8 + lw) * 1024);
    };
    auto stageA = [&](int st) {
      char* dst = lds + AD_OFF + (st % 5) * 16384 + lane * 16;
      glds16_dev(asrc + st * 128, dst + (2 * lw) * 1024);
      glds16_dev(asrc + st * 128 + 8 * 1280, dst + (2 * lw + 1) * 1024);
    };
    if (lw == 7 && lane < 12) glds16((const u32x4*)(b1 + cb * 96) + lane, lds + B1_OFF + lane * 16);
    if (lw == 6 && lane < 10) glds16((const u32x4*)(b2 + cb2 * 80) + lane, lds + B2_OFF + lane * 16);
    __builtin_amdgcn_s_barrier();
#pragma unroll
    for (int st = 0; st < 6; ++st) stageB(st);
    wait_cluster(lcnt, lcnt + 2, cnt1, lw, lane);
#pragma unroll
    for (int st = 0; st < 5; ++st) stageA(st);
#pragma unroll
    for (int st = 0; st < 10; ++st) {
      const int ka = (st == 0) ? 8 : (st <= 6) ? 6 : (st == 7) ? 4 : (st == 8) ? 2 : 0;
      const int kb = (st <= 1) ? 0 : (st <= 5) ? st - 1 : (st == 6) ? 3 : (st == 7) ? 2 : (st == 8) ? 1 : 0;
      if (lw < 4) wait_vm_rt(ka + 2 * kb); else wait_vm_rt(ka + kb);
      __builtin_amdgcn_s_barrier();
      if (st >= 1 && st + 4 < 10) stageA(st + 4);
      if (st >= 1 && st + 5 < 10) stageB(st + 5);
    }
    __builtin_amdgcn_s_barrier();
    const u32x4* bsrc2 = w2p + (size_t)cb2 * 1536 * 10 + lw * 64 + lane;
    auto stageB2 = [&](int st) {
      char* dst = lds + BE_OFF + (st % NBE) * 20480 + lane * 16;
      glds16(bsrc2 + st * 1280, dst + lw * 1024);
      glds16(bsrc2 + st * 1280 + 512, dst + (8 + lw) * 1024);
      if (lw < 4) glds16(bsrc2 + st * 1280 + 1024, dst + (16 + lw) * 1024);
    };
#pragma unroll
    for (int st = 0; st < NEARLY; ++st) stageB2(st);
    if (NEARLY < NBE) {
      lds_spin(lcnt + 1, 8u);
#pragma unroll
      for (int st = NEARLY; st < NBE; ++st) stageB2(st);
    }
    {
      const int id = lw * 64 + lane, r = id / 10, ch = id % 10;
      glds16_dev((const u32x4*)((const uint16_t*)h + (size_t)(rb2 * 64 + r) * D_ + cb2 * 80) + ch, lds + RES_OFF + id * 16);
      if (lw >= 6) {
        const int id2 = (lw + 2) * 64 + lane, r2 = id2 / 10, ch2 = id2 % 10;
        glds16_dev((const u32x4*)((const uint16_t*)h + (size_t)(rb2 * 64 + r2) * D_ + cb2 * 80) + ch2, lds + RES_OFF + id2 * 16);
      }
    }
    if (lw == 0) wait_cluster(lcnt + 1, lcnt + 3, cnt2, lw, lane);
    wait_vm<0>();
#pragma unroll
    for (int st = 0; st < 12; ++st) {
      const int later = (st < NBE) ? 99 : ((st + 5 < 11 ? st + 5 : 11) - st);
      if (later != 99) { if (lw < 4) wait_vm_rt(3 * later); else wait_vm_rt(2 * later); }
      __builtin_amdgcn_s_barrier();
      if (st >= 1 && st + 6 < 12) stageB2(st + 6);
    }
    __builtin_amdgcn_s_barrier();
    (void)nb; (void)nb2;
    return;
  }

  STAMP(2, 0);
  {
    f32x2(*ex)[360] = (f32x2(*)[360])(lds + AD_OFF);
    f32x2(*xx)[384] = (f32x2(*)[384])(lds + AD_OFF + 23040);
    const int j = wv;
    const int s0 = 32 * xcd + idx;
    const int row = s0 + 256 * j;
    const int prow = s0 * 8 + j;
    f32x2 v[8];
    {
      const f32x2* src = T + (size_t)s0 * TP;
      const uint32_t kk = min(tid, DF_ - 1);
#pragma unroll
      for (int q = 0; q < 8; ++q) v[q] = (src + (size_t)q * 256 * TP)[kk];
    }
    uint32_t xd[5];
#pragma unroll
    for (int i = 0; i < 5; ++i) xd[i] = x[row * 320 + lane + 64 * i];
    if (tid < 4) lcnt[tid] = 0;

    f32x2 tw1[4], tw2[7], twq[7], twk[8];
#pragma unroll
    for (int r = 1; r < 5; ++r) tw1[r - 1] = twid((float)(lane * r) * (1.0f / 320.0f));
    const int l0 = lane & 7, rr = lane >> 3;
#pragma unroll
    for (int q = 1; q < 8; ++q) tw2[q - 1] = twid((float)(l0 * q) * (1.0f / 64.0f));
#pragma unroll
    for (int q = 1; q < 8; ++q) twq[q - 1] = twid((float)(s0 * q) * (1.0f / 2048.0f));
#pragma unroll
    for (int p = 0; p < 8; ++p) twk[p] = twid((float)(rr + 5 * l0 + 40 * p) * (1.0f / 640.0f));
    pin(tw1); pin(tw2); pin(twq); pin(twk);
    __builtin_amdgcn_sched_barrier(0);
    STAMP(2, 1);

    if (j < 6) {
#pragma unroll
      for (int q = 1; q < 8; ++q) v[q] = cmulc(v[q], twq[q - 1]);
      fft8<true>(v);
      f32x2 msk;
      msk.x = 1.0f; msk.y = (tid == 0 || tid == 320) ? 0.0f : 1.0f;
#pragma unroll
      for (int jj = 0; jj < 8; ++jj) xx[jj][tid] = v[jj] * msk;
    }
    RAW_BAR();
    asm volatile("" ::: "memory");
    f32x2* E = ex[j];
    if (lane < 40) {
      f32x2* Er = E + rr * 72;
      const f32x2* Xa = xx[j] + rr + 5 * l0;
      const f32x2* Xb = xx[j] + 320 - rr - 5 * l0;
#pragma unroll
      for (int p = 0; p < 8; ++p) {
        const f32x2 a = Xa[40 * p], b = Xb[-40 * p];
        f32x2 e, d;
        e.x = a.x + b.x; e.y = a.y - b.y;
        d.x = a.x - b.x; d.y = a.y + b.y;
        v[p] = cadd_pi(e, cmulc(d, twk[p]));
      }
      fft8<true>(v);
      Er[l0 * 9] = v[0];
#pragma unroll
      for (int a = 1; a < 8; ++a) Er[l0 * 9 + a] = cmulc(v[a], tw2[a - 1]);
      wave_sync();
#pragma unroll
      for (int q = 0; q < 8; ++q) v[q] = Er[q * 9 + l0];
      fft8<true>(v);
      wave_sync();
#pragma unroll
      for (int l1 = 0; l1 < 8; ++l1) Er[l0 + 8 * l1] = v[l1];
    }
    wave_sync();
    f32x2 u[5];
    u[0] = E[lane];
#pragma unroll
    for (int r = 1; r < 5; ++r) u[r] = cmulc(E[r * 72 + lane], tw1[r - 1]);
    fft5<true>(u);

    const f32x2 sc = {1.0f / 2621440.0f, 1.0f / 2621440.0f};
    f32x2 hv[5];
    uint32_t zp[5];
#pragma unroll
    for (int i = 0; i < 5; ++i) {
      const uint32_t hp = bf_pack(bf_unpack(xd[i]) + bf_round(u[i] * sc));
      st_wt32(h + prow * 320 + lane + 64 * i, hp);
      hv[i] = bf_unpack(hp);
    }
    row_layernorm(hv, zp);
#pragma unroll
    for (int i = 0; i < 5; ++i) st_wt32(hn + prow * 320 + lane + 64 * i, zp[i]);
    STAMP(2, 2);
    publish(lcnt, cnt1, lane);
    STAMP(2, 3);
  }
  const int g = lane >> 4, q4 = (lane & 15) >> 2, p4 = lane & 3;
  {
    const int wm = wv >> 1, wn = wv & 1;
    f32x4 acc[2][3];
#pragma unroll
    for (int mi = 0; mi < 2; ++mi)
#pragma unroll
      for (int ni = 0; ni < 3; ++ni) acc[mi][ni] = (f32x4){0.f, 0.f, 0.f, 0.f};
    const int ar = 32 * wm + (lane & 15);
    const int aoff0 = AD_OFF + ar * 128 + (((lane >> 4) ^ (ar & 7)) << 4);
    const int aoff1 = AD_OFF + ar * 128 + (((4 + (lane >> 4)) ^ (ar & 7)) << 4);
    int boff[3];
#pragma unroll
    for (int ni = 0; ni < 3; ++ni) boff[ni] = BD_OFF + (8 * g + q4) * 192 + p4 * 8 + (((3 * wn + ni) ^ (g & 1)) << 5);
    bf16x8 af0[2], bf0[3], af1[2], bf1[3];
    auto rdD = [&](bf16x8 (&af)[2], bf16x8 (&bf)[3], int st, int ks) {
      int sa = (st % 5) * 16384, sb = (st % 6) * 12288;
      asm volatile("" : "+s"(sa), "+s"(sb));
#pragma unroll
      for (int mi = 0; mi < 2; ++mi) af[mi] = *(const bf16x8*)(lds + sa + (ks ? aoff1 : aoff0) + mi * 2048);
#pragma unroll
      for (int ni = 0; ni < 3; ++ni)
        bf[ni] = tr_frag(lds + sb + boff[ni] + ks * 6144, lds + sb + boff[ni] + ks * 6144 + 768);
    };
    auto mmD = [&](const bf16x8 (&af)[2], const bf16x8 (&bf)[3]) {
#pragma unroll
      for (int mi = 0; mi < 2; ++mi)
#pragma unroll
        for (int ni = 0; ni < 3; ++ni)
          acc[mi][ni] = __builtin_amdgcn_mfma_f32_16x16x32_bf16(bf[ni], af[mi], acc[mi][ni], 0, 0, 0);
    };
    RAW_BAR();
    STAMP(2, 4);
    rdD(af0, bf0, 0, 0);
#pragma unroll
    for (int st = 0; st < 10; ++st) {
      rdD(af1, bf1, st, 1);
      mmD(af0, bf0);
      interleave_mfma_ds<6, 8>();
      __builtin_amdgcn_sched_barrier(0);
      RAW_BAR();
      __builtin_amdgcn_sched_barrier(0);
      if (st < 9) rdD(af0, bf0, st + 1, 0);
      mmD(af1, bf1);
      if (st < 9) interleave_mfma_ds<6, 8>();
      __builtin_amdgcn_sched_barrier(0);
    }
    STAMP(2, 5);
#pragma unroll
    for (int ni = 0; ni < 3; ++ni) {
      const u32x2 bb = *(const u32x2*)(lds + B1_OFF + ((3 * wn + ni) * 16 + 4 * (lane >> 4)) * 2);
      const f32x2 b01 = bf_unpack(bb.x), b23 = bf_unpack(bb.y);
      const int NT = cb * 6 + 3 * wn + ni;
      const int ks2 = NT >> 1, gg = 2 * (NT & 1) + (lane >> 5), half = (lane >> 4) & 1;
#pragma unroll
      for (int mi = 0; mi < 2; ++mi) {
        f32x2 a01 = {acc[mi][ni][0], acc[mi][ni][1]}, a23 = {acc[mi][ni][2], acc[mi][ni][3]};
        a01 = bf_unpack(pk_relu_bf16(bf_pack(bf_round(a01) + b01)));
        a23 = bf_unpack(pk_relu_bf16(bf_pack(bf_round(a23) + b23)));
        u32x2 ov;
        ov.x = bf_pack(a01 * a01); ov.y = bf_pack(a23 * a23);
        const int mt = cl * 8 + 2 * wm + mi;
        st_wt64(h1 + ((((mt >> 2) * 48 + ks2) * 4 + (mt & 3)) * 64 + (lane & 15) + 16 * gg) * 2 + half, ov);
      }
    }
    publish(lcnt + 1, cnt2, lane);
    STAMP(2, 6);
  }
  {
    const int wmp = wv & 1, wk = wv >> 1;
    f32x4 acc[2][5];
#pragma unroll
    for (int mi = 0; mi < 2; ++mi)
#pragma unroll
      for (int ni = 0; ni < 5; ++ni) acc[mi][ni] = (f32x4){0.f, 0.f, 0.f, 0.f};
    const int boff = BE_OFF + (16 * (g >> 1) + 4 * (g & 1) + q4 + 32 * wk) * 160 + p4 * 8;
    const __amdgpu_buffer_rsrc_t rs = __builtin_amdgcn_make_buffer_rsrc((void*)h1, 0, S_ * F_ * 2, 0x00020000);
    const int avoff = ((rb2 * 48 + wk) * 4 + 2 * wmp) * 1024 + lane * 16;
    bf16x8 af[12][2], bfr[2][5];
    auto ldA = [&](int st) {
      af[st][0] = __builtin_bit_cast(bf16x8, __builtin_amdgcn_raw_buffer_load_b128(rs, avoff, st * 16384, 16));
      af[st][1] = __builtin_bit_cast(bf16x8, __builtin_amdgcn_raw_buffer_load_b128(rs, avoff + 1024, st * 16384, 16));
    };
    auto rdE = [&](bf16x8 (&bf)[5], int st) {
      int sb = (st % NBE) * 20480;
      asm volatile("" : "+s"(sb));
#pragma unroll
      for (int ni = 0; ni < 5; ++ni) bf[ni] = tr_frag(lds + sb + boff + ni * 32, lds + sb + boff + ni * 32 + 1280);
    };
    constexpr int APF = 4;
    lds_spin(lcnt + 3, 1u);
#pragma unroll
    for (int i = 0; i < APF; ++i) ldA(i);
    RAW_BAR();
    STAMP(2, 7);
    rdE(bfr[0], 0);
#pragma unroll
    for (int st = 0; st < 12; ++st) {
      if (st < 11) {
        RAW_BAR();
        __builtin_amdgcn_sched_barrier(0);
        rdE(bfr[(st + 1) & 1], st + 1);
      }
#pragma unroll
      for (int ni = 0; ni < 5; ++ni)
#pragma unroll
        for (int mi = 0; mi < 2; ++mi)
          acc[mi][ni] = __builtin_amdgcn_mfma_f32_16x16x32_bf16(bfr[st & 1][ni], af[st][mi], acc[mi][ni], 0, 0, 0);
      if (st < 11) interleave_mfma_ds<10, 10>();
      __builtin_amdgcn_sched_barrier(0);
      if (st + APF < 12) ldA(st + APF);
    }
    STAMP(2, 8);
    f32x4* red = (f32x4*)(lds + BE_OFF) + (wmp * 40) * 64 + lane;
#pragma unroll
    for (int t = 0; t < 10; ++t)
      if ((t & 3) != wk) red[(t * 4 + wk) * 64] = acc[t / 5][t % 5];
    RAW_BAR();
    asm volatile("" ::: "memory");
#pragma unroll
    for (int t = 0; t < 10; ++t) {
      if ((t & 3) == wk) {
        const int mi = t / 5, ni = t % 5;
        f32x4 o = acc[mi][ni];
#pragma unroll
        for (int w2_ = 0; w2_ < 4; ++w2_)
          if (w2_ != (t & 3)) o += red[(t * 4 + w2_) * 64];
        const int lr = 32 * wmp + 16 * mi + (lane & 15);
        const int prow = rb2 * 64 + lr;
        const int mrow = (prow >> 3) + 256 * (prow & 7);
        const int nc = 16 * ni + 4 * (lane >> 4);
        const u32x2 bb = *(const u32x2*)(lds + B2_OFF + nc * 2);
        const u32x2 rsd = *(const u32x2*)(lds + RES_OFF + lr * 160 + nc * 2);
        f32x2 a01 = {o[0], o[1]}, a23 = {o[2], o[3]};
        u32x2 ov;
        ov.x = bf_pack(bf_unpack(rsd.x) + bf_round(bf_round(a01) + bf_unpack(bb.x)));
        ov.y = bf_pack(bf_unpack(rsd.y) + bf_round(bf_round(a23) + bf_unpack(bb.y)));
        *(u32x2*)(out + (size_t)mrow * D_ + cb2 * 80 + nc) = ov;
      }
    }
  }
  STAMP_END(2, 9);
}

__global__ void k_zz_end() {}
extern "C" void kernel_launch(void* const* d_in, const int* in_sizes, int n_in, void* d_out, int out_size,
                              void* d_ws, size_t ws_size, hipStream_t stream) {
  const uint32_t* x = (const uint32_t*)d_in[0];
  const uint16_t* wr = (const uint16_t*)d_in[1];
  const uint16_t* wi = (const uint16_t*)d_in[2];
  const uint16_t* w1 = (const uint16_t*)d_in[4];
  const uint16_t* b1 = (const uint16_t*)d_in[5];
  const uint16_t* w2 = (const uint16_t*)d_in[6];
  const uint16_t* b2 = (const uint16_t*)d_in[7];
  char* ws = (char*)d_ws;
  size_t off = 0;
  f32x2* T = (f32x2*)(ws + off); off += (size_t)8 * 256 * TP * 8;
  uint32_t* h = (uint32_t*)(ws + off); off += (size_t)S_ * D_ * 2;
  uint32_t* hn = (uint32_t*)(ws + off); off += (size_t)S_ * D_ * 2;
  u32x4* h1 = (u32x4*)(ws + off); off += (size_t)S_ * F_ * 2;
  u32x4* w2p = (u32x4*)(ws + off); off += (size_t)D_ * F_ * 2;

  uint32_t* cnt = (uint32_t*)(ws + off); off += 32 * 256;
  k_fwd_rows<<<256, 1024, 0, stream>>>(x, T, wr, wi, (const uint32_t*)w1, (const u32x4*)w2, w2p, cnt);
  k_cols<<<328, 256, 0, stream>>>(T, wr, wi);
  k_tail<<<256, 1024, 0, stream>>>(T, x, h, hn, (u32x2*)h1, (const char*)w1, b1, w2p, b2, (uint16_t*)d_out, cnt);
}
```

```cpp
#include <hip/hip_runtime.h>
#include <hip/hip_bf16.h>
#include <stdint.h>

typedef float f32x2 __attribute__((ext_vector_type(2)));
typedef float f32x4 __attribute__((ext_vector_type(4)));
typedef short bf16x8 __attribute__((ext_vector_type(8)));
typedef unsigned int u32x4 __attribute__((ext_vector_type(4)));
typedef unsigned int u32x2 __attribute__((ext_vector_type(2)));

#define DEV __device__ __forceinline__

#define STAMP(kern, i)
#define STAMP_END(kern, i)
#define STAMP_TID 0

static constexpr int S_ = 2048, D_ = 640, F_ = 1536, DF_ = 321;
static constexpr int TP = 328;
static constexpr int NEARLY = 4;

DEV float bf_lo(uint32_t p) { return __builtin_bit_cast(float, p << 16); }
DEV float bf_hi(uint32_t p) { return __builtin_bit_cast(float, p & 0xffff0000u); }
DEV f32x2 bf_unpack(uint32_t p) { f32x2 r; r.x = bf_lo(p); r.y = bf_hi(p); return r; }
typedef __bf16 bf16x2_t __attribute__((ext_vector_type(2)));
DEV uint32_t bf_pack(f32x2 v) {
  return __builtin_bit_cast(uint32_t, __builtin_convertvector(v, bf16x2_t));
}
DEV f32x2 bf_round(f32x2 v) { return bf_unpack(bf_pack(v)); }
DEV float bfr(float f) { f32x2 v; v.x = f; v.y = f; return bf_lo(bf_pack(v)); }

DEV f32x2 cadd_mi(f32x2 b, f32x2 d) {
  f32x2 r;
  asm("v_pk_add_f32 %0, %1, %2 op_sel:[0,1] op_sel_hi:[1,0] neg_hi:[0,1]" : "=v"(r) : "v"(b), "v"(d));
  return r;
}
DEV f32x2 cadd_pi(f32x2 b, f32x2 d) {
  f32x2 r;
  asm("v_pk_add_f32 %0, %1, %2 op_sel:[0,1] op_sel_hi:[1,0] neg_lo:[0,1]" : "=v"(r) : "v"(b), "v"(d));
  return r;
}
DEV f32x2 cnadd_mi(f32x2 b, f32x2 d) {
  f32x2 r;
  asm("v_pk_add_f32 %0, %1, %2 op_sel:[0,1] op_sel_hi:[1,0] neg_lo:[1,0] neg_hi:[1,1]" : "=v"(r) : "v"(b), "v"(d));
  return r;
}
DEV f32x2 cnadd_pi(f32x2 b, f32x2 d) {
  f32x2 r;
  asm("v_pk_add_f32 %0, %1, %2 op_sel:[0,1] op_sel_hi:[1,0] neg_lo:[1,1] neg_hi:[1,0]" : "=v"(r) : "v"(b), "v"(d));
  return r;
}
DEV f32x2 cfma_mi(f32x2 s, f32x2 d, f32x2 b) {
  f32x2 r;
  asm("v_pk_fma_f32 %0, %1, %2, %3 op_sel:[0,1,0] op_sel_hi:[1,0,1] neg_hi:[0,1,0]" : "=v"(r) : "v"(s), "v"(d), "v"(b));
  return r;
}
DEV f32x2 cfma_pi(f32x2 s, f32x2 d, f32x2 b) {
  f32x2 r;
  asm("v_pk_fma_f32 %0, %1, %2, %3 op_sel:[0,1,0] op_sel_hi:[1,0,1] neg_lo:[0,1,0]" : "=v"(r) : "v"(s), "v"(d), "v"(b));
  return r;
}
DEV f32x2 pfma(f32x2 a, f32x2 b, f32x2 c) { return __builtin_elementwise_fma(a, b, c); }
DEV f32x2 cmul(f32x2 a, f32x2 w) {
  f32x2 t, r;
  asm("v_pk_mul_f32 %0, %1, %2 op_sel:[0,0] op_sel_hi:[0,1]" : "=v"(t) : "v"(a), "v"(w));
  asm("v_pk_fma_f32 %0, %1, %2, %3 op_sel:[1,1,0] op_sel_hi:[1,0,1] neg_lo:[0,1,0]" : "=v"(r) : "v"(a), "v"(w), "v"(t));
  return r;
}
DEV f32x2 cmulc(f32x2 a, f32x2 w) {
  f32x2 t, r;
  asm("v_pk_mul_f32 %0, %1, %2 op_sel:[0,0] op_sel_hi:[0,1] neg_hi:[0,1]" : "=v"(t) : "v"(a), "v"(w));
  asm("v_pk_fma_f32 %0, %1, %2, %3 op_sel:[1,1,0] op_sel_hi:[1,0,1]" : "=v"(r) : "v"(a), "v"(w), "v"(t));
  return r;
}
DEV f32x2 twid(float f) {
  f32x2 r;
  r.x = __builtin_amdgcn_cosf(f);
  r.y = __builtin_amdgcn_sinf(-f);
  return r;
}
DEV void st_wt(f32x2* p, f32x2 v) {
  __hip_atomic_store((unsigned long long*)p, __builtin_bit_cast(unsigned long long, v), __ATOMIC_RELAXED,
                     __HIP_MEMORY_SCOPE_AGENT);
}
DEV void st_wt32(uint32_t* p, uint32_t v) { __hip_atomic_store(p, v, __ATOMIC_RELAXED, __HIP_MEMORY_SCOPE_AGENT); }
DEV void st_wt64(u32x2* p, u32x2 v) {
  __hip_atomic_store((unsigned long long*)p, __builtin_bit_cast(unsigned long long, v), __ATOMIC_RELAXED,
                     __HIP_MEMORY_SCOPE_AGENT);
}
DEV void st_wt128(void* p, u32x4 v) {
  asm volatile("global_store_dwordx4 %0, %1, off sc1\n\ts_nop 1" ::"v"(p), "v"(v) : "memory");
}
template <int N> DEV void pin(f32x2 (&a)[N]) {
#pragma unroll
  for (int i = 0; i < N; ++i) asm volatile("" : "+v"(a[i]));
}
template <bool INV> DEV f32x2 add_rot(f32x2 b, f32x2 d) { return INV ? cadd_pi(b, d) : cadd_mi(b, d); }
template <bool INV> DEV f32x2 sub_rot(f32x2 b, f32x2 d) { return INV ? cadd_mi(b, d) : cadd_pi(b, d); }

template <bool INV> DEV void fft4(f32x2& a0, f32x2& a1, f32x2& a2, f32x2& a3) {
  f32x2 b0 = a0 + a2, b1 = a0 - a2, b2 = a1 + a3, d = a1 - a3;
  a0 = b0 + b2; a2 = b0 - b2;
  a1 = add_rot<INV>(b1, d); a3 = sub_rot<INV>(b1, d);
}
template <bool INV> DEV void fft8(f32x2 (&v)[8]) {
  const f32x2 r = {0.70710678118654752440f, 0.70710678118654752440f};
  const f32x2 nr = {-0.70710678118654752440f, -0.70710678118654752440f};
  f32x2 e0 = v[0] + v[4], e1 = v[1] + v[5], e2 = v[2] + v[6], e3 = v[3] + v[7];
  f32x2 o0 = v[0] - v[4], o1 = v[1] - v[5], o2 = v[2] - v[6], o3 = v[3] - v[7];
  fft4<INV>(e0, e1, e2, e3);
  f32x2 p1 = INV ? cadd_pi(o1, o1) : cadd_mi(o1, o1);
  f32x2 p3 = INV ? cnadd_pi(o3, o3) : cnadd_mi(o3, o3);
  f32x2 s = p1 + p3, d = p1 - p3;
  f32x2 b0 = add_rot<INV>(o0, o2), b1 = sub_rot<INV>(o0, o2);
  v[0] = e0; v[2] = e1; v[4] = e2; v[6] = e3;
  v[1] = pfma(r, s, b0); v[5] = pfma(nr, s, b0);
  v[3] = INV ? cfma_pi(r, d, b1) : cfma_mi(r, d, b1);
  v[7] = INV ? cfma_mi(r, d, b1) : cfma_pi(r, d, b1);
}
template <bool INV> DEV void fft5(f32x2 (&v)[5]) {
  const f32x2 c1 = {0.30901699437494742410f, 0.30901699437494742410f};
  const f32x2 c2 = {-0.80901699437494742410f, -0.80901699437494742410f};
  const f32x2 s1 = {0.95105651629515357212f, 0.95105651629515357212f};
  const f32x2 s2 = {0.58778525229247312917f, 0.58778525229247312917f};
  const f32x2 ns1 = {-0.95105651629515357212f, -0.95105651629515357212f};
  f32x2 t1 = v[1] + v[4], t2 = v[2] + v[3], t3 = v[1] - v[4], t4 = v[2] - v[3];
  f32x2 a1 = pfma(c2, t2, pfma(c1, t1, v[0]));
  f32x2 a2 = pfma(c1, t2, pfma(c2, t1, v[0]));
  f32x2 b1 = pfma(s2, t4, s1 * t3);
  f32x2 b2 = pfma(ns1, t4, s2 * t3);
  v[0] = v[0] + t1 + t2;
  v[1] = add_rot<INV>(a1, b1); v[4] = sub_rot<INV>(a1, b1);
  v[2] = add_rot<INV>(a2, b2); v[3] = sub_rot<INV>(a2, b2);
}

template <int CTRL> DEV float dppmov(float v) {
  return __builtin_bit_cast(float, __builtin_amdgcn_update_dpp(0, __builtin_bit_cast(int, v), CTRL, 0xf, 0xf, false));
}
DEV float wave_sum(float v) {
  v += dppmov<0xB1>(v);
  v += dppmov<0x4E>(v);
  v += dppmov<0x141>(v);
  v += dppmov<0x140>(v);
  int iv = __builtin_bit_cast(int, v);
  float a = __builtin_bit_cast(float, __builtin_amdgcn_readlane(iv, 0));
  float b = __builtin_bit_cast(float, __builtin_amdgcn_readlane(iv, 16));
  float c = __builtin_bit_cast(float, __builtin_amdgcn_readlane(iv, 32));
  float d = __builtin_bit_cast(float, __builtin_amdgcn_readlane(iv, 48));
  return (a + b) + (c + d);
}
DEV void wave_sync() {
  __builtin_amdgcn_fence(__ATOMIC_RELEASE, "wavefront");
  __builtin_amdgcn_wave_barrier();
  __builtin_amdgcn_fence(__ATOMIC_ACQUIRE, "wavefront");
}

DEV void row_layernorm(const f32x2 (&xv)[5], uint32_t (&zp)[5]) {
  f32x2 s2 = (xv[0] + xv[1]) + (xv[2] + xv[3]) + xv[4];
  const float mean = wave_sum(s2.x + s2.y) * (1.0f / 640.0f);
  const f32x2 nm = {-mean, -mean};
  f32x2 q2 = {0.f, 0.f};
#pragma unroll
  for (int i = 0; i < 5; ++i) { f32x2 d = xv[i] + nm; q2 = pfma(d, d, q2); }
  const float var = wave_sum(q2.x + q2.y) * (1.0f / 640.0f);
  f32x2 mv; mv.x = mean; mv.y = var;
  mv = bf_round(mv);
  const float den = bfr(sqrtf(bfr(mv.y + 1e-5f)));
  const float rden = 1.0f / den;
  const f32x2 nmb = {-mv.x, -mv.x}, rd = {rden, rden};
#pragma unroll
  for (int i = 0; i < 5; ++i) zp[i] = bf_pack(bf_round(xv[i] + nmb) * rd);
}

__global__ __launch_bounds__(1024) void k_fwd_rows(const uint32_t* __restrict__ x, f32x2* __restrict__ T,
                                                  const uint16_t* __restrict__ wr, const uint16_t* __restrict__ wi,
                                                  const uint32_t* __restrict__ w1, const u32x4* __restrict__ w2,
                                                  u32x4* __restrict__ w2p, uint32_t* __restrict__ cnt) {
  __shared__ f32x2 ex[8][360];
  __shared__ f32x2 zz[8][320];
  if (threadIdx.x >= 512) {
    const int tid = threadIdx.x - 512, lane = tid & 63, j = tid >> 6;
  const int wid = min((int)blockIdx.x * 512 + tid, 122879);
  const u32x4 wchunk = w2[wid];

  uint32_t pf = 0;
  {
    const char* pa = nullptr;
    if (lane < 12) {
      const int t = (blockIdx.x & 7) + 8 * (8 * (blockIdx.x >> 3) + j);
      pa = (const char*)(((lane < 6) ? wr : wi) + (t * DF_ + min(64 * (lane % 6), DF_ - 2)));
      pa = (const char*)((uintptr_t)pa & ~(uintptr_t)3);
    } else if (lane >= 56) {
      pa = (const char*)(w1 + (blockIdx.x * 60 + min(j * 8 + (lane - 56), 59)) * 32);
    }
    else if (lane < 18) {
      unsigned long long c0, c1;
      asm volatile("s_getpc_b64 s[40:41]\n\ts_add_u32 s40, s40, _Z6k_colsPDv2_fPKtS2_@rel32@lo+4\n\ts_addc_u32 s41, s41, _Z6k_colsPDv2_fPKtS2_@rel32@hi+12\n\ts_mov_b64 %0, s[40:41]" : "=s"(c0) :: "s40", "s41", "scc");
      asm volatile("s_getpc_b64 s[40:41]\n\ts_add_u32 s40, s40, _Z8k_zz_endv@rel32@lo+4\n\ts_addc_u32 s41, s41, _Z8k_zz_endv@rel32@hi+12\n\ts_mov_b64 %0, s[40:41]" : "=s"(c1) :: "s40", "s41", "scc");
      const long long len = (long long)(c1 - c0);
      const int li = ((blockIdx.x >> 3) * 8 + j) * 2 + (lane - 16);
      if (lane >= 16 && len > 0 && len < 65536 && li * 128 < len) pa = (const char*)(c0 + li * 128);
    }
    if (pa) pf = *(const uint32_t*)pa;
  }

  if (blockIdx.x < 240) {
    const int wk_ = wid / 80, wc_ = wid % 80;
    const int rho = (wk_ & ~12) | ((wk_ & 4) << 1) | ((wk_ & 8) >> 1);
    st_wt128(w2p + ((wc_ / 10) * 1536 + rho) * 10 + (wc_ % 10), wchunk);
  }
  asm volatile("" ::"v"(pf));
    return;
  }
  const int tid = threadIdx.x, lane = tid & 63, j = tid >> 6;
  STAMP(0, 0);
  if (blockIdx.x == 0 && tid < 64) st_wt32(cnt + tid * 32, 0u);
  const int s0 = 32 * (blockIdx.x & 7) + (blockIdx.x >> 3);
  const int row = s0 + 256 * j;
  uint32_t xd[5];
#pragma unroll
  for (int i = 0; i < 5; ++i) xd[i] = x[row * 320 + lane + 64 * i];
  f32x2 tw1[4], tw2[7], twq[7];
#pragma unroll
  for (int r = 1; r < 5; ++r) tw1[r - 1] = twid((float)(lane * r) * (1.0f / 320.0f));
  const int l0 = lane & 7, rr = lane >> 3;
#pragma unroll
  for (int q = 1; q < 8; ++q) tw2[q - 1] = twid((float)(l0 * q) * (1.0f / 64.0f));
#pragma unroll
  for (int q = 1; q < 8; ++q) twq[q - 1] = twid((float)(s0 * q) * (1.0f / 2048.0f));
  f32x2 twk_[1] = {twid((float)tid * (1.0f / 640.0f))};
  pin(tw1); pin(tw2); pin(twq); pin(twk_);
  const f32x2 twk = twk_[0];
  __builtin_amdgcn_sched_barrier(0);
  STAMP(0, 1);

  f32x2 u[5];
  {
    f32x2 xv[5];
    uint32_t zp[5];
#pragma unroll
    for (int i = 0; i < 5; ++i) xv[i] = bf_unpack(xd[i]);
    row_layernorm(xv, zp);
#pragma unroll
    for (int i = 0; i < 5; ++i) u[i] = bf_unpack(zp[i]);
  }
  STAMP(0, 2);
  f32x2* E = ex[j];
  fft5<false>(u);
  E[lane] = u[0];
#pragma unroll
  for (int r = 1; r < 5; ++r) E[r * 72 + lane] = cmul(u[r], tw1[r - 1]);
  wave_sync();
  if (lane < 40) {
    f32x2 v[8];
    f32x2* Er = E + rr * 72;
#pragma unroll
    for (int l1 = 0; l1 < 8; ++l1) v[l1] = Er[l0 + 8 * l1];
    fft8<false>(v);
    wave_sync();
    Er[l0] = v[0];
#pragma unroll
    for (int q = 1; q < 8; ++q) Er[q * 9 + l0] = cmul(v[q], tw2[q - 1]);
    wave_sync();
#pragma unroll
    for (int a = 0; a < 8; ++a) v[a] = Er[l0 * 9 + a];
    fft8<false>(v);
    f32x2* Z = zz[j] + rr + 5 * l0;
#pragma unroll
    for (int p = 0; p < 8; ++p) Z[40 * p] = v[p];
  }
  STAMP(0, 3);
  __syncthreads();
  STAMP(0, 4);
  if (j < 6) {
    const int k = tid;
    const int ka = (k >= 320) ? 0 : k, kb = (k == 0 || k >= 320) ? 0 : 320 - k;
    f32x2 v[8];
#pragma unroll
    for (int jj = 0; jj < 8; ++jj) {
      const f32x2 a = zz[jj][ka], b = zz[jj][kb];
      f32x2 e, d;
      e.x = a.x + b.x; e.y = a.y - b.y;
      d.x = a.x - b.x; d.y = a.y + b.y;
      v[jj] = cadd_mi(e, cmul(d, twk));
    }
    fft8<false>(v);
    if (k < DF_) {
      f32x2* dst = T + (size_t)s0 * TP;
      st_wt(dst + k, v[0]);
#pragma unroll
      for (int q = 1; q < 8; ++q) st_wt(dst + (size_t)q * 256 * TP + k, cmul(v[q], twq[q - 1]));
    }
  }
  STAMP(0, 5);
  STAMP_END(0, 6);
}

__global__ __launch_bounds__(256) void k_cols(f32x2* __restrict__ T, const uint16_t* __restrict__ wr,
                                              const uint16_t* __restrict__ wi) {
  __shared__ f32x2 e1[2048];
  __shared__ f32x2 e2[4][576];
  __shared__ __attribute__((aligned(16))) uint32_t wl[2048];
  const int tid = threadIdx.x, lane = tid & 63, wv = tid >> 6;
  STAMP(1, 0);
  const int q = blockIdx.x & 7, kg = blockIdx.x >> 3;
  const int c = tid & 7, a = tid >> 3;
  const int k = kg * 8 + c;
  const bool valid = k < DF_;
  f32x2* colbase = T + (size_t)(q * 256) * TP;
  const uint32_t coff = a * TP + (valid ? k : DF_ - 1);
  f32x2 v[8];
#pragma unroll
  for (int j1 = 0; j1 < 8; ++j1) v[j1] = (colbase + (size_t)j1 * 32 * TP)[coff];
  const int kb = min(8 * kg, DF_ - 8);
  u32x4 wre, wim;
  __builtin_memcpy(&wre, wr + (size_t)(q + 8 * tid) * DF_ + kb, 16);
  __builtin_memcpy(&wim, wi + (size_t)(q + 8 * tid) * DF_ + kb, 16);

  const int b = (lane >> 3) & 3, p1lo = lane >> 5, p1 = 2 * wv + p1lo;
  f32x2 twa[7], twb[7];
#pragma unroll
  for (int p = 1; p < 8; ++p) twa[p - 1] = twid((float)(a * p) * (1.0f / 256.0f));
#pragma unroll
  for (int p = 1; p < 8; ++p) twb[p - 1] = twid((float)(b * p) * (1.0f / 32.0f));
  pin(twa); pin(twb);
  __builtin_amdgcn_sched_barrier(0);
  STAMP(1, 1);

  fft8<false>(v);
  e1[tid] = v[0];
#pragma unroll
  for (int p = 1; p < 8; ++p) e1[p * 256 + tid] = cmul(v[p], twa[p - 1]);
  {
    u32x4 o0, o1;
    o0.x = __builtin_amdgcn_perm(wim.x, wre.x, 0x05040100); o0.y = __builtin_amdgcn_perm(wim.x, wre.x, 0x07060302);
    o0.z = __builtin_amdgcn_perm(wim.y, wre.y, 0x05040100); o0.w = __builtin_amdgcn_perm(wim.y, wre.y, 0x07060302);
    o1.x = __builtin_amdgcn_perm(wim.z, wre.z, 0x05040100); o1.y = __builtin_amdgcn_perm(wim.z, wre.z, 0x07060302);
    o1.z = __builtin_amdgcn_perm(wim.w, wre.w, 0x05040100); o1.w = __builtin_amdgcn_perm(wim.w, wre.w, 0x07060302);
    *(u32x4*)(wl + tid * 8) = o0;
    *(u32x4*)(wl + tid * 8 + 4) = o1;
  }
  STAMP(1, 2);
  __syncthreads();
  STAMP(1, 3);
  uint32_t wf[8];
  {
    const uint32_t* wlp = wl + (p1 + 16 * b) * 8 + min(8 * kg + c - kb, 7);
#pragma unroll
    for (int e = 0; e < 2; ++e)
#pragma unroll
      for (int mm = 0; mm < 4; ++mm) wf[e * 4 + mm] = wlp[(8 * e + 64 * mm) * 8];
  }
  f32x2* E1 = e1 + p1 * 256 + (lane & 31);
#pragma unroll
  for (int j2 = 0; j2 < 8; ++j2) v[j2] = E1[32 * j2];
  fft8<false>(v);
  f32x2* E2w = e2[wv] + p1lo * 288 + (lane & 31);
  E2w[0] = v[0];
#pragma unroll
  for (int p = 1; p < 8; ++p) E2w[p * 36] = cmul(v[p], twb[p - 1]);
  wave_sync();
  f32x2* E2r = e2[wv] + p1lo * 288 + (2 * b) * 36 + c;
#pragma unroll
  for (int e = 0; e < 2; ++e)
#pragma unroll
    for (int bb = 0; bb < 4; ++bb) v[e * 4 + bb] = E2r[e * 36 + bb * 8];
  fft4<false>(v[0], v[1], v[2], v[3]);
  fft4<false>(v[4], v[5], v[6], v[7]);
  STAMP(1, 4);
#pragma unroll
  for (int i = 0; i < 8; ++i) v[i] = cmul(v[i], bf_unpack(wf[i]));
  fft4<true>(v[0], v[1], v[2], v[3]);
  fft4<true>(v[4], v[5], v[6], v[7]);
  STAMP(1, 5);
#pragma unroll
  for (int e = 0; e < 2; ++e)
#pragma unroll
    for (int bb = 0; bb < 4; ++bb) E2r[e * 36 + bb * 8] = v[e * 4 + bb];
  wave_sync();
  v[0] = E2w[0];
#pragma unroll
  for (int p = 1; p < 8; ++p) v[p] = cmulc(E2w[p * 36], twb[p - 1]);
  fft8<true>(v);
#pragma unroll
  for (int j2 = 0; j2 < 8; ++j2) E1[32 * j2] = v[j2];
  STAMP(1, 6);
  __syncthreads();
  STAMP(1, 7);
  v[0] = e1[tid];
#pragma unroll
  for (int p = 1; p < 8; ++p) v[p] = cmulc(e1[p * 256 + tid], twa[p - 1]);
  fft8<true>(v);
  if (valid) {
#pragma unroll
    for (int j1 = 0; j1 < 8; ++j1) st_wt(colbase + (size_t)j1 * 32 * TP + coff, v[j1]);
  }
  STAMP(1, 8);
  STAMP_END(1, 9);
}

template <int N> DEV void wait_vm() { asm volatile("s_waitcnt vmcnt(%0)" ::"n"(N) : "memory"); }
#define RAW_BAR() do { asm volatile("s_waitcnt lgkmcnt(0)" ::: "memory"); __builtin_amdgcn_s_barrier(); } while (0)

DEV void glds16(const void* src, char* lds) {
  __builtin_amdgcn_global_load_lds((const unsigned*)src, (unsigned*)lds, 16, 0, 0);
}
DEV uint32_t pk_relu_bf16(uint32_t p) {
  uint32_t r;
  asm("v_pk_max_i16 %0, %1, 0" : "=v"(r) : "v"(p));
  return r;
}

typedef short v4s_t __attribute__((ext_vector_type(4)));
typedef __attribute__((address_space(3))) v4s_t* lds_v4s_ptr;
DEV bf16x8 tr_frag(const char* p0, const char* p1) {
  const v4s_t a = __builtin_amdgcn_ds_read_tr16_b64_v4i16((lds_v4s_ptr)p0);
  const v4s_t b = __builtin_amdgcn_ds_read_tr16_b64_v4i16((lds_v4s_ptr)p1);
  return __builtin_shufflevector(a, b, 0, 1, 2, 3, 4, 5, 6, 7);
}

#define SGB(mask, n) __builtin_amdgcn_sched_group_barrier(mask, n, 0)
template <int NM, int ND> DEV void interleave_mfma_ds() {
  if constexpr (NM == 6) {
    SGB(0x008, 1); SGB(0x100, 2); SGB(0x008, 1); SGB(0x100, 2); SGB(0x008, 1); SGB(0x100, 2);
    SGB(0x008, 1); SGB(0x100, 2); SGB(0x008, 2);
  } else if constexpr (NM == 10) {
    SGB(0x008, 1); SGB(0x100, 2); SGB(0x008, 1); SGB(0x100, 2); SGB(0x008, 1); SGB(0x100, 2);
    SGB(0x008, 1); SGB(0x100, 2); SGB(0x008, 1); SGB(0x100, 2); SGB(0x008, 5);
  } else {
    SGB(0x008, 1); SGB(0x100, 2); SGB(0x008, 1); SGB(0x100, 2); SGB(0x008, 1); SGB(0x100, 2);
    SGB(0x008, 1); SGB(0x100, 2); SGB(0x008, 1); SGB(0x100, 2);
  }
}
DEV void wait_vm_rt(int n) {
  switch (n) {
    case 0: wait_vm<0>(); break;   case 1: wait_vm<1>(); break;   case 2: wait_vm<2>(); break;
    case 3: wait_vm<3>(); break;   case 4: wait_vm<4>(); break;   case 5: wait_vm<5>(); break;
    case 6: wait_vm<6>(); break;   case 7: wait_vm<7>(); break;   case 8: wait_vm<8>(); break;
    case 9: wait_vm<9>(); break;   case 10: wait_vm<10>(); break; case 11: wait_vm<11>(); break;
    case 12: wait_vm<12>(); break; case 13: wait_vm<13>(); break; case 14: wait_vm<14>(); break;
    default: wait_vm<0>(); break;
  }
}
DEV void glds16_dev(const void* src, char* lds) {
  __builtin_amdgcn_global_load_lds((const unsigned*)src, (unsigned*)lds, 16, 0, 16);
}
DEV void lds_spin(uint32_t* w, uint32_t want) {
  for (int i = 0; i < (1 << 24); ++i) {
    const uint32_t v = __hip_atomic_load(w, __ATOMIC_RELAXED, __HIP_MEMORY_SCOPE_WORKGROUP);
    if ((uint32_t)__builtin_amdgcn_readfirstlane(v) >= want) break;
    __builtin_amdgcn_s_sleep(1);
  }
  asm volatile("" ::: "memory");
}
DEV void wait_cluster(uint32_t* lcnt, uint32_t* lflag, uint32_t* cnt, int lw, int lane) {
  if (lw == 0) {
    lds_spin(lcnt, 8u);
    for (int i = 0; i < (1 << 22); ++i) {
      uint32_t v = 0;
      if (lane == 0) {
        const uint32_t zero = 0;
        asm volatile("global_atomic_add %0, %1, %2, off sc0\n\ts_waitcnt vmcnt(0)" : "=&v"(v) : "v"(cnt), "v"(zero) : "memory");
      }
      if ((uint32_t)__builtin_amdgcn_readfirstlane(v) >= 16u) break;
      __builtin_amdgcn_s_sleep(1);
    }
    asm volatile("" ::: "memory");
    if (lane == 0) __hip_atomic_store(lflag, 1u, __ATOMIC_RELAXED, __HIP_MEMORY_SCOPE_WORKGROUP);
  } else {
    lds_spin(lflag, 1u);
  }
}
DEV void publish(uint32_t* lcnt, uint32_t* gcnt, int lane) {
  asm volatile("s_waitcnt vmcnt(0)" ::: "memory");
  if (lane == 0) {
    const uint32_t old = __hip_atomic_fetch_add(lcnt, 1u, __ATOMIC_RELAXED, __HIP_MEMORY_SCOPE_WORKGROUP);
    if (old == 7u) {
      __hip_atomic_fetch_add(gcnt, 1u, __ATOMIC_RELAXED, __HIP_MEMORY_SCOPE_AGENT);
    }
  }
}

#undef STAMP_TID
#define STAMP_TID 0
__global__ __launch_bounds__(1024) void k_tail(const f32x2* __restrict__ T, const uint32_t* __restrict__ x,
                                               uint32_t* h, uint32_t* hn, u32x2* h1,
                                               const char* __restrict__ w1, const uint16_t* __restrict__ b1,
                                               const u32x4* __restrict__ w2p, const uint16_t* __restrict__ b2,
                                               uint16_t* __restrict__ out, uint32_t* cnt) {
  constexpr int BD_OFF = 0, AD_OFF = 6 * 12288;
  constexpr int BE_OFF = 0, NBE = 7, RES_OFF = NBE * 20480;
  constexpr int MISC_OFF = 163840 - 1024, B1_OFF = MISC_OFF, LC_OFF = MISC_OFF + 256, B2_OFF = MISC_OFF + 512;
  __shared__ __attribute__((aligned(16))) char lds[163840];
  const int tid = threadIdx.x, lane = tid & 63;
  const int wv = __builtin_amdgcn_readfirstlane(tid >> 6);
  const int xcd = blockIdx.x & 7, idx = blockIdx.x >> 3;
  const int cl = 2 * xcd + (idx >> 4), mi_ = idx & 15;
  const int cb = mi_, rb2 = 2 * cl + (mi_ >> 3), cb2 = mi_ & 7;
  uint32_t* cnt1 = cnt + cl * 64;
  uint32_t* cnt2 = cnt + (16 + cl) * 64;
  uint32_t* lcnt = (uint32_t*)(lds + LC_OFF);

  if (wv >= 8) {
    const int lw = wv - 8;
    const int nb = (lw < 4) ? 2 : 1, nb2 = (lw < 4) ? 3 : 2;
    const int arow = lane >> 3;
    const char* asrc = (const char*)hn + (size_t)(cl * 128 + 16 * lw + arow) * 1280 + (((lane & 7) ^ arow) << 4);
    const int id0 = lw * 64 + lane, r0 = id0 / 12, c0 = (id0 % 12) ^ (((r0 >> 3) & 1) << 1);
    const int id1 = (8 + lw) * 64 + lane, r1 = id1 / 12, c1 = (id1 % 12) ^ (((r1 >> 3) & 1) << 1);
    const char* bsrc0 = w1 + (size_t)r0 * 3072 + cb * 192 + c0 * 16;
    const char* bsrc1 = w1 + (size_t)r1 * 3072 + cb * 192 + c1 * 16;
    auto stageB = [&](int st) {
      char* dst = lds + BD_OFF + (st % 6) * 12288 + lane * 16;
      glds16(bsrc0 + (size_t)st * 64 * 3072, dst + lw * 1024);
      if (lw < 4) glds16(bsrc1 + (size_t)st * 64 * 3072, dst + (8 + lw) * 1024);
    };
    auto stageA = [&](int st) {
      char* dst = lds + AD_OFF + (st % 5) * 16384 + lane * 16;
      glds16_dev(asrc + st * 128, dst + (2 * lw) * 1024);
      glds16_dev(asrc + st * 128 + 8 * 1280, dst + (2 * lw + 1) * 1024);
    };
    if (lw == 7 && lane < 12) glds16((const u32x4*)(b1 + cb * 96) + lane, lds + B1_OFF + lane * 16);
    if (lw == 6 && lane < 10) glds16((const u32x4*)(b2 + cb2 * 80) + lane, lds + B2_OFF + lane * 16);
    __builtin_amdgcn_s_barrier();
#pragma unroll
    for (int st = 0; st < 6; ++st) stageB(st);
    wait_cluster(lcnt, lcnt + 2, cnt1, lw, lane);
#pragma unroll
    for (int st = 0; st < 5; ++st) stageA(st);
#pragma unroll
    for (int st = 0; st < 10; ++st) {
      const int ka = (st == 0) ? 8 : (st <= 6) ? 6 : (st == 7) ? 4 : (st == 8) ? 2 : 0;
      const int kb = (st <= 1) ? 0 : (st <= 5) ? st - 1 : (st == 6) ? 3 : (st == 7) ? 2 : (st == 8) ? 1 : 0;
      if (lw < 4) wait_vm_rt(ka + 2 * kb); else wait_vm_rt(ka + kb);
      __builtin_amdgcn_s_barrier();
      if (st >= 1 && st + 4 < 10) stageA(st + 4);
      if (st >= 1 && st + 5 < 10) stageB(st + 5);
    }
    __builtin_amdgcn_s_barrier();
    const u32x4* bsrc2 = w2p + (size_t)cb2 * 1536 * 10 + lw * 64 + lane;
    auto stageB2 = [&](int st) {
      char* dst = lds + BE_OFF + (st % NBE) * 20480 + lane * 16;
      glds16(bsrc2 + st * 1280, dst + lw * 1024);
      glds16(bsrc2 + st * 1280 + 512, dst + (8 + lw) * 1024);
      if (lw < 4) glds16(bsrc2 + st * 1280 + 1024, dst + (16 + lw) * 1024);
    };
#pragma unroll
    for (int st = 0; st < NEARLY; ++st) stageB2(st);
    if (NEARLY < NBE) {
      lds_spin(lcnt + 1, 8u);
#pragma unroll
      for (int st = NEARLY; st < NBE; ++st) stageB2(st);
    }
    {
      const int id = lw * 64 + lane, r = id / 10, ch = id % 10;
      glds16_dev((const u32x4*)((const uint16_t*)h + (size_t)(rb2 * 64 + r) * D_ + cb2 * 80) + ch, lds + RES_OFF + id * 16);
      if (lw >= 6) {
        const int id2 = (lw + 2) * 64 + lane, r2 = id2 / 10, ch2 = id2 % 10;
        glds16_dev((const u32x4*)((const uint16_t*)h + (size_t)(rb2 * 64 + r2) * D_ + cb2 * 80) + ch2, lds + RES_OFF + id2 * 16);
      }
    }
    wait_vm<0>();
#pragma unroll
    for (int st = 0; st < 12; ++st) {
      const int later = (st < NBE) ? 99 : ((st + 5 < 11 ? st + 5 : 11) - st);
      if (later != 99) { if (lw < 4) wait_vm_rt(3 * later); else wait_vm_rt(2 * later); }
      __builtin_amdgcn_s_barrier();
      if (st >= 1 && st + 6 < 12) stageB2(st + 6);
    }
    __builtin_amdgcn_s_barrier();
    (void)nb; (void)nb2;
    return;
  }

  STAMP(2, 0);
  {
    f32x2(*ex)[360] = (f32x2(*)[360])(lds + AD_OFF);
    f32x2(*xx)[384] = (f32x2(*)[384])(lds + AD_OFF + 23040);
    const int j = wv;
    const int s0 = 32 * xcd + idx;
    const int row = s0 + 256 * j;
    const int prow = s0 * 8 + j;
    f32x2 v[8];
    {
      const f32x2* src = T + (size_t)s0 * TP;
      const uint32_t kk = min(tid, DF_ - 1);
#pragma unroll
      for (int q = 0; q < 8; ++q) v[q] = (src + (size_t)q * 256 * TP)[kk];
    }
    uint32_t xd[5];
#pragma unroll
    for (int i = 0; i < 5; ++i) xd[i] = x[row * 320 + lane + 64 * i];
    if (tid < 4) lcnt[tid] = 0;

    f32x2 tw1[4], tw2[7], twq[7], twk[8];
#pragma unroll
    for (int r = 1; r < 5; ++r) tw1[r - 1] = twid((float)(lane * r) * (1.0f / 320.0f));
    const int l0 = lane & 7, rr = lane >> 3;
#pragma unroll
    for (int q = 1; q < 8; ++q) tw2[q - 1] = twid((float)(l0 * q) * (1.0f / 64.0f));
#pragma unroll
    for (int q = 1; q < 8; ++q) twq[q - 1] = twid((float)(s0 * q) * (1.0f / 2048.0f));
#pragma unroll
    for (int p = 0; p < 8; ++p) twk[p] = twid((float)(rr + 5 * l0 + 40 * p) * (1.0f / 640.0f));
    pin(tw1); pin(tw2); pin(twq); pin(twk);
    __builtin_amdgcn_sched_barrier(0);
    STAMP(2, 1);

    if (j < 6) {
#pragma unroll
      for (int q = 1; q < 8; ++q) v[q] = cmulc(v[q], twq[q - 1]);
      fft8<true>(v);
      f32x2 msk;
      msk.x = 1.0f; msk.y = (tid == 0 || tid == 320) ? 0.0f : 1.0f;
#pragma unroll
      for (int jj = 0; jj < 8; ++jj) xx[jj][tid] = v[jj] * msk;
    }
    RAW_BAR();
    asm volatile("" ::: "memory");
    f32x2* E = ex[j];
    if (lane < 40) {
      f32x2* Er = E + rr * 72;
      const f32x2* Xa = xx[j] + rr + 5 * l0;
      const f32x2* Xb = xx[j] + 320 - rr - 5 * l0;
#pragma unroll
      for (int p = 0; p < 8; ++p) {
        const f32x2 a = Xa[40 * p], b = Xb[-40 * p];
        f32x2 e, d;
        e.x = a.x + b.x; e.y = a.y - b.y;
        d.x = a.x - b.x; d.y = a.y + b.y;
        v[p] = cadd_pi(e, cmulc(d, twk[p]));
      }
      fft8<true>(v);
      Er[l0 * 9] = v[0];
#pragma unroll
      for (int a = 1; a < 8; ++a) Er[l0 * 9 + a] = cmulc(v[a], tw2[a - 1]);
      wave_sync();
#pragma unroll
      for (int q = 0; q < 8; ++q) v[q] = Er[q * 9 + l0];
      fft8<true>(v);
      wave_sync();
#pragma unroll
      for (int l1 = 0; l1 < 8; ++l1) Er[l0 + 8 * l1] = v[l1];
    }
    wave_sync();
    f32x2 u[5];
    u[0] = E[lane];
#pragma unroll
    for (int r = 1; r < 5; ++r) u[r] = cmulc(E[r * 72 + lane], tw1[r - 1]);
    fft5<true>(u);

    const f32x2 sc = {1.0f / 2621440.0f, 1.0f / 2621440.0f};
    f32x2 hv[5];
    uint32_t zp[5];
#pragma unroll
    for (int i = 0; i < 5; ++i) {
      const uint32_t hp = bf_pack(bf_unpack(xd[i]) + bf_round(u[i] * sc));
      st_wt32(h + prow * 320 + lane + 64 * i, hp);
      hv[i] = bf_unpack(hp);
    }
    row_layernorm(hv, zp);
#pragma unroll
    for (int i = 0; i < 5; ++i) st_wt32(hn + prow * 320 + lane + 64 * i, zp[i]);
    STAMP(2, 2);
    publish(lcnt, cnt1, lane);
    STAMP(2, 3);
  }
  const int g = lane >> 4, q4 = (lane & 15) >> 2, p4 = lane & 3;
  {
    const int wm = wv >> 1, wn = wv & 1;
    f32x4 acc[2][3];
#pragma unroll
    for (int mi = 0; mi < 2; ++mi)
#pragma unroll
      for (int ni = 0; ni < 3; ++ni) acc[mi][ni] = (f32x4){0.f, 0.f, 0.f, 0.f};
    const int ar = 32 * wm + (lane & 15);
    const int aoff0 = AD_OFF + ar * 128 + (((lane >> 4) ^ (ar & 7)) << 4);
    const int aoff1 = AD_OFF + ar * 128 + (((4 + (lane >> 4)) ^ (ar & 7)) << 4);
    int boff[3];
#pragma unroll
    for (int ni = 0; ni < 3; ++ni) boff[ni] = BD_OFF + (8 * g + q4) * 192 + p4 * 8 + (((3 * wn + ni) ^ (g & 1)) << 5);
    bf16x8 af0[2], bf0[3], af1[2], bf1[3];
    auto rdD = [&](bf16x8 (&af)[2], bf16x8 (&bf)[3], int st, int ks) {
      int sa = (st % 5) * 16384, sb = (st % 6) * 12288;
      asm volatile("" : "+s"(sa), "+s"(sb));
#pragma unroll
      for (int mi = 0; mi < 2; ++mi) af[mi] = *(const bf16x8*)(lds + sa + (ks ? aoff1 : aoff0) + mi * 2048);
#pragma unroll
      for (int ni = 0; ni < 3; ++ni)
        bf[ni] = tr_frag(lds + sb + boff[ni] + ks * 6144, lds + sb + boff[ni] + ks * 6144 + 768);
    };
    auto mmD = [&](const bf16x8 (&af)[2], const bf16x8 (&bf)[3]) {
#pragma unroll
      for (int mi = 0; mi < 2; ++mi)
#pragma unroll
        for (int ni = 0; ni < 3; ++ni)
          acc[mi][ni] = __builtin_amdgcn_mfma_f32_16x16x32_bf16(bf[ni], af[mi], acc[mi][ni], 0, 0, 0);
    };
    RAW_BAR();
    STAMP(2, 4);
    rdD(af0, bf0, 0, 0);
#pragma unroll
    for (int st = 0; st < 10; ++st) {
      rdD(af1, bf1, st, 1);
      mmD(af0, bf0);
      interleave_mfma_ds<6, 8>();
      __builtin_amdgcn_sched_barrier(0);
      RAW_BAR();
      __builtin_amdgcn_sched_barrier(0);
      if (st < 9) rdD(af0, bf0, st + 1, 0);
      mmD(af1, bf1);
      if (st < 9) interleave_mfma_ds<6, 8>();
      __builtin_amdgcn_sched_barrier(0);
    }
    STAMP(2, 5);
#pragma unroll
    for (int ni = 0; ni < 3; ++ni) {
      const u32x2 bb = *(const u32x2*)(lds + B1_OFF + ((3 * wn + ni) * 16 + 4 * (lane >> 4)) * 2);
      const f32x2 b01 = bf_unpack(bb.x), b23 = bf_unpack(bb.y);
      const int NT = cb * 6 + 3 * wn + ni;
      const int ks2 = NT >> 1, gg = 2 * (NT & 1) + (lane >> 5), half = (lane >> 4) & 1;
#pragma unroll
      for (int mi = 0; mi < 2; ++mi) {
        f32x2 a01 = {acc[mi][ni][0], acc[mi][ni][1]}, a23 = {acc[mi][ni][2], acc[mi][ni][3]};
        a01 = bf_unpack(pk_relu_bf16(bf_pack(bf_round(a01) + b01)));
        a23 = bf_unpack(pk_relu_bf16(bf_pack(bf_round(a23) + b23)));
        u32x2 ov;
        ov.x = bf_pack(a01 * a01); ov.y = bf_pack(a23 * a23);
        const int mt = cl * 8 + 2 * wm + mi;
        st_wt64(h1 + ((((mt >> 2) * 48 + ks2) * 4 + (mt & 3)) * 64 + (lane & 15) + 16 * gg) * 2 + half, ov);
      }
    }
    publish(lcnt + 1, cnt2, lane);
    STAMP(2, 6);
    if (wv == 0) wait_cluster(lcnt + 1, lcnt + 3, cnt2, 0, lane);
  }
  {
    const int wmp = wv & 1, wk = wv >> 1;
    f32x4 acc[2][5];
#pragma unroll
    for (int mi = 0; mi < 2; ++mi)
#pragma unroll
      for (int ni = 0; ni < 5; ++ni) acc[mi][ni] = (f32x4){0.f, 0.f, 0.f, 0.f};
    const int boff = BE_OFF + (16 * (g >> 1) + 4 * (g & 1) + q4 + 32 * wk) * 160 + p4 * 8;
    const __amdgpu_buffer_rsrc_t rs = __builtin_amdgcn_make_buffer_rsrc((void*)h1, 0, S_ * F_ * 2, 0x00020000);
    const int avoff = ((rb2 * 48 + wk) * 4 + 2 * wmp) * 1024 + lane * 16;
    bf16x8 af[12][2], bfr[2][5];
    auto ldA = [&](int st) {
      af[st][0] = __builtin_bit_cast(bf16x8, __builtin_amdgcn_raw_buffer_load_b128(rs, avoff, st * 16384, 16));
      af[st][1] = __builtin_bit_cast(bf16x8, __builtin_amdgcn_raw_buffer_load_b128(rs, avoff + 1024, st * 16384, 16));
    };
    auto rdE = [&](bf16x8 (&bf)[5], int st) {
      int sb = (st % NBE) * 20480;
      asm volatile("" : "+s"(sb));
#pragma unroll
      for (int ni = 0; ni < 5; ++ni) bf[ni] = tr_frag(lds + sb + boff + ni * 32, lds + sb + boff + ni * 32 + 1280);
    };
    constexpr int APF = 4;
    lds_spin(lcnt + 3, 1u);
#pragma unroll
    for (int i = 0; i < APF; ++i) ldA(i);
    RAW_BAR();
    STAMP(2, 7);
    rdE(bfr[0], 0);
#pragma unroll
    for (int st = 0; st < 12; ++st) {
      if (st < 11) {
        RAW_BAR();
        __builtin_amdgcn_sched_barrier(0);
        rdE(bfr[(st + 1) & 1], st + 1);
      }
#pragma unroll
      for (int ni = 0; ni < 5; ++ni)
#pragma unroll
        for (int mi = 0; mi < 2; ++mi)
          acc[mi][ni] = __builtin_amdgcn_mfma_f32_16x16x32_bf16(bfr[st & 1][ni], af[st][mi], acc[mi][ni], 0, 0, 0);
      if (st < 11) interleave_mfma_ds<10, 10>();
      __builtin_amdgcn_sched_barrier(0);
      if (st + APF < 12) ldA(st + APF);
    }
    STAMP(2, 8);
    f32x4* red = (f32x4*)(lds + BE_OFF) + (wmp * 40) * 64 + lane;
#pragma unroll
    for (int t = 0; t < 10; ++t)
      if ((t & 3) != wk) red[(t * 4 + wk) * 64] = acc[t / 5][t % 5];
    RAW_BAR();
    asm volatile("" ::: "memory");
#pragma unroll
    for (int t = 0; t < 10; ++t) {
      if ((t & 3) == wk) {
        const int mi = t / 5, ni = t % 5;
        f32x4 o = acc[mi][ni];
#pragma unroll
        for (int w2_ = 0; w2_ < 4; ++w2_)
          if (w2_ != (t & 3)) o += red[(t * 4 + w2_) * 64];
        const int lr = 32 * wmp + 16 * mi + (lane & 15);
        const int prow = rb2 * 64 + lr;
        const int mrow = (prow >> 3) + 256 * (prow & 7);
        const int nc = 16 * ni + 4 * (lane >> 4);
        const u32x2 bb = *(const u32x2*)(lds + B2_OFF + nc * 2);
        const u32x2 rsd = *(const u32x2*)(lds + RES_OFF + lr * 160 + nc * 2);
        f32x2 a01 = {o[0], o[1]}, a23 = {o[2], o[3]};
        u32x2 ov;
        ov.x = bf_pack(bf_unpack(rsd.x) + bf_round(bf_round(a01) + bf_unpack(bb.x)));
        ov.y = bf_pack(bf_unpack(rsd.y) + bf_round(bf_round(a23) + bf_unpack(bb.y)));
        *(u32x2*)(out + (size_t)mrow * D_ + cb2 * 80 + nc) = ov;
      }
    }
  }
  STAMP_END(2, 9);
}

__global__ void k_zz_end() {}
extern "C" void kernel_launch(void* const* d_in, const int* in_sizes, int n_in, void* d_out, int out_size,
                              void* d_ws, size_t ws_size, hipStream_t stream) {
  const uint32_t* x = (const uint32_t*)d_in[0];
  const uint16_t* wr = (const uint16_t*)d_in[1];
  const uint16_t* wi = (const uint16_t*)d_in[2];
  const uint16_t* w1 = (const uint16_t*)d_in[4];
  const uint16_t* b1 = (const uint16_t*)d_in[5];
  const uint16_t* w2 = (const uint16_t*)d_in[6];
  const uint16_t* b2 = (const uint16_t*)d_in[7];
  char* ws = (char*)d_ws;
  size_t off = 0;
  f32x2* T = (f32x2*)(ws + off); off += (size_t)8 * 256 * TP * 8;
  uint32_t* h = (uint32_t*)(ws + off); off += (size_t)S_ * D_ * 2;
  uint32_t* hn = (uint32_t*)(ws + off); off += (size_t)S_ * D_ * 2;
  u32x4* h1 = (u32x4*)(ws + off); off += (size_t)S_ * F_ * 2;
  u32x4* w2p = (u32x4*)(ws + off); off += (size_t)D_ * F_ * 2;

  uint32_t* cnt = (uint32_t*)(ws + off); off += 32 * 256;
  k_fwd_rows<<<256, 1024, 0, stream>>>(x, T, wr, wi, (const uint32_t*)w1, (const u32x4*)w2, w2p, cnt);
  k_cols<<<328, 256, 0, stream>>>(T, wr, wi);
  k_tail<<<256, 1024, 0, stream>>>(T, x, h, hn, (u32x2*)h1, (const char*)w1, b1, w2p, b2, (uint16_t*)d_out, cnt);
}
```
